```python
import torch

def _softmax(x, axis=-1):
    return torch.softmax(x, dim=axis)
def _gelu(x):
    return torch.nn.functional.gelu(x, approximate="tanh")
def _silu(x):
    return torch.nn.functional.silu(x)
def _sigmoid(x):
    return torch.sigmoid(x)
def _layernorm(x, axis=-1, eps=1e-5):
    m = x.mean(dim=axis, keepdim=True)
    v = x.var(dim=axis, keepdim=True, unbiased=False)
    return (x - m) / torch.sqrt(v + eps)
def _scatter_add(tgt, idx, src, axis=0):
    return tgt.clone().index_add_(axis, idx, src)
def _segment_sum(src, seg, n):
    out = torch.zeros((n,) + src.shape[1:], dtype=src.dtype)
    return out.index_add_(0, seg, src)
def _pad(x, pw):
    flat = tuple(v for pair in reversed(pw) for v in pair)
    return torch.nn.functional.pad(x, flat)

kernel_name = 'concat_heads__ssm_conv_s4__ssm_conv_s5__prompt'

def setup_inputs():

    _w = (lambda a: (lambda t: t.to(torch.bfloat16) if t.is_floating_point() else t)(torch.as_tensor(a)))
    def _A_setup():
        B, L, D, N = 1, 2048, 576, 128
        A_re = -(lambda _x: torch.exp(torch.as_tensor(_x)))((lambda *s: torch.randn(*s, dtype=torch.bfloat16) * 3.2)(D, N))
        A_im = torch.pi * (lambda *s: torch.randn(*s, dtype=torch.bfloat16) * 0.05)(D, N)
        B_re = (lambda *s: torch.randn(*s, dtype=torch.bfloat16) * 0.1)(D, N)
        B_im = (lambda *s: torch.randn(*s, dtype=torch.bfloat16) * 0.1)(D, N)
        C_re = (lambda *s: torch.randn(*s, dtype=torch.bfloat16) * 0.1)(D, N)
        C_im = (lambda *s: torch.randn(*s, dtype=torch.bfloat16) * 0.1)(D, N)
        log_dt = (lambda *s: torch.randn(*s, dtype=torch.bfloat16) * 0.05)(D) * 0.5 - 4.0
        Dskip = (lambda *s: torch.randn(*s, dtype=torch.bfloat16) * 0.05)(D)
        return {
            "u": _w((lambda *s: torch.randn(*s, dtype=torch.bfloat16) * 0.05)(B, L, D)),
            "A_re": _w(A_re),
            "A_im": _w(A_im),
            "B_re": _w(B_re),
            "B_im": _w(B_im),
            "C_re": _w(C_re),
            "C_im": _w(C_im),
            "log_dt": _w(log_dt),
            "Dskip": _w(Dskip),
        }
    _A_in = _A_setup()

    def _B_setup():
        B, S, D = 1, 2048, 576
        P = D
        Lambda_re = -0.5 * (lambda _x: torch.exp(torch.as_tensor(_x)))((lambda *s: torch.randn(*s, dtype=torch.bfloat16) * 0.2)(P))
        Lambda_im = (lambda *s: torch.randn(*s, dtype=torch.bfloat16) * 0.4)(P) * 1.0
        B_re = (lambda *s: torch.randn(*s, dtype=torch.bfloat16) * 0.2)(P, D) * (D ** -0.5)
        B_im = (lambda *s: torch.randn(*s, dtype=torch.bfloat16) * 0.2)(P, D) * (D ** -0.5)
        C_re = (lambda *s: torch.randn(*s, dtype=torch.bfloat16) * 0.2)(D, P) * (P ** -0.5)
        C_im = (lambda *s: torch.randn(*s, dtype=torch.bfloat16) * 0.2)(D, P) * (P ** -0.5)
        log_dt = (lambda *s: torch.randn(*s, dtype=torch.bfloat16) * 0.2)(P) * 0.3 - 2.0
        Dskip = (lambda *s: torch.randn(*s, dtype=torch.bfloat16) * 0.05)(D)
        return {
            "x": _w((lambda *s: torch.randn(*s, dtype=torch.bfloat16) * 0.05)(B, S, D)),
            "Lambda_re": _w(Lambda_re),
            "Lambda_im": _w(Lambda_im),
            "B_re": _w(B_re),
            "B_im": _w(B_im),
            "C_re": _w(C_re),
            "C_im": _w(C_im),
            "log_dt": _w(log_dt),
            "Dskip": _w(Dskip),
        }
    _B_in = _B_setup()

    return {
        "A_u": _A_in["u"],
        "A_A_re": _A_in["A_re"],
        "A_A_im": _A_in["A_im"],
        "A_B_re": _A_in["B_re"],
        "A_B_im": _A_in["B_im"],
        "A_C_re": _A_in["C_re"],
        "A_C_im": _A_in["C_im"],
        "A_log_dt": _A_in["log_dt"],
        "A_Dskip": _A_in["Dskip"],
        "B_x": _B_in["x"],
        "B_Lambda_re": _B_in["Lambda_re"],
        "B_Lambda_im": _B_in["Lambda_im"],
        "B_B_re": _B_in["B_re"],
        "B_B_im": _B_in["B_im"],
        "B_C_re": _B_in["C_re"],
        "B_C_im": _B_in["C_im"],
        "B_log_dt": _B_in["log_dt"],
        "B_Dskip": _B_in["Dskip"],
    }

def reference(A_u, A_A_re, A_A_im, A_B_re, A_B_im, A_C_re, A_C_im, A_log_dt, A_Dskip, B_x, B_Lambda_re, B_Lambda_im, B_B_re, B_B_im, B_C_re, B_C_im, B_log_dt, B_Dskip):
    def _A_ref(u, A_re, A_im, B_re, B_im, C_re, C_im, log_dt, Dskip):
        Bsz, L, D = u.shape
        dt = torch.exp(log_dt)[:, None]
        A = A_re + 1j * A_im
        Bc = B_re + 1j * B_im
        Cc = C_re + 1j * C_im
        logdA = dt * A
        dB = dt * Bc
        l = (lambda x, dt: x.to(dt))(torch.arange(L), A_re.dtype)
        P = torch.exp(logdA[:, :, None] * l[None, None, :])
        K = torch.einsum("dn,dnl,dn->dl", Cc, P, dB).real
        L2 = 2 * L
        Kp = _pad(K, ((0, 0), (0, L)))
        up = (lambda x, *d: x.permute(*d))(u, 0, 2, 1)
        up = _pad(up, ((0, 0), (0, 0), (0, L)))
        Kf = (lambda x, axes=(-2,-1): torch.fft.rfft2(x.float(), dim=axes))(Kp[:, None, :], axes=(-2, -1))
        Uf = (lambda x, axes=(-2,-1): torch.fft.rfft2(x.float(), dim=axes))(up[:, :, None, :], axes=(-2, -1))
        Yf = Uf * Kf[None, :, :, :]
        y = (lambda x, s, axes=(-2,-1): torch.fft.irfft2(x, s=s, dim=axes).to(torch.bfloat16))(Yf, (1, L2), axes=(-2, -1))[:, :, 0, :L]
        y = (lambda x, *d: x.permute(*d))(y, 0, 2, 1) + u * Dskip[None, None, :]
        return y

    def _B_ref(x, Lambda_re, Lambda_im, B_re, B_im, C_re, C_im, log_dt, Dskip):
        Bsz, L, H = x.shape
        Lam = Lambda_re + 1j * Lambda_im
        Bmat = B_re + 1j * B_im
        Cmat = C_re + 1j * C_im
        dt = torch.exp(log_dt)
        Lam_bar = torch.exp(dt * Lam)
        B_bar = ((Lam_bar - 1.0) / Lam)[:, None] * Bmat
        Bu = torch.einsum("bld,pd->blp", x + 0j, B_bar)
        h = torch.zeros_like(Bu[:, 0, :])
        hs = []
        for t in range(L):
            h = Lam_bar[None, :] * h + Bu[:, t, :]
            hs.append(h)
        hseq = torch.stack(hs, 1)
        y = 2.0 * torch.einsum("blp,dp->bld", hseq, Cmat).real + x * Dskip[None, None, :]
        return y

    a = _A_ref(A_u, A_A_re, A_A_im, A_B_re, A_B_im, A_C_re, A_C_im, A_log_dt, A_Dskip)
    b = _B_ref(B_x, B_Lambda_re, B_Lambda_im, B_B_re, B_B_im, B_C_re, B_C_im, B_log_dt, B_Dskip)
    return torch.concatenate([a, b], axis=-1)
```

```cpp
#include <hip/hip_runtime.h>
#include <hip/hip_bf16.h>
#include <stdint.h>

typedef unsigned short u16;
__device__ __forceinline__ float bf2f(u16 v) { return __uint_as_float(((unsigned)v) << 16); }
__device__ __forceinline__ u16 f2bf(float f) { unsigned u = __float_as_uint(f); u += 0x7FFF + ((u >> 16) & 1); return (u16)(u >> 16); }
__device__ __forceinline__ float rbf(float f) { return bf2f(f2bf(f)); }

constexpr int L = 2048, D = 576, NS = 128;

__global__ void kgen(const u16* Are, const u16* Aim, const u16* Bre, const u16* Bim, const u16* Cre, const u16* Cim,
                     const u16* logdt, float* K) {
  int d = blockIdx.x;
  __shared__ float sa[NS], sb[NS], wr[NS], wi[NS];
  float dt = rbf(expf(bf2f(logdt[d])));
  if (threadIdx.x < NS) {
    int n = threadIdx.x;
    sa[n] = dt * bf2f(Are[d * NS + n]);
    sb[n] = dt * bf2f(Aim[d * NS + n]);
    float br = dt * bf2f(Bre[d * NS + n]), bi = dt * bf2f(Bim[d * NS + n]);
    float cr = bf2f(Cre[d * NS + n]), ci = bf2f(Cim[d * NS + n]);
    wr[n] = cr * br - ci * bi; wi[n] = cr * bi + ci * br;
  }
  __syncthreads();
  for (int l = threadIdx.x; l < L; l += blockDim.x) {
    float lf = rbf((float)l);
    float acc = 0.f;
    for (int n = 0; n < NS; ++n) {
      float e = expf(sa[n] * lf);
      float s, c; sincosf(sb[n] * lf, &s, &c);
      acc += e * (wr[n] * c - wi[n] * s);
    }
    K[d * L + l] = acc;
  }
}

__global__ void conv(const u16* u, const float* K, const u16* Dskip, float* out) {
  int d = blockIdx.x;
  __shared__ float su[L], sk[L];
  for (int i = threadIdx.x; i < L; i += blockDim.x) { su[i] = bf2f(u[i * D + d]); sk[i] = K[d * L + i]; }
  __syncthreads();
  float ds = bf2f(Dskip[d]);
  for (int t = threadIdx.x; t < L; t += blockDim.x) {
    float acc = 0.f;
    for (int s = 0; s <= t; ++s) acc += sk[t - s] * su[s];
    float y = rbf(acc);
    float sk2 = rbf(su[t] * ds);
    out[t * (2 * D) + d] = rbf(y + sk2);
  }
}

__global__ void gemm1(const u16* x, const u16* Bre, const u16* Bim, const u16* Lre, const u16* Lim, const u16* logdt, float2* Bu) {
  int t = blockIdx.x;
  __shared__ float sx[D];
  for (int i = threadIdx.x; i < D; i += blockDim.x) sx[i] = bf2f(x[t * D + i]);
  __syncthreads();
  for (int p = threadIdx.x; p < D; p += blockDim.x) {
    float ar = 0, ai = 0;
    for (int k = 0; k < D; ++k) { ar += sx[k] * bf2f(Bre[p * D + k]); ai += sx[k] * bf2f(Bim[p * D + k]); }
    float dt = rbf(expf(bf2f(logdt[p])));
    float lr = bf2f(Lre[p]), li = bf2f(Lim[p]);
    float e = expf(dt * lr); float s, c; sincosf(dt * li, &s, &c);
    float br = e * c - 1.f, bi = e * s;
    float den = lr * lr + li * li;
    float cr = (br * lr + bi * li) / den, ci = (bi * lr - br * li) / den;
    Bu[t * D + p] = make_float2(cr * ar - ci * ai, cr * ai + ci * ar);
  }
}

__global__ void scan(float2* Bu, const u16* Lre, const u16* Lim, const u16* logdt) {
  int p = blockIdx.x * blockDim.x + threadIdx.x;
  if (p >= D) return;
  float dt = rbf(expf(bf2f(logdt[p])));
  float lr = bf2f(Lre[p]), li = bf2f(Lim[p]);
  float e = expf(dt * lr); float s, c; sincosf(dt * li, &s, &c);
  float ar = e * c, ai = e * s;
  float hr = 0, hi = 0;
  for (int t = 0; t < L; ++t) {
    float2 b = Bu[t * D + p];
    float nr = ar * hr - ai * hi + b.x, ni = ar * hi + ai * hr + b.y;
    hr = nr; hi = ni;
    Bu[t * D + p] = make_float2(hr, hi);
  }
}

__global__ void gemm2(const float2* h, const u16* Cre, const u16* Cim, const u16* x, const u16* Dskip, float* out) {
  int t = blockIdx.x;
  __shared__ float2 sh[D];
  for (int i = threadIdx.x; i < D; i += blockDim.x) sh[i] = h[t * D + i];
  __syncthreads();
  for (int d = threadIdx.x; d < D; d += blockDim.x) {
    float a = 0;
    for (int p = 0; p < D; ++p) a += sh[p].x * bf2f(Cre[d * D + p]) - sh[p].y * bf2f(Cim[d * D + p]);
    out[t * (2 * D) + D + d] = 2.f * a + rbf(bf2f(x[t * D + d]) * bf2f(Dskip[d]));
  }
}

extern "C" void kernel_launch(void* const* d_in, const int* in_sizes, int n_in,
                              void* d_out, int out_size, void* d_ws, size_t ws_size,
                              hipStream_t stream) {
  const u16* const* in = (const u16* const*)d_in;
  float* out = (float*)d_out;
  float* K = (float*)d_ws;
  float2* Bu = (float2*)((char*)d_ws + (size_t)D * L * 4);
  kgen<<<D, 256, 0, stream>>>(in[1], in[2], in[3], in[4], in[5], in[6], in[7], K);
  conv<<<D, 256, 0, stream>>>(in[0], K, in[8], out);
  gemm1<<<L, 192, 0, stream>>>(in[9], in[12], in[13], in[10], in[11], in[16], Bu);
  scan<<<9, 64, 0, stream>>>(Bu, in[10], in[11], in[16]);
  gemm2<<<L, 192, 0, stream>>>(Bu, in[14], in[15], in[9], in[17], out);
}
```

```cpp
#include <hip/hip_runtime.h>
#include <stdint.h>

typedef unsigned short u16; typedef unsigned u32; typedef unsigned long long u64;
typedef __bf16 bf16x8 __attribute__((ext_vector_type(8)));
typedef __bf16 bf16x2 __attribute__((ext_vector_type(2)));
typedef short s16x4 __attribute__((ext_vector_type(4)));
typedef float f32x16 __attribute__((ext_vector_type(16)));
typedef float f32x4 __attribute__((ext_vector_type(4)));
typedef float f32x2 __attribute__((ext_vector_type(2)));
typedef u32 u32x4 __attribute__((ext_vector_type(4)));
typedef u32 u32x2 __attribute__((ext_vector_type(2)));
#define LAS __attribute__((address_space(3)))
#define GAS __attribute__((address_space(1)))
#define DEV __device__ __forceinline__

struct Params { const u16* in[18]; float* out; char* ws; };

constexpr u32 WS_EPOCH = 0, WS_FLAG = 4096, WS_GRAN = 65536, WS_H = 262144;
constexpr u32 H_BYTES = 16u * 4u * 72u * 1024u;
constexpr u32 LDS_BYTES = 163840, SCR = 147456;
constexpr int SPIN_MAX = 1 << 16;

#define STAMP_DECL
#define ST(i)
#define STAMP_FLUSH(P)

DEV float bf2f(u16 v) { return __uint_as_float(((u32)v) << 16); }
DEV u32 pack2(float a, float b) { bf16x2 r; r[0] = (__bf16)a; r[1] = (__bf16)b; return __builtin_bit_cast(u32, r); }
DEV float rbf(float f) { return (float)(__bf16)f; }
DEV void bar() { asm volatile("s_waitcnt lgkmcnt(0)\n\ts_barrier" ::: "memory"); }
DEV u32 mix32(u32 x) { x *= 0x9E3779B1u; x ^= x >> 15; x *= 0x85EBCA77u; x ^= x >> 13; return x + 0x632BE5ABu; }
DEV f32x2 cmul(f32x2 a, f32x2 b) { return f32x2{a.x * b.x - a.y * b.y, a.x * b.y + a.y * b.x}; }
DEV f32x2 cfma(f32x2 a, f32x2 b, f32x2 c) { return f32x2{fmaf(a.x, b.x, fmaf(-a.y, b.y, c.x)), fmaf(a.x, b.y, fmaf(a.y, b.x, c.y))}; }
DEV f32x2 cexp(float re, float im) {
  float e = __builtin_amdgcn_exp2f(re * 1.44269504088896341f);
  float r = __builtin_amdgcn_fractf(im * 0.15915494309189535f);
  return f32x2{e * __builtin_amdgcn_cosf(r), e * __builtin_amdgcn_sinf(r)};
}
DEV void glds4(const char* gb, u32 ldsb, u32 v0, u32 v1, u32 v2, u32 v3) {
  u32 keep;
  asm volatile("s_mov_b32 %0, m0\n\ts_mov_b32 m0, %2\n\ts_nop 0\n\t"
               "global_load_lds_dwordx4 %3, %1\n\tglobal_load_lds_dwordx4 %4, %1 offset:1024\n\t"
               "global_load_lds_dwordx4 %5, %1 offset:2048\n\tglobal_load_lds_dwordx4 %6, %1 offset:3072\n\ts_mov_b32 m0, %0"
               : "=&s"(keep) : "s"(gb), "s"(ldsb), "v"(v0), "v"(v1), "v"(v2), "v"(v3) : "memory");
}
template <class T> DEV T ldsr(u32 a) { return *(const LAS T*)(size_t)a; }
template <class T> DEV void ldsw(u32 a, T v) { *(LAS T*)(size_t)a = v; }
DEV f32x2 pr(const f32x16& a, int m) { return f32x2{a[2 * m], a[2 * m + 1]}; }

#define RA 1
DEV void swap16(float x, float& lo, float& hi) { auto r = __builtin_amdgcn_permlane16_swap(__float_as_uint(x), __float_as_uint(x), false, false); lo = __uint_as_float(r[0]); hi = __uint_as_float(r[1]); }
DEV void swap32(float x, float& lo, float& hi) { auto r = __builtin_amdgcn_permlane32_swap(__float_as_uint(x), __float_as_uint(x), false, false); lo = __uint_as_float(r[0]); hi = __uint_as_float(r[1]); }
DEV int wrap9(int v) { return v >= 9 ? v - 9 : v; }

DEV void g_role(const Params& P, int c, int j) {
  const int lane = threadIdx.x & 63, wv = __builtin_amdgcn_readfirstlane(threadIdx.x >> 6);
  STAMP_DECL
  ST(0);
  const u64 ep = *(const __attribute__((address_space(4))) u64*)(P.ws + WS_EPOCH);
  const int rot = wrap9(j + 4 * (c & 1));
  constexpr int NSL = RA + 2;
  if (wv >= 4) {
    const int w = wv - 4;
    const int l31 = lane & 31, hh = lane >> 5;
    u32 vx[4], vb[4];
#pragma unroll
    for (int q = 0; q < 4; ++q) {
      int i = 2 * w + (q >> 1), qq = 2 * (q & 1) + (lane >> 5), r = (lane >> 3) & 3;
      int time = 32 * qq + 16 * (r & 1) + 2 * i + (r >> 1);
      u32 ch = (u32)(((lane & 7) ^ ((4 * (q & 1) + (lane >> 4)) & 7)) << 4) + 3072 - 1024 * q;
      vx[q] = (128 * c + time) * 1152 + ch;
      vb[q] = (64 * j + 32 * (w & 1) + 8 * q + (lane >> 3)) * 1152 + ch;
    }
    u32 dk0, dk1;
    asm volatile("global_load_ushort %0, %2, %3\n\tglobal_load_ushort %1, %2, %3 offset:64" : "=&v"(dk0), "=&v"(dk1) : "v"((64 * j + l31) * 2), "s"(P.in[17]) : "memory");
    const char* xb = (const char*)P.in[9] - 3072;
    const char* bb = (const char*)(w < 2 ? P.in[12] : P.in[13]) - 3072;
    const char* cb = (const char*)(w < 2 ? P.in[14] : P.in[15]) - 3072;
#pragma unroll
    for (int s = 0; s <= RA; ++s) {
      u32 sl = s * 32768 + w * 4096; int ks = wrap9(rot + s);
      glds4(xb + ks * 128, sl, vx[0], vx[1], vx[2], vx[3]);
      glds4(bb + ks * 128, sl + 16384, vb[0], vb[1], vb[2], vb[3]);
    }
    u32 tlo = mix32((u32)ep), thi = mix32((u32)(ep >> 32) ^ 0x5bd1e995u);
    asm volatile("" :: "s"(tlo), "s"(thi));
    ST(1);
    s16x4 xs[8];
    asm volatile("s_waitcnt vmcnt(%2)" : "+v"(dk0), "+v"(dk1) : "n"(8 * RA) : "memory");
    const float dsk0 = __uint_as_float(dk0 << 16), dsk1 = __uint_as_float(dk1 << 16);
    int slot_cur = 0, slot_new = RA + 1 == NSL ? 0 : RA + 1, kn = wrap9(rot + RA + 1);
    int cpc = 0;
#pragma unroll 1
    for (int s = 0; s < 9; ++s) {
      bar();
      u32 sl = slot_new * 32768 + w * 4096;
      if (s + RA + 1 <= 8) {
        glds4(xb + kn * 128, sl, vx[0], vx[1], vx[2], vx[3]);
        glds4(bb + kn * 128, sl + 16384, vb[0], vb[1], vb[2], vb[3]);
      } else {
        glds4(cb + slot_new * 256, sl, vb[0], vb[1], vb[2], vb[3]);
        glds4(cb + slot_new * 256 + 128, sl + 16384, vb[0], vb[1], vb[2], vb[3]);
      }
      if (wrap9(rot + s) == j) {
        const int e = (lane >> 2) & 3, pp = lane & 3;
        u32 sb = slot_cur * 32768 + (32 * hh + 4 * w + 16 * (e >> 1) + 2 * (e & 1)) * 128 + (((2 * ((lane >> 4) & 1) + (pp >> 1)) ^ ((w << 1) | (e & 1))) << 4) + (pp & 1) * 8;
#pragma unroll
        for (int g = 0; g < 4; ++g) {
          xs[g] = __builtin_amdgcn_ds_read_tr16_b64_v4i16((LAS s16x4*)(size_t)(sb + (64 * (g & 1) + (g >> 1)) * 128));
          xs[4 + g] = __builtin_amdgcn_ds_read_tr16_b64_v4i16((LAS s16x4*)(size_t)((sb ^ 64) + (64 * (g & 1) + (g >> 1)) * 128));
        }
      }
      slot_cur = slot_cur + 1 == NSL ? 0 : slot_cur + 1; slot_new = slot_new + 1 == NSL ? 0 : slot_new + 1; kn = wrap9(kn + 1);
      asm volatile("s_waitcnt vmcnt(%0)" :: "n"(8 * RA) : "memory");
    }
    bar();
    ST(3);
    for (int q = 2 * NSL; q < 9; ++q) glds4(cb + q * 128, q * 16384 + w * 4096, vb[0], vb[1], vb[2], vb[3]);
    glds4(cb + (8 % NSL) * 256, (8 % NSL) * 32768 + w * 4096, vb[0], vb[1], vb[2], vb[3]);
    glds4(cb + (8 % NSL) * 256 + 128, (8 % NSL) * 32768 + 16384 + w * 4096, vb[0], vb[1], vb[2], vb[3]);
    f32x16 acc0, acc1;
#pragma unroll
    for (int r = 0; r < 16; ++r) {
      acc0[r] = rbf(bf2f((u16)xs[r >> 2][r & 3]) * dsk0);
      acc1[r] = rbf(bf2f((u16)xs[4 + (r >> 2)][r & 3]) * dsk1);
    }
    asm volatile("" : "+v"(acc0), "+v"(acc1));
    asm volatile("s_waitcnt vmcnt(0)" ::: "memory");
    ST(1);
    bar();
    ST(4);
    {
      const u64 tag = ((u64)thi << 32) | tlo;
      GAS u64* fp = (GAS u64*)(P.ws + WS_FLAG + (c * 9 + (lane < 9 ? lane : 0)) * 256);
      for (int it = 0; it < SPIN_MAX; ++it) {
        u64 v = __hip_atomic_load(fp, __ATOMIC_RELAXED, __HIP_MEMORY_SCOPE_AGENT);
        if (__all(v == tag)) break;
        __builtin_amdgcn_s_sleep(1);
      }
      asm volatile("" ::: "memory");
    }
    ST(5);
    if (c == 15 && j == 0 && w == 0 && lane == 0)
      __hip_atomic_store((GAS u64*)(P.ws + WS_EPOCH), ((u64)((u32)(ep >> 32) + 1u) << 32) | (u32)((u32)ep + 1u), __ATOMIC_RELAXED, __HIP_MEMORY_SCOPE_AGENT);
    __amdgpu_buffer_rsrc_t hr = __builtin_amdgcn_make_buffer_rsrc(P.ws + WS_H, 0, H_BYTES, 0x00020000);
    const u32 hs = (c * 4 + w) * 72 * 1024;
    const int r2 = j;
    int pcs[9];
#pragma unroll
    for (int q = 0; q < 9; ++q) pcs[q] = wrap9(r2 + q);
    u32 aoff[4];
#pragma unroll
    for (int s = 0; s < 4; ++s) aoff[s] = l31 * 128 + (((2 * s + hh) ^ ((l31 >> 1) & 7)) << 4);
#define PFD 12
    constexpr int PF = PFD;
    u32x4 hf[PF];
#define HLOAD(k) __builtin_amdgcn_raw_buffer_load_b128(hr, lane * 16 + ((k) & 3) * 1024, hs + pcs[(k) >> 3] * 8192 + (((k) >> 2) & 1) * 4096, 16)
#pragma unroll
    for (int k = 0; k < PF; ++k) hf[k] = HLOAD(k);
    bf16x8 cf[2][4];
#define CREAD(set, k) do { _Pragma("unroll") for (int u = 0; u < 2; ++u) { u32 ca = pcs[((k) + u) >> 3] * 16384 + aoff[((k) + u) & 3]; u32 cbase = ((((k) + u) >> 2) & 1) * 8192; \
      cf[set][2 * u] = ldsr<bf16x8>(ca + cbase); cf[set][2 * u + 1] = ldsr<bf16x8>(ca + cbase + 4096); } } while (0)
    CREAD(0, 0);
    __builtin_amdgcn_sched_barrier(0);
#pragma unroll
    for (int k = 0; k < 72; k += 2) {
      const int set = (k >> 1) & 1;
      if (k + 2 < 72) CREAD(set ^ 1, k + 2);
      __builtin_amdgcn_sched_barrier(0);
#pragma unroll
      for (int u = 0; u < 2; ++u) {
        bf16x8 a = __builtin_bit_cast(bf16x8, hf[(k + u) % PF]);
        acc0 = __builtin_amdgcn_mfma_f32_32x32x16_bf16(a, cf[set][2 * u], acc0, 0, 0, 0);
        acc1 = __builtin_amdgcn_mfma_f32_32x32x16_bf16(a, cf[set][2 * u + 1], acc1, 0, 0, 0);
        if (k + u + PF < 72) hf[(k + u) % PF] = HLOAD(k + u + PF);
      }
      asm volatile("" : "+v"(acc0), "+v"(acc1));
      __builtin_amdgcn_sched_barrier(0);
    }
    ST(6);
    float* op = P.out + (size_t)(128 * c + 32 * w + 4 * hh) * 1152 + 576 + 64 * j + l31;
#pragma unroll
    for (int r = 0; r < 16; ++r) {
      op[(8 * (r >> 2) + (r & 3)) * 1152] = acc0[r];
      op[(8 * (r >> 2) + (r & 3)) * 1152 + 32] = acc1[r];
    }
    ST(7);
  } else {
    const int l15 = lane & 15, q4 = lane >> 4;
    const int p = 64 * j + 16 * wv + l15;
    float dt = rbf(__builtin_amdgcn_exp2f(bf2f(P.in[16][p]) * 1.44269504088896341f));
    float Lr = bf2f(P.in[10][p]), Li = bf2f(P.in[11][p]);
    f32x2 lam = cexp(dt * Lr, dt * Li);
    float inv = 2.f / (Lr * Lr + Li * Li);
    f32x2 coef = cmul(f32x2{lam.x - 1.f, lam.y}, f32x2{Lr * inv, -Li * inv});
    f32x2 mu = cmul(lam, lam); mu = cmul(mu, mu); mu = cmul(mu, mu); mu = cmul(mu, mu);
    f32x2 nu = cmul(mu, mu), nu2 = cmul(nu, nu), La = cmul(nu2, nu2);
    f32x2 La2 = cmul(La, La), La4 = cmul(La2, La2);
    f32x2 one = {1.f, 0.f};
    f32x2 Lg = cmul((q4 & 2) ? La2 : one, (q4 & 1) ? La : one);
    f32x2 nuq = (q4 & 1) ? nu : one;
    f32x2 cm0 = cmul(coef, cmul((q4 & 2) ? nu2 : one, nuq)), cm1 = cmul(cm0, mu);
    u32 tlo = mix32((u32)ep), thi = mix32((u32)(ep >> 32) ^ 0x5bd1e995u);
    asm volatile("" :: "s"(tlo), "s"(thi));
    asm volatile("" : "+v"(lam), "+v"(coef), "+v"(mu), "+v"(La4), "+v"(Lg), "+v"(cm0), "+v"(cm1), "+v"(nu), "+v"(nu2), "+v"(nuq));
    f32x4 ar[8], ai[8];
#pragma unroll
    for (int i = 0; i < 8; ++i) { ar[i] = f32x4{0.f, 0.f, 0.f, 0.f}; ai[i] = ar[i]; }
    ST(1);
    u32 xa0 = l15 * 128 + ((q4 ^ (l15 >> 1)) << 4), xa1 = xa0 ^ 64, ba0 = xa0 + 16384 + wv * 2048, ba1 = ba0 ^ 64;
    bf16x8 A[2][8], Bq[2][2];
#define STEP(cs, xa, ba) do { Bq[cs ^ 1][0] = ldsr<bf16x8>(ba); Bq[cs ^ 1][1] = ldsr<bf16x8>(ba + 8192); \
      _Pragma("unroll") for (int i = 0; i < 8; ++i) { A[cs ^ 1][i] = ldsr<bf16x8>(xa + i * 2048); \
        ar[i] = __builtin_amdgcn_mfma_f32_16x16x32_bf16(A[cs][i], Bq[cs][0], ar[i], 0, 0, 0); ai[i] = __builtin_amdgcn_mfma_f32_16x16x32_bf16(A[cs][i], Bq[cs][1], ai[i], 0, 0, 0); \
        asm volatile("" : "+v"(ar[i]), "+v"(ai[i])); __builtin_amdgcn_sched_barrier(0); } } while (0)
    bar();
    ST(2);
    Bq[0][0] = ldsr<bf16x8>(ba0); Bq[0][1] = ldsr<bf16x8>(ba0 + 8192);
#pragma unroll
    for (int i = 0; i < 8; ++i) A[0][i] = ldsr<bf16x8>(xa0 + i * 2048);
    __builtin_amdgcn_sched_barrier(0);
    int slot = 0;
#pragma unroll 1
    for (int s = 0; s < 9; ++s) {
      STEP(0, xa1, ba1);
      const u32 dl = slot + 1 == NSL ? (u32)(-(NSL - 1) * 32768) : 32768u;
      slot = slot + 1 == NSL ? 0 : slot + 1;
      xa0 += dl; xa1 += dl; ba0 += dl; ba1 += dl;
      bar();
      STEP(1, xa0, ba0);
    }
    ST(3);
    const f32x2 lr2 = {lam.x, lam.x}, li2 = {lam.y, lam.y};
    f32x2 Br[16], Bi[16];
#pragma unroll
    for (int i = 0; i < 8; ++i) { Br[2 * i] = f32x2{ar[i][0], ar[i][1]}; Br[2 * i + 1] = f32x2{ar[i][2], ar[i][3]}; Bi[2 * i] = f32x2{ai[i][0], ai[i][1]}; Bi[2 * i + 1] = f32x2{ai[i][2], ai[i][3]}; }
    f32x2 sr = Br[0], si = Bi[0];
#pragma unroll
    for (int k = 1; k < 16; ++k) {
      f32x2 t1 = __builtin_elementwise_fma(lr2, sr, Br[k]), t2 = __builtin_elementwise_fma(lr2, si, Bi[k]);
      f32x2 n1 = __builtin_elementwise_fma(-li2, si, t1), n2 = __builtin_elementwise_fma(li2, sr, t2);
      sr = n1; si = n2;
    }
    const f32x2 S0 = {sr.x, si.x};
    f32x2 T = cfma(mu, S0, f32x2{sr.y, si.y}), lo, hi;
    { float a, b; swap16(T.x, a, b); lo.x = a; hi.x = b; swap16(T.y, a, b); lo.y = a; hi.y = b; }
    f32x2 U = cfma(nu, lo, hi), E = (q4 & 1) ? lo : f32x2{0.f, 0.f};
    { float a, b; swap32(U.x, a, b); lo.x = a; hi.x = b; swap32(U.y, a, b); lo.y = a; hi.y = b; }
    f32x2 tot = cfma(nu2, lo, hi);
    if (q4 == 0 && c < 15) {
      u32x4 gv = {__float_as_uint(tot.x), tlo, __float_as_uint(tot.y), thi};
      __amdgpu_buffer_rsrc_t gr = __builtin_amdgcn_make_buffer_rsrc(P.ws + WS_GRAN, 0, 9 * 16 * 1024, 0x00020000);
      __builtin_amdgcn_raw_buffer_store_b128(gv, gr, l15 * 16, (j * 16 + c) * 1024 + wv * 256, 16);
    }
    ST(4);
    if (q4 & 2) E = cfma(nuq, lo, E);
    f32x2 La_ = cmul(coef, E), Lb_ = cmul(coef, cfma(mu, E, S0));
    const f32x2 cr2 = {coef.x, coef.x}, ci2 = {coef.y, coef.y};
#pragma unroll
    for (int k = 0; k < 16; ++k) {
      f32x2 n1 = __builtin_elementwise_fma(-ci2, Bi[k], cr2 * Br[k]), n2 = __builtin_elementwise_fma(ci2, Br[k], cr2 * Bi[k]);
      Br[k] = n1; Bi[k] = n2;
    }
#pragma unroll
    for (int k = 0; k < 16; ++k) asm volatile("" : "+v"(Br[k]), "+v"(Bi[k]));
    asm volatile("" : "+v"(La_), "+v"(Lb_));
    ST(8);
    f32x2 H = {0.f, 0.f};
    if (c > 0) {
      const char* gb = P.ws + WS_GRAN + j * 16384 + wv * 256;
      u32 go[4]; bool valid[4];
#pragma unroll
      for (int n = 0; n < 4; ++n) { int cs = c - 1 - q4 - 4 * n; valid[n] = cs >= 0; go[n] = (cs < 0 ? 0 : cs) * 1024 + l15 * 16; }
      u32x4 g0, g1, g2, g3;
      for (int it = 0; it < SPIN_MAX; ++it) {
        asm volatile("global_load_dwordx4 %0, %4, %8 sc1\n\tglobal_load_dwordx4 %1, %5, %8 sc1\n\tglobal_load_dwordx4 %2, %6, %8 sc1\n\tglobal_load_dwordx4 %3, %7, %8 sc1\n\ts_waitcnt vmcnt(0)"
                     : "=&v"(g0), "=&v"(g1), "=&v"(g2), "=&v"(g3) : "v"(go[0]), "v"(go[1]), "v"(go[2]), "v"(go[3]), "s"(gb) : "memory");
        bool ok = (!valid[0] || (g0.y == tlo && g0.w == thi)) && (!valid[1] || (g1.y == tlo && g1.w == thi)) &&
                  (!valid[2] || (g2.y == tlo && g2.w == thi)) && (!valid[3] || (g3.y == tlo && g3.w == thi));
        if (__all(ok)) break;
        __builtin_amdgcn_s_sleep(1);
      }
      f32x2 z = {0.f, 0.f};
      f32x2 s3 = valid[3] ? f32x2{__uint_as_float(g3.x), __uint_as_float(g3.z)} : z;
      f32x2 s2 = valid[2] ? f32x2{__uint_as_float(g2.x), __uint_as_float(g2.z)} : z;
      f32x2 s1 = valid[1] ? f32x2{__uint_as_float(g1.x), __uint_as_float(g1.z)} : z;
      f32x2 s0 = valid[0] ? f32x2{__uint_as_float(g0.x), __uint_as_float(g0.z)} : z;
      H = cmul(Lg, cfma(La4, cfma(La4, cfma(La4, s3, s2), s1), s0));
      float a, b;
      swap16(H.x, a, b); H.x = a + b; swap16(H.y, a, b); H.y = a + b;
      swap32(H.x, a, b); H.x = a + b; swap32(H.y, a, b); H.y = a + b;
    }
    ST(5);
    f32x2 sa = cfma(cm0, H, La_), sb_ = cfma(cm1, H, Lb_);
    sr = f32x2{sa.x, sb_.x}; si = f32x2{sa.y, sb_.y};
#pragma unroll
    for (int k = 0; k < 16; ++k) {
      f32x2 t1 = __builtin_elementwise_fma(lr2, sr, Br[k]), t2 = __builtin_elementwise_fma(lr2, si, Bi[k]);
      f32x2 n1 = __builtin_elementwise_fma(-li2, si, t1), n2 = __builtin_elementwise_fma(li2, sr, t2);
      sr = n1; si = n2; Br[k] = sr; Bi[k] = si;
    }
    ST(10);
    __amdgpu_buffer_rsrc_t hr = __builtin_amdgcn_make_buffer_rsrc(P.ws + WS_H, 0, H_BYTES, 0x00020000);
    const u32 my = SCR + wv * 4096;
    const u32 fsw = ((l15 & 3) << 2) | ((l15 >> 2) & 1);
    const u32 wa = my + l15 * 256;
    u32 ra[2];
#pragma unroll
    for (int u = 0; u < 2; ++u) {
      int row = 8 * (q4 >> 1) + 4 * u + (l15 >> 2);
      u32 fs = ((row & 3) << 2) | ((row >> 2) & 1);
      ra[u] = my + row * 256 + (((2 * (q4 & 1) + ((l15 & 3) >> 1)) ^ fs) << 4) + (l15 & 1) * 8;
    }
#pragma unroll
    for (int part = 0; part < 2; ++part) {
#pragma unroll
      for (int e0 = 0; e0 < 2; ++e0)
#pragma unroll
        for (int hf = 0; hf < 2; ++hf) {
          u32x4 v;
#pragma unroll
          for (int q = 0; q < 4; ++q) {
            int k = 8 * hf + 2 * q;
            v[q] = part ? pack2(-Bi[k][e0], -Bi[k + 1][e0]) : pack2(Br[k][e0], Br[k + 1][e0]);
          }
          ldsw(wa + (((4 * q4 + 2 * e0 + hf) ^ fsw) << 4), v);
        }
#pragma unroll
      for (int rt = 0; rt < 4; ++rt) {
        s16x4 lo4 = __builtin_amdgcn_ds_read_tr16_b64_v4i16((LAS s16x4*)(size_t)(ra[0] ^ (rt * 64)));
        s16x4 hi4 = __builtin_amdgcn_ds_read_tr16_b64_v4i16((LAS s16x4*)(size_t)(ra[1] ^ (rt * 64)));
        u32x2 l2 = __builtin_bit_cast(u32x2, lo4), h2 = __builtin_bit_cast(u32x2, hi4);
        u32x4 v = {l2.x, l2.y, h2.x, h2.y};
        __builtin_amdgcn_raw_buffer_store_b128(v, hr, lane * 16, ((c * 4 + rt) * 72 + j * 8 + part * 4 + wv) * 1024, 16);
      }
    }
    ST(6);
    asm volatile("s_waitcnt vmcnt(0)" ::: "memory");
    ST(9);
    bar();
    if (wv == 0 && lane == 0)
      __hip_atomic_store((GAS u64*)(P.ws + WS_FLAG + (c * 9 + j) * 256), ((u64)thi << 32) | tlo, __ATOMIC_RELAXED, __HIP_MEMORY_SCOPE_AGENT);
    ST(7);
  }
  STAMP_FLUSH(P);
}

struct C2 { f32x2 r, i; };
DEV C2 cm2(const C2& a, const C2& b) {
  C2 o; o.r = __builtin_elementwise_fma(-a.i, b.i, a.r * b.r); o.i = __builtin_elementwise_fma(a.i, b.r, a.r * b.i); return o;
}
DEV C2 cexp2(f32x2 re, f32x2 im) { f32x2 a = cexp(re.x, im.x), b = cexp(re.y, im.y); C2 o; o.r = f32x2{a.x, b.x}; o.i = f32x2{a.y, b.y}; return o; }
DEV u32x2 pk4(const C2& a) { return u32x2{pack2(a.r.x, a.r.y), pack2(a.i.x, a.i.y)}; }
DEV f32x2 up2(u32 v) { return f32x2{__uint_as_float(v << 16), __uint_as_float(v & 0xffff0000u)}; }
DEV f32x4 mf16(u32x4 a, u32x4 b, f32x4 c) { return __builtin_amdgcn_mfma_f32_16x16x32_bf16(__builtin_bit_cast(bf16x8, a), __builtin_bit_cast(bf16x8, b), c, 0, 0, 0); }
DEV u32x4 bandop(int x, int lo, int hi, u32 mul) {
  u32 m = (u32)((((1ull << (hi - lo + 1)) - 1) << 32) >> (32 + hi - x)) & 0xffu;
  u32 sp = m | (m << 15);
  u32x4 o;
#pragma unroll
  for (int i = 0; i < 4; ++i) o[i] = ((sp >> (2 * i)) & 0x10001u) * mul;
  return o;
}
DEV u32x4 rd2(u32 a0, u32 a1) { u32x2 x = ldsr<u32x2>(a0), y = ldsr<u32x2>(a1); return u32x4{x.x, x.y, y.x, y.y}; }
constexpr u32 A_RAW = 0, A_UT = 32768, UT_STR = 4640, A_KB = A_UT + 8 * UT_STR, KB_STR = 8704, KP = 544, CPS = 2112;

DEV void a_role(const Params& P, int d0) {
  const int lane = threadIdx.x & 63, wv = __builtin_amdgcn_readfirstlane(threadIdx.x >> 6);
  const int d = d0 + wv, j = lane & 15, q = lane >> 4;
  STAMP_DECL
  ST(0);
  u32 pAr, pAi, pBr, pBi, pCr, pCi;
  {
    const u32 vo = d * 256 + lane * 4;
    asm volatile("global_load_dword %0, %6, %7\n\tglobal_load_dword %1, %6, %8\n\tglobal_load_dword %2, %6, %9\n\t"
                 "global_load_dword %3, %6, %10\n\tglobal_load_dword %4, %6, %11\n\tglobal_load_dword %5, %6, %12"
                 : "=&v"(pAr), "=&v"(pAi), "=&v"(pBr), "=&v"(pBi), "=&v"(pCr), "=&v"(pCi)
                 : "v"(vo), "s"(P.in[1]), "s"(P.in[2]), "s"(P.in[3]), "s"(P.in[4]), "s"(P.in[5]), "s"(P.in[6]) : "memory");
    const u32 uo = (256 * wv + lane) * 1152 + d0 * 2 + 3072;
    glds4((const char*)P.in[0] - 3072, A_RAW + wv * 4096, uo, uo + 64 * 1152 - 1024, uo + 128 * 1152 - 2048, uo + 192 * 1152 - 3072);
  }
  typedef const __attribute__((address_space(4))) u32* cptr;
  const u32 sdt = *(cptr)((const char*)P.in[7] + ((d * 2) & ~3)), sds = *(cptr)((const char*)P.in[8] + ((d * 2) & ~3));
  const float ldt = __uint_as_float((d & 1) ? (sdt & 0xffff0000u) : (sdt << 16));
  const float dsk = __uint_as_float((d & 1) ? (sds & 0xffff0000u) : (sds << 16));
  const float dt = rbf(__builtin_amdgcn_exp2f(ldt * 1.44269504088896341f));
  u32x4 bnd[6];
  bnd[0] = bandop(j + 16 - 8 * q, 3, 5, 0x3f80); bnd[1] = bandop(j + 16 - 8 * q, 6, 10, 0x3f80); bnd[2] = bandop(j + 16 - 8 * q, 11, 13, 0x3f80);
  bnd[3] = bandop(j + 24 - 8 * q, 14, 18, 0x3f80); bnd[4] = bandop(j + 16 - 8 * q, 5, 11, 0x3f80); bnd[5] = bandop(j + 24 - 8 * q, 12, 20, 0x3f80);
  u32x4 afx = bandop(j + 16 - 8 * q, 3, 4, 1);
  const u32 ut = A_UT + wv * UT_STR, kb = A_KB + wv * KB_STR;
  ldsw(ut + lane * 8, u32x2{0u, 0u});
#pragma unroll
  for (int i = 0; i < 6; ++i) asm volatile("" : "+v"(bnd[i]));
  asm volatile("" : "+v"(afx));
  ST(1);
  asm volatile("s_waitcnt vmcnt(4)" : "+v"(pAr), "+v"(pAi), "+v"(pBr), "+v"(pBi), "+v"(pCr), "+v"(pCi) :: "memory");
  ST(2);
  const f32x2 dt2 = {dt, dt};
  const f32x2 are = dt2 * up2(pAr), aim = dt2 * up2(pAi);
  C2 W; { C2 B = {dt2 * up2(pBr), dt2 * up2(pBi)}, Cc = {up2(pCr), up2(pCi)}; W = cm2(Cc, B); W.i = -W.i; }
  const u32 wrow = kb + lane * 8, rfr = kb + j * KP + q * 16;
  u32x4 Yf[8];
  C2 E8 = cexp2(8.f * are, 8.f * aim);
  {
    C2 Y[16];
    Y[1] = E8;
    ldsw(wrow, u32x2{0x3f803f80u, 0u});
    ldsw(wrow + KP, pk4(Y[1]));
#pragma unroll
    for (int i = 2; i < 16; ++i) { Y[i] = (i & 1) ? cm2(Y[i - 1], Y[1]) : cm2(Y[i / 2], Y[i / 2]); ldsw(wrow + i * KP, pk4(Y[i])); }
#pragma unroll
    for (int s = 0; s < 8; ++s) Yf[s] = ldsr<u32x4>(rfr + 64 * s);
  }
  E8.i = -E8.i;
  C2 E[8];
  E[1] = cexp2(are, -aim);
  const C2 G = cexp2(128.f * are, -128.f * aim);
  E[2] = cm2(E[1], E[1]); E[3] = cm2(E[2], E[1]); E[4] = cm2(E[2], E[2]); E[5] = cm2(E[4], E[1]); E[6] = cm2(E[4], E[2]); E[7] = cm2(E[4], E[3]);
  f32x4 T0 = {0.f, 0.f, 0.f, 0.f}, T1 = T0, T2 = T0;
  C2 Pc = W;
#pragma unroll
  for (int h = 0; h < 2; ++h) {
    ldsw(wrow + (8 * h) * KP, pk4(Pc));
#pragma unroll
    for (int r = 1; r < 8; ++r) ldsw(wrow + (8 * h + r) * KP, pk4(cm2(Pc, E[r])));
    Pc = cm2(Pc, G);
  }
#pragma unroll
  for (int s = 0; s < 8; ++s) T0 = mf16(ldsr<u32x4>(rfr + 64 * s), Yf[s], T0);
  u32x2 keep[4];
#pragma unroll
  for (int h = 0; h < 2; ++h) {
    keep[2 * h] = pk4(cm2(Pc, E[4])); keep[2 * h + 1] = pk4(cm2(Pc, E8));
    ldsw(wrow + (8 * h) * KP, pk4(Pc)); ldsw(wrow + (8 * h + 1) * KP, pk4(cm2(Pc, E[2])));
    ldsw(wrow + (8 * h + 4) * KP, keep[2 * h]); ldsw(wrow + (8 * h + 5) * KP, pk4(cm2(Pc, E[6])));
    Pc = cm2(Pc, G);
  }
#pragma unroll
  for (int g = 0; g < 4; ++g) {
    ldsw(wrow + (4 * g + 2) * KP, pk4(Pc)); ldsw(wrow + (4 * g + 3) * KP, pk4(cm2(Pc, E[4])));
    Pc = cm2(Pc, G);
  }
#pragma unroll
  for (int s = 0; s < 8; ++s) T1 = mf16(ldsr<u32x4>(rfr + 64 * s), Yf[s], T1);
#pragma unroll
  for (int g = 0; g < 8; ++g) { ldsw(wrow + (4 * (g >> 1) + (g & 1)) * KP, pk4(Pc)); Pc = cm2(Pc, G); }
  ldsw(wrow + 2 * KP, pk4(Pc));
#pragma unroll
  for (int g = 0; g < 4; ++g) ldsw(wrow + (4 * g + 3) * KP, keep[g]);
#pragma unroll
  for (int s = 0; s < 8; ++s) T2 = mf16(ldsr<u32x4>(rfr + 64 * s), Yf[s], T2);
  ST(3);
  asm volatile("s_waitcnt vmcnt(0)" ::: "memory");
  ST(4);
  {
    const u32 ra = A_RAW + wv * 4096 + (8 * q + 4 * ((j & 3) >> 1) + (j >> 2)) * 16 + (j & 1) * 8;
    const u32 wa = A_UT + (j & 7) * UT_STR + (256 + 256 * wv + 8 * q + 4 * (j >> 3)) * 2;
#pragma unroll
    for (int i = 0; i < 8; ++i) {
      s16x4 v = __builtin_amdgcn_ds_read_tr16_b64_v4i16((LAS s16x4*)(size_t)(ra + i * 512));
      ldsw(wa + i * 64, v);
    }
  }
  const u32 c3 = kb + 3 * CPS;
  ldsw(c3 + lane * 16, u32x4{0u, 0u, 0u, 0u}); ldsw(c3 + 1024 + lane * 16, u32x4{0u, 0u, 0u, 0u});
  {
    const int l0 = 128 * (q >> 1) + 4 * (q & 1) + 8 * j;
    ldsw(c3 + (544 - l0) * 2, u32x2{pack2(T0[3], T0[2]), pack2(T0[1], T0[0])});
    ldsw(c3 + (288 - l0) * 2, u32x2{pack2(T2[3], T1[1]), pack2(T1[0], T1[0])});
    if (lane == 63) ldsw(c3 + 64, u32x2{pack2(0.f, T2[3]), pack2(T2[3], T2[3])});
    const int mu0 = 32 + 8 * q + (j >> 1), od = j & 1;
    u32 p2 = pack2(lane == 0 ? T2[0] : T1[2], T1[3]);
    u32 p3 = pack2(lane == 0 ? T2[2] : T2[0], T2[1]);
    const int ia = lane == 0 ? 647 + 204 - 63 : (od ? 647 + 68 - mu0 : 647 + 204 + 1 - mu0);
    ldsw(c3 + ia * 2, (u16)p2);
    ldsw(c3 + (647 + 136 * od - mu0) * 2, (u16)(p2 >> 16));
    const int i3 = (od ? 967 - 64 : 967 + 84 - 63) - 16 * q - (j >> 1);
    ldsw(c3 + (lane == 0 ? 967 + 84 - 127 : i3) * 2, (u16)p3);
    ldsw(c3 + (i3 - 8) * 2, (u16)(p3 >> 16));
    const u32 k1024 = __builtin_amdgcn_readfirstlane(p2) & 0xffffu;
#pragma unroll
    for (int i = 0; i < 4; ++i) afx[i] *= k1024;
#pragma unroll
    for (int h = 0; h < 2; ++h) {
      u32x4 x = ldsr<u32x4>(c3 + h * 1024 + lane * 16); u32x2 y = ldsr<u32x2>(c3 + h * 1024 + lane * 16 + 16);
      u32 a0 = __builtin_amdgcn_alignbit(x.y, x.x, 16), a1 = __builtin_amdgcn_alignbit(x.z, x.y, 16), a2 = __builtin_amdgcn_alignbit(x.w, x.z, 16),
          a3 = __builtin_amdgcn_alignbit(y.x, x.w, 16), a4 = __builtin_amdgcn_alignbit(y.y, y.x, 16);
      ldsw(kb + 2 * CPS + h * 1024 + lane * 16, u32x4{a0, a1, a2, a3});
      ldsw(kb + 1 * CPS + h * 1024 + lane * 16, u32x4{x.y, x.z, x.w, y.x});
      ldsw(kb + h * 1024 + lane * 16, u32x4{a1, a2, a3, a4});
    }
  }
  u32x4 Af[17], t2[3][2], t3[4];
  {
    const u32 fb = kb + (j & 3) * CPS - 8 * (j >> 2);
    const u32 fa = fb + 16 * q + (544 - 16 - 512) * 2;
#pragma unroll
    for (int n = 0; n < 17; ++n) Af[n] = rd2(fa + (16 - n) * 64, fa + (16 - n) * 64 + 8);
    const u32 ft = fb + 8 * q;
#pragma unroll
    for (int D = 2; D < 5; ++D) { u32 a = ft + (644 - 16 * D) * 2; t2[D - 2][0] = rd2(a, a + 136); t2[D - 2][1] = rd2(a + 272, a + 408); }
#pragma unroll
    for (int D = 4; D < 8; ++D) { u32 a = ft + (964 - 16 * D) * 2; t3[D - 4] = rd2(a, a + 168); }
  }
  ST(5);
  bar();
  ST(6);
  f32x4 acc[8];
#pragma unroll
  for (int i = 0; i < 8; ++i) acc[i] = f32x4{0.f, 0.f, 0.f, 0.f};
  const f32x4 zz = {0.f, 0.f, 0.f, 0.f};
  const u32 ub = ut + (16 * j + 8 * q) * 2;
  const u32 yb = ut + (16 * j + 4 * q) * 2;
#define BADDR(k) (ub + (256 + 256 * ((k) >> 3) - 16 - 32 * ((k) & 7)) * 2)
  constexpr int NB = 4;
  u32x4 Bf[NB], W1;
#pragma unroll
  for (int k = 0; k < NB - 1; ++k) Bf[k] = ldsr<u32x4>(BADDR(k));
  W1 = ldsr<u32x4>(ub + (256 - 24) * 2);
  f32x4 V[6]; u32x4 vp[3]; u32x2 us;
#pragma unroll
  for (int e = 0; e < 8; ++e) {
#pragma unroll
    for (int b = 0; b < 8; ++b) {
      const int k = 8 * e + b;
      if (k + NB - 1 < 64) Bf[(k + NB - 1) % NB] = ldsr<u32x4>(BADDR(k + NB - 1));
      if (b == 5) { us = ldsr<u32x2>(yb + (256 + 256 * e) * 2); }
      if (b == 6 && e < 5) W1 = ldsr<u32x4>(ub + (256 + 256 * (e + 1) - 24) * 2);
      const u32x4 B = Bf[k % NB];
      acc[e] = mf16(Af[b], B, acc[e]);
      if (e < 7) acc[e + 1] = mf16(Af[8 + b], B, acc[e + 1]);
      if (b == 0) {
        if (e < 6) { V[0] = mf16(B, bnd[0], zz); V[1] = mf16(B, bnd[1], zz); V[2] = mf16(B, bnd[2], zz); V[3] = mf16(W1, bnd[3], zz); }
        if (e < 4) { V[4] = mf16(B, bnd[4], zz); V[5] = mf16(W1, bnd[5], zz); }
        if (e < 6) acc[e + 2] = mf16(Af[16], B, acc[e + 2]);
        if (e < 4) acc[e + 4] = mf16(afx, B, acc[e + 4]);
        if (e > 0) {
          f32x2 u01 = up2(us.x), u23 = up2(us.y);
          float o0 = rbf(acc[e - 1][0]) + rbf(u01.x * dsk), o1 = rbf(acc[e - 1][1]) + rbf(u01.y * dsk);
          float o2 = rbf(acc[e - 1][2]) + rbf(u23.x * dsk), o3 = rbf(acc[e - 1][3]) + rbf(u23.y * dsk);
          ldsw(yb + (256 * (e - 1)) * 2, u32x2{pack2(o0, o1), pack2(o2, o3)});
        }
      }
      if (b == 1 && e < 6) {
#pragma unroll
        for (int p = 0; p < 3; ++p) if (p < 2 || e < 4)
          vp[p] = u32x4{pack2(V[2 * p][0], V[2 * p][1]), pack2(V[2 * p][2], V[2 * p][3]), pack2(V[2 * p + 1][0], V[2 * p + 1][1]), pack2(V[2 * p + 1][2], V[2 * p + 1][3])};
      }
      if (b >= 2 && b <= 4 && e + b < 8) { acc[e + b] = mf16(vp[0], t2[b - 2][0], acc[e + b]); acc[e + b] = mf16(vp[1], t2[b - 2][1], acc[e + b]); }
      if (b >= 4 && e + b < 8) acc[e + b] = mf16(vp[2], t3[b - 4], acc[e + b]);
      __builtin_amdgcn_sched_barrier(0);
    }
  }
  {
    f32x2 u01 = up2(us.x), u23 = up2(us.y);
    float o0 = rbf(acc[7][0]) + rbf(u01.x * dsk), o1 = rbf(acc[7][1]) + rbf(u01.y * dsk);
    float o2 = rbf(acc[7][2]) + rbf(u23.x * dsk), o3 = rbf(acc[7][3]) + rbf(u23.y * dsk);
    ldsw(yb + (256 * 7) * 2, u32x2{pack2(o0, o1), pack2(o2, o3)});
  }
  ST(7);
  bar();
  ST(8);
  {
    const u32 ra = A_UT + (4 * (q & 1) + (j >> 2)) * UT_STR + (256 * wv + 16 * (q >> 1) + 4 * (j & 3)) * 2;
    float* op = P.out + (size_t)(256 * wv + 16 * (q >> 1) + j) * 1152 + d0 + 4 * (q & 1);
#pragma unroll
    for (int i = 0; i < 8; ++i) {
      u32x2 v = __builtin_bit_cast(u32x2, __builtin_amdgcn_ds_read_tr16_b64_v4i16((LAS s16x4*)(size_t)(ra + i * 64)));
      f32x2 a = up2(v.x), b = up2(v.y);
      *(f32x4*)(op + (size_t)i * 32 * 1152) = f32x4{a.x, a.y, b.x, b.y};
    }
  }
  ST(9);
  STAMP_FLUSH(P);
}

__global__ __launch_bounds__(512, 2) void fused(Params P) {
  const int b = blockIdx.x, xcd = b & 7, slot = b >> 3;
  if (slot < 18) g_role(P, 2 * xcd + (slot >= 9), slot >= 9 ? slot - 9 : slot);
  if (slot >= 18 && slot < 27) a_role(P, 72 * xcd + 8 * (slot - 18));
}

extern "C" void kernel_launch(void* const* d_in, const int* in_sizes, int n_in, void* d_out, int out_size, void* d_ws, size_t ws_size, hipStream_t stream) {
  Params P;
  for (int i = 0; i < 18; ++i) P.in[i] = (const u16*)d_in[i];
  P.out = (float*)d_out; P.ws = (char*)d_ws;
  (void)hipFuncSetAttribute((const void*)fused, hipFuncAttributeMaxDynamicSharedMemorySize, LDS_BYTES);
  fused<<<256, 512, LDS_BYTES, stream>>>(P);
}
```

```cpp
#define OAUX 2
#include <hip/hip_runtime.h>
#include <stdint.h>

typedef unsigned short u16; typedef unsigned u32; typedef unsigned long long u64;
typedef __bf16 bf16x8 __attribute__((ext_vector_type(8)));
typedef __bf16 bf16x2 __attribute__((ext_vector_type(2)));
typedef short s16x4 __attribute__((ext_vector_type(4)));
typedef float f32x16 __attribute__((ext_vector_type(16)));
typedef float f32x4 __attribute__((ext_vector_type(4)));
typedef float f32x2 __attribute__((ext_vector_type(2)));
typedef u32 u32x4 __attribute__((ext_vector_type(4)));
typedef u32 u32x2 __attribute__((ext_vector_type(2)));
#define LAS __attribute__((address_space(3)))
#define GAS __attribute__((address_space(1)))
#define DEV __device__ __forceinline__

struct Params { const u16* in[18]; float* out; char* ws; };

constexpr u32 WS_EPOCH = 0, WS_FLAG = 4096, WS_GRAN = 65536, WS_H = 262144;
constexpr u32 H_BYTES = 16u * 4u * 72u * 1024u;
constexpr u32 LDS_BYTES = 163840, SCR = 147456;
constexpr int SPIN_MAX = 1 << 16;
#define OAUXA OAUX

#define STAMP_DECL
#define ST(i)
#define STAMP_FLUSH(P)

DEV float bf2f(u16 v) { return __uint_as_float(((u32)v) << 16); }
DEV u32 pack2(float a, float b) { bf16x2 r; r[0] = (__bf16)a; r[1] = (__bf16)b; return __builtin_bit_cast(u32, r); }
DEV float rbf(float f) { return (float)(__bf16)f; }
DEV void bar() { asm volatile("s_waitcnt lgkmcnt(0)\n\ts_barrier" ::: "memory"); }
DEV u32 mix32(u32 x) { x *= 0x9E3779B1u; x ^= x >> 15; x *= 0x85EBCA77u; x ^= x >> 13; return x + 0x632BE5ABu; }
DEV f32x2 cmul(f32x2 a, f32x2 b) { return f32x2{a.x * b.x - a.y * b.y, a.x * b.y + a.y * b.x}; }
DEV f32x2 cfma(f32x2 a, f32x2 b, f32x2 c) { return f32x2{fmaf(a.x, b.x, fmaf(-a.y, b.y, c.x)), fmaf(a.x, b.y, fmaf(a.y, b.x, c.y))}; }
DEV f32x2 cexp(float re, float im) {
  float e = __builtin_amdgcn_exp2f(re * 1.44269504088896341f);
  float r = __builtin_amdgcn_fractf(im * 0.15915494309189535f);
  return f32x2{e * __builtin_amdgcn_cosf(r), e * __builtin_amdgcn_sinf(r)};
}
DEV void glds4(const char* gb, u32 ldsb, u32 v0, u32 v1, u32 v2, u32 v3) {
  u32 keep;
  asm volatile("s_mov_b32 %0, m0\n\ts_mov_b32 m0, %2\n\ts_nop 0\n\t"
               "global_load_lds_dwordx4 %3, %1\n\tglobal_load_lds_dwordx4 %4, %1 offset:1024\n\t"
               "global_load_lds_dwordx4 %5, %1 offset:2048\n\tglobal_load_lds_dwordx4 %6, %1 offset:3072\n\ts_mov_b32 m0, %0"
               : "=&s"(keep) : "s"(gb), "s"(ldsb), "v"(v0), "v"(v1), "v"(v2), "v"(v3) : "memory");
}
template <class T> DEV T ldsr(u32 a) { return *(const LAS T*)(size_t)a; }
template <class T> DEV void ldsw(u32 a, T v) { *(LAS T*)(size_t)a = v; }
DEV f32x2 pr(const f32x16& a, int m) { return f32x2{a[2 * m], a[2 * m + 1]}; }

#define RA 1
DEV void swap16(float x, float& lo, float& hi) { auto r = __builtin_amdgcn_permlane16_swap(__float_as_uint(x), __float_as_uint(x), false, false); lo = __uint_as_float(r[0]); hi = __uint_as_float(r[1]); }
DEV void swap32(float x, float& lo, float& hi) { auto r = __builtin_amdgcn_permlane32_swap(__float_as_uint(x), __float_as_uint(x), false, false); lo = __uint_as_float(r[0]); hi = __uint_as_float(r[1]); }
DEV int wrap9(int v) { return v >= 9 ? v - 9 : v; }

DEV void g_role(const Params& P, int c, int j) {
  const int lane = threadIdx.x & 63, wv = __builtin_amdgcn_readfirstlane(threadIdx.x >> 6);
  STAMP_DECL
  ST(0);
  const u64 ep = *(const __attribute__((address_space(4))) u64*)(P.ws + WS_EPOCH);
  const int rot = wrap9(j + 4 * (c & 1));
  constexpr int NSL = RA + 2;
  if (wv >= 4) {
    const int w = wv - 4;
    const int l31 = lane & 31, hh = lane >> 5;
    u32 vx[4], vb[4];
#pragma unroll
    for (int q = 0; q < 4; ++q) {
      int i = 2 * w + (q >> 1), qq = 2 * (q & 1) + (lane >> 5), r = (lane >> 3) & 3;
      int time = 32 * qq + 16 * (r & 1) + 2 * i + (r >> 1);
      u32 ch = (u32)(((lane & 7) ^ ((4 * (q & 1) + (lane >> 4)) & 7)) << 4) + 3072 - 1024 * q;
      vx[q] = (128 * c + time) * 1152 + ch;
      vb[q] = (64 * j + 32 * (w & 1) + 8 * q + (lane >> 3)) * 1152 + ch;
    }
    u32 dk0, dk1;
    asm volatile("global_load_ushort %0, %2, %3\n\tglobal_load_ushort %1, %2, %3 offset:64" : "=&v"(dk0), "=&v"(dk1) : "v"((64 * j + l31) * 2), "s"(P.in[17]) : "memory");
    const char* xb = (const char*)P.in[9] - 3072;
    const char* bb = (const char*)(w < 2 ? P.in[12] : P.in[13]) - 3072;
    const char* cb = (const char*)(w < 2 ? P.in[14] : P.in[15]) - 3072;
#pragma unroll
    for (int s = 0; s <= RA; ++s) {
      u32 sl = s * 32768 + w * 4096; int ks = wrap9(rot + s);
      glds4(xb + ks * 128, sl, vx[0], vx[1], vx[2], vx[3]);
      glds4(bb + ks * 128, sl + 16384, vb[0], vb[1], vb[2], vb[3]);
    }
    u32 tlo = mix32((u32)ep), thi = mix32((u32)(ep >> 32) ^ 0x5bd1e995u);
    asm volatile("" :: "s"(tlo), "s"(thi));
    ST(1);
    s16x4 xs[8];
    asm volatile("s_waitcnt vmcnt(%2)" : "+v"(dk0), "+v"(dk1) : "n"(8 * RA) : "memory");
    const float dsk0 = __uint_as_float(dk0 << 16), dsk1 = __uint_as_float(dk1 << 16);
    int slot_cur = 0, slot_new = RA + 1 == NSL ? 0 : RA + 1, kn = wrap9(rot + RA + 1);
    int cpc = 0;
#pragma unroll 1
    for (int s = 0; s < 9; ++s) {
      bar();
      u32 sl = slot_new * 32768 + w * 4096;
      if (s + RA + 1 <= 8) {
        glds4(xb + kn * 128, sl, vx[0], vx[1], vx[2], vx[3]);
        glds4(bb + kn * 128, sl + 16384, vb[0], vb[1], vb[2], vb[3]);
      } else {
        glds4(cb + slot_new * 256, sl, vb[0], vb[1], vb[2], vb[3]);
        glds4(cb + slot_new * 256 + 128, sl + 16384, vb[0], vb[1], vb[2], vb[3]);
      }
      if (wrap9(rot + s) == j) {
        const int e = (lane >> 2) & 3, pp = lane & 3;
        u32 sb = slot_cur * 32768 + (32 * hh + 4 * w + 16 * (e >> 1) + 2 * (e & 1)) * 128 + (((2 * ((lane >> 4) & 1) + (pp >> 1)) ^ ((w << 1) | (e & 1))) << 4) + (pp & 1) * 8;
#pragma unroll
        for (int g = 0; g < 4; ++g) {
          xs[g] = __builtin_amdgcn_ds_read_tr16_b64_v4i16((LAS s16x4*)(size_t)(sb + (64 * (g & 1) + (g >> 1)) * 128));
          xs[4 + g] = __builtin_amdgcn_ds_read_tr16_b64_v4i16((LAS s16x4*)(size_t)((sb ^ 64) + (64 * (g & 1) + (g >> 1)) * 128));
        }
      }
      slot_cur = slot_cur + 1 == NSL ? 0 : slot_cur + 1; slot_new = slot_new + 1 == NSL ? 0 : slot_new + 1; kn = wrap9(kn + 1);
      asm volatile("s_waitcnt vmcnt(%0)" :: "n"(8 * RA) : "memory");
    }
    bar();
    ST(3);
    for (int q = 2 * NSL; q < 9; ++q) glds4(cb + q * 128, q * 16384 + w * 4096, vb[0], vb[1], vb[2], vb[3]);
    glds4(cb + (8 % NSL) * 256, (8 % NSL) * 32768 + w * 4096, vb[0], vb[1], vb[2], vb[3]);
    glds4(cb + (8 % NSL) * 256 + 128, (8 % NSL) * 32768 + 16384 + w * 4096, vb[0], vb[1], vb[2], vb[3]);
    f32x16 acc0, acc1;
#pragma unroll
    for (int r = 0; r < 16; ++r) {
      acc0[r] = rbf(bf2f((u16)xs[r >> 2][r & 3]) * dsk0);
      acc1[r] = rbf(bf2f((u16)xs[4 + (r >> 2)][r & 3]) * dsk1);
    }
    asm volatile("" : "+v"(acc0), "+v"(acc1));
    asm volatile("s_waitcnt vmcnt(0)" ::: "memory");
    ST(1);
    bar();
    ST(4);
    {
      const u64 tag = ((u64)thi << 32) | tlo;
      GAS u64* fp = (GAS u64*)(P.ws + WS_FLAG + (c * 9 + (lane < 9 ? lane : 0)) * 256);
      for (int it = 0; it < SPIN_MAX; ++it) {
        u64 v = __hip_atomic_load(fp, __ATOMIC_RELAXED, __HIP_MEMORY_SCOPE_AGENT);
        if (__all(v == tag)) break;
        __builtin_amdgcn_s_sleep(1);
      }
      asm volatile("" ::: "memory");
    }
    ST(5);
    if (c == 15 && j == 0 && w == 0 && lane == 0)
      __hip_atomic_store((GAS u64*)(P.ws + WS_EPOCH), ((u64)((u32)(ep >> 32) + 1u) << 32) | (u32)((u32)ep + 1u), __ATOMIC_RELAXED, __HIP_MEMORY_SCOPE_AGENT);
    __amdgpu_buffer_rsrc_t hr = __builtin_amdgcn_make_buffer_rsrc(P.ws + WS_H, 0, H_BYTES, 0x00020000);
    const u32 hs = (c * 4 + w) * 72 * 1024;
    const int r2 = j;
    int pcs[9];
#pragma unroll
    for (int q = 0; q < 9; ++q) pcs[q] = wrap9(r2 + q);
    u32 aoff[4];
#pragma unroll
    for (int s = 0; s < 4; ++s) aoff[s] = l31 * 128 + (((2 * s + hh) ^ ((l31 >> 1) & 7)) << 4);
#define PFD 12
    constexpr int PF = PFD;
    u32x4 hf[PF];
#define HLOAD(k) __builtin_amdgcn_raw_buffer_load_b128(hr, lane * 16 + ((k) & 3) * 1024, hs + pcs[(k) >> 3] * 8192 + (((k) >> 2) & 1) * 4096, 16)
#pragma unroll
    for (int k = 0; k < PF; ++k) hf[k] = HLOAD(k);
    bf16x8 cf[2][4];
#define CREAD(set, k) do { _Pragma("unroll") for (int u = 0; u < 2; ++u) { u32 ca = pcs[((k) + u) >> 3] * 16384 + aoff[((k) + u) & 3]; u32 cbase = ((((k) + u) >> 2) & 1) * 8192; \
      cf[set][2 * u] = ldsr<bf16x8>(ca + cbase); cf[set][2 * u + 1] = ldsr<bf16x8>(ca + cbase + 4096); } } while (0)
    CREAD(0, 0);
    __builtin_amdgcn_sched_barrier(0);
#pragma unroll
    for (int k = 0; k < 72; k += 2) {
      const int set = (k >> 1) & 1;
      if (k + 2 < 72) CREAD(set ^ 1, k + 2);
      __builtin_amdgcn_sched_barrier(0);
#pragma unroll
      for (int u = 0; u < 2; ++u) {
        bf16x8 a = __builtin_bit_cast(bf16x8, hf[(k + u) % PF]);
        acc0 = __builtin_amdgcn_mfma_f32_32x32x16_bf16(a, cf[set][2 * u], acc0, 0, 0, 0);
        acc1 = __builtin_amdgcn_mfma_f32_32x32x16_bf16(a, cf[set][2 * u + 1], acc1, 0, 0, 0);
        if (k + u + PF < 72) hf[(k + u) % PF] = HLOAD(k + u + PF);
      }
      asm volatile("" : "+v"(acc0), "+v"(acc1));
      __builtin_amdgcn_sched_barrier(0);
    }
    ST(6);
    __amdgpu_buffer_rsrc_t orr = __builtin_amdgcn_make_buffer_rsrc(P.out, 0, 2048 * 1152 * 4, 0x00020000);
    const u32 ov = ((128 * c + 32 * w + 4 * hh) * 1152 + 576 + 64 * j + l31) * 4;
#pragma unroll
    for (int r = 0; r < 16; ++r) {
      __builtin_amdgcn_raw_buffer_store_b32(__float_as_uint(acc0[r]), orr, ov, (8 * (r >> 2) + (r & 3)) * 4608, OAUX);
      __builtin_amdgcn_raw_buffer_store_b32(__float_as_uint(acc1[r]), orr, ov + 128, (8 * (r >> 2) + (r & 3)) * 4608, OAUX);
    }
    ST(7);
  } else {
    const int l15 = lane & 15, q4 = lane >> 4;
    const int p = 64 * j + 16 * wv + l15;
    float dt = rbf(__builtin_amdgcn_exp2f(bf2f(P.in[16][p]) * 1.44269504088896341f));
    float Lr = bf2f(P.in[10][p]), Li = bf2f(P.in[11][p]);
    f32x2 lam = cexp(dt * Lr, dt * Li);
    float inv = 2.f / (Lr * Lr + Li * Li);
    f32x2 coef = cmul(f32x2{lam.x - 1.f, lam.y}, f32x2{Lr * inv, -Li * inv});
    f32x2 mu = cmul(lam, lam); mu = cmul(mu, mu); mu = cmul(mu, mu); mu = cmul(mu, mu);
    f32x2 nu = cmul(mu, mu), nu2 = cmul(nu, nu), La = cmul(nu2, nu2);
    f32x2 La2 = cmul(La, La), La4 = cmul(La2, La2);
    f32x2 one = {1.f, 0.f};
    f32x2 Lg = cmul((q4 & 2) ? La2 : one, (q4 & 1) ? La : one);
    f32x2 nuq = (q4 & 1) ? nu : one;
    f32x2 cm0 = cmul(coef, cmul((q4 & 2) ? nu2 : one, nuq)), cm1 = cmul(cm0, mu);
    u32 tlo = mix32((u32)ep), thi = mix32((u32)(ep >> 32) ^ 0x5bd1e995u);
    asm volatile("" :: "s"(tlo), "s"(thi));
    asm volatile("" : "+v"(lam), "+v"(coef), "+v"(mu), "+v"(La4), "+v"(Lg), "+v"(cm0), "+v"(cm1), "+v"(nu), "+v"(nu2), "+v"(nuq));
    f32x4 ar[8], ai[8];
#pragma unroll
    for (int i = 0; i < 8; ++i) { ar[i] = f32x4{0.f, 0.f, 0.f, 0.f}; ai[i] = ar[i]; }
    ST(1);
    u32 xa0 = l15 * 128 + ((q4 ^ (l15 >> 1)) << 4), xa1 = xa0 ^ 64, ba0 = xa0 + 16384 + wv * 2048, ba1 = ba0 ^ 64;
    bf16x8 A[2][8], Bq[2][2];
#define STEP(cs, xa, ba) do { Bq[cs ^ 1][0] = ldsr<bf16x8>(ba); Bq[cs ^ 1][1] = ldsr<bf16x8>(ba + 8192); \
      _Pragma("unroll") for (int i = 0; i < 8; ++i) { A[cs ^ 1][i] = ldsr<bf16x8>(xa + i * 2048); \
        ar[i] = __builtin_amdgcn_mfma_f32_16x16x32_bf16(A[cs][i], Bq[cs][0], ar[i], 0, 0, 0); ai[i] = __builtin_amdgcn_mfma_f32_16x16x32_bf16(A[cs][i], Bq[cs][1], ai[i], 0, 0, 0); \
        asm volatile("" : "+v"(ar[i]), "+v"(ai[i])); __builtin_amdgcn_sched_barrier(0); } } while (0)
    bar();
    ST(2);
    Bq[0][0] = ldsr<bf16x8>(ba0); Bq[0][1] = ldsr<bf16x8>(ba0 + 8192);
#pragma unroll
    for (int i = 0; i < 8; ++i) A[0][i] = ldsr<bf16x8>(xa0 + i * 2048);
    __builtin_amdgcn_sched_barrier(0);
    int slot = 0;
#pragma unroll 1
    for (int s = 0; s < 9; ++s) {
      STEP(0, xa1, ba1);
      const u32 dl = slot + 1 == NSL ? (u32)(-(NSL - 1) * 32768) : 32768u;
      slot = slot + 1 == NSL ? 0 : slot + 1;
      xa0 += dl; xa1 += dl; ba0 += dl; ba1 += dl;
      bar();
      STEP(1, xa0, ba0);
    }
    ST(3);
    const f32x2 lr2 = {lam.x, lam.x}, li2 = {lam.y, lam.y};
    f32x2 Br[16], Bi[16];
#pragma unroll
    for (int i = 0; i < 8; ++i) { Br[2 * i] = f32x2{ar[i][0], ar[i][1]}; Br[2 * i + 1] = f32x2{ar[i][2], ar[i][3]}; Bi[2 * i] = f32x2{ai[i][0], ai[i][1]}; Bi[2 * i + 1] = f32x2{ai[i][2], ai[i][3]}; }
    f32x2 sr = Br[0], si = Bi[0];
#pragma unroll
    for (int k = 1; k < 16; ++k) {
      f32x2 t1 = __builtin_elementwise_fma(lr2, sr, Br[k]), t2 = __builtin_elementwise_fma(lr2, si, Bi[k]);
      f32x2 n1 = __builtin_elementwise_fma(-li2, si, t1), n2 = __builtin_elementwise_fma(li2, sr, t2);
      sr = n1; si = n2;
    }
    const f32x2 S0 = {sr.x, si.x};
    f32x2 T = cfma(mu, S0, f32x2{sr.y, si.y}), lo, hi;
    { float a, b; swap16(T.x, a, b); lo.x = a; hi.x = b; swap16(T.y, a, b); lo.y = a; hi.y = b; }
    f32x2 U = cfma(nu, lo, hi), E = (q4 & 1) ? lo : f32x2{0.f, 0.f};
    { float a, b; swap32(U.x, a, b); lo.x = a; hi.x = b; swap32(U.y, a, b); lo.y = a; hi.y = b; }
    f32x2 tot = cfma(nu2, lo, hi);
    if (q4 == 0 && c < 15) {
      u32x4 gv = {__float_as_uint(tot.x), tlo, __float_as_uint(tot.y), thi};
      __amdgpu_buffer_rsrc_t gr = __builtin_amdgcn_make_buffer_rsrc(P.ws + WS_GRAN, 0, 9 * 16 * 1024, 0x00020000);
      __builtin_amdgcn_raw_buffer_store_b128(gv, gr, l15 * 16, (j * 16 + c) * 1024 + wv * 256, 16);
    }
    ST(4);
    if (q4 & 2) E = cfma(nuq, lo, E);
    f32x2 La_ = cmul(coef, E), Lb_ = cmul(coef, cfma(mu, E, S0));
    const f32x2 cr2 = {coef.x, coef.x}, ci2 = {coef.y, coef.y};
#pragma unroll
    for (int k = 0; k < 16; ++k) {
      f32x2 n1 = __builtin_elementwise_fma(-ci2, Bi[k], cr2 * Br[k]), n2 = __builtin_elementwise_fma(ci2, Br[k], cr2 * Bi[k]);
      Br[k] = n1; Bi[k] = n2;
    }
#pragma unroll
    for (int k = 0; k < 16; ++k) asm volatile("" : "+v"(Br[k]), "+v"(Bi[k]));
    asm volatile("" : "+v"(La_), "+v"(Lb_));
    ST(8);
    f32x2 H = {0.f, 0.f};
    if (c > 0) {
      const char* gb = P.ws + WS_GRAN + j * 16384 + wv * 256;
      u32 go[4]; bool valid[4];
#pragma unroll
      for (int n = 0; n < 4; ++n) { int cs = c - 1 - q4 - 4 * n; valid[n] = cs >= 0; go[n] = (cs < 0 ? 0 : cs) * 1024 + l15 * 16; }
      u32x4 g0, g1, g2, g3;
      for (int it = 0; it < SPIN_MAX; ++it) {
        asm volatile("global_load_dwordx4 %0, %4, %8 sc1\n\tglobal_load_dwordx4 %1, %5, %8 sc1\n\tglobal_load_dwordx4 %2, %6, %8 sc1\n\tglobal_load_dwordx4 %3, %7, %8 sc1\n\ts_waitcnt vmcnt(0)"
                     : "=&v"(g0), "=&v"(g1), "=&v"(g2), "=&v"(g3) : "v"(go[0]), "v"(go[1]), "v"(go[2]), "v"(go[3]), "s"(gb) : "memory");
        bool ok = (!valid[0] || (g0.y == tlo && g0.w == thi)) && (!valid[1] || (g1.y == tlo && g1.w == thi)) &&
                  (!valid[2] || (g2.y == tlo && g2.w == thi)) && (!valid[3] || (g3.y == tlo && g3.w == thi));
        if (__all(ok)) break;
        __builtin_amdgcn_s_sleep(1);
      }
      f32x2 z = {0.f, 0.f};
      f32x2 s3 = valid[3] ? f32x2{__uint_as_float(g3.x), __uint_as_float(g3.z)} : z;
      f32x2 s2 = valid[2] ? f32x2{__uint_as_float(g2.x), __uint_as_float(g2.z)} : z;
      f32x2 s1 = valid[1] ? f32x2{__uint_as_float(g1.x), __uint_as_float(g1.z)} : z;
      f32x2 s0 = valid[0] ? f32x2{__uint_as_float(g0.x), __uint_as_float(g0.z)} : z;
      H = cmul(Lg, cfma(La4, cfma(La4, cfma(La4, s3, s2), s1), s0));
      float a, b;
      swap16(H.x, a, b); H.x = a + b; swap16(H.y, a, b); H.y = a + b;
      swap32(H.x, a, b); H.x = a + b; swap32(H.y, a, b); H.y = a + b;
    }
    ST(5);
    f32x2 sa = cfma(cm0, H, La_), sb_ = cfma(cm1, H, Lb_);
    sr = f32x2{sa.x, sb_.x}; si = f32x2{sa.y, sb_.y};
#pragma unroll
    for (int k = 0; k < 16; ++k) {
      f32x2 t1 = __builtin_elementwise_fma(lr2, sr, Br[k]), t2 = __builtin_elementwise_fma(lr2, si, Bi[k]);
      f32x2 n1 = __builtin_elementwise_fma(-li2, si, t1), n2 = __builtin_elementwise_fma(li2, sr, t2);
      sr = n1; si = n2; Br[k] = sr; Bi[k] = si;
    }
    ST(10);
    __amdgpu_buffer_rsrc_t hr = __builtin_amdgcn_make_buffer_rsrc(P.ws + WS_H, 0, H_BYTES, 0x00020000);
    const u32 my = SCR + wv * 4096;
    const u32 fsw = ((l15 & 3) << 2) | ((l15 >> 2) & 1);
    const u32 wa = my + l15 * 256;
    u32 ra[2];
#pragma unroll
    for (int u = 0; u < 2; ++u) {
      int row = 8 * (q4 >> 1) + 4 * u + (l15 >> 2);
      u32 fs = ((row & 3) << 2) | ((row >> 2) & 1);
      ra[u] = my + row * 256 + (((2 * (q4 & 1) + ((l15 & 3) >> 1)) ^ fs) << 4) + (l15 & 1) * 8;
    }
#pragma unroll
    for (int part = 0; part < 2; ++part) {
#pragma unroll
      for (int e0 = 0; e0 < 2; ++e0)
#pragma unroll
        for (int hf = 0; hf < 2; ++hf) {
          u32x4 v;
#pragma unroll
          for (int q = 0; q < 4; ++q) {
            int k = 8 * hf + 2 * q;
            v[q] = part ? pack2(-Bi[k][e0], -Bi[k + 1][e0]) : pack2(Br[k][e0], Br[k + 1][e0]);
          }
          ldsw(wa + (((4 * q4 + 2 * e0 + hf) ^ fsw) << 4), v);
        }
#pragma unroll
      for (int rt = 0; rt < 4; ++rt) {
        s16x4 lo4 = __builtin_amdgcn_ds_read_tr16_b64_v4i16((LAS s16x4*)(size_t)(ra[0] ^ (rt * 64)));
        s16x4 hi4 = __builtin_amdgcn_ds_read_tr16_b64_v4i16((LAS s16x4*)(size_t)(ra[1] ^ (rt * 64)));
        u32x2 l2 = __builtin_bit_cast(u32x2, lo4), h2 = __builtin_bit_cast(u32x2, hi4);
        u32x4 v = {l2.x, l2.y, h2.x, h2.y};
        __builtin_amdgcn_raw_buffer_store_b128(v, hr, lane * 16, ((c * 4 + rt) * 72 + j * 8 + part * 4 + wv) * 1024, 16);
      }
    }
    ST(6);
    asm volatile("s_waitcnt vmcnt(0)" ::: "memory");
    ST(9);
    bar();
    if (wv == 0 && lane == 0)
      __hip_atomic_store((GAS u64*)(P.ws + WS_FLAG + (c * 9 + j) * 256), ((u64)thi << 32) | tlo, __ATOMIC_RELAXED, __HIP_MEMORY_SCOPE_AGENT);
    ST(7);
  }
  STAMP_FLUSH(P);
}

struct C2 { f32x2 r, i; };
DEV C2 cm2(const C2& a, const C2& b) {
  C2 o; o.r = __builtin_elementwise_fma(-a.i, b.i, a.r * b.r); o.i = __builtin_elementwise_fma(a.i, b.r, a.r * b.i); return o;
}
DEV C2 cexp2(f32x2 re, f32x2 im) { f32x2 a = cexp(re.x, im.x), b = cexp(re.y, im.y); C2 o; o.r = f32x2{a.x, b.x}; o.i = f32x2{a.y, b.y}; return o; }
DEV u32x2 pk4(const C2& a) { return u32x2{pack2(a.r.x, a.r.y), pack2(a.i.x, a.i.y)}; }
DEV f32x2 up2(u32 v) { return f32x2{__uint_as_float(v << 16), __uint_as_float(v & 0xffff0000u)}; }
DEV f32x4 mf16(u32x4 a, u32x4 b, f32x4 c) { return __builtin_amdgcn_mfma_f32_16x16x32_bf16(__builtin_bit_cast(bf16x8, a), __builtin_bit_cast(bf16x8, b), c, 0, 0, 0); }
DEV u32x4 bandop(int x, int lo, int hi, u32 mul) {
  u32 m = (u32)((((1ull << (hi - lo + 1)) - 1) << 32) >> (32 + hi - x)) & 0xffu;
  u32 sp = m | (m << 15);
  u32x4 o;
#pragma unroll
  for (int i = 0; i < 4; ++i) o[i] = ((sp >> (2 * i)) & 0x10001u) * mul;
  return o;
}
DEV u32x4 rd2(u32 a0, u32 a1) { u32x2 x = ldsr<u32x2>(a0), y = ldsr<u32x2>(a1); return u32x4{x.x, x.y, y.x, y.y}; }
constexpr u32 A_RAW = 0, A_UT = 32768, UT_STR = 4640, A_KB = A_UT + 8 * UT_STR, KB_STR = 8704, KP = 544, CPS = 2112;

DEV void a_role(const Params& P, int d0) {
  const int lane = threadIdx.x & 63, wv = __builtin_amdgcn_readfirstlane(threadIdx.x >> 6);
  const int d = d0 + wv, j = lane & 15, q = lane >> 4;
  STAMP_DECL
  ST(0);
  u32 pAr, pAi, pBr, pBi, pCr, pCi;
  {
    const u32 vo = d * 256 + lane * 4;
    asm volatile("global_load_dword %0, %6, %7\n\tglobal_load_dword %1, %6, %8\n\tglobal_load_dword %2, %6, %9\n\t"
                 "global_load_dword %3, %6, %10\n\tglobal_load_dword %4, %6, %11\n\tglobal_load_dword %5, %6, %12"
                 : "=&v"(pAr), "=&v"(pAi), "=&v"(pBr), "=&v"(pBi), "=&v"(pCr), "=&v"(pCi)
                 : "v"(vo), "s"(P.in[1]), "s"(P.in[2]), "s"(P.in[3]), "s"(P.in[4]), "s"(P.in[5]), "s"(P.in[6]) : "memory");
    const u32 uo = (256 * wv + lane) * 1152 + d0 * 2 + 3072;
    glds4((const char*)P.in[0] - 3072, A_RAW + wv * 4096, uo, uo + 64 * 1152 - 1024, uo + 128 * 1152 - 2048, uo + 192 * 1152 - 3072);
  }
  typedef const __attribute__((address_space(4))) u32* cptr;
  const u32 sdt = *(cptr)((const char*)P.in[7] + ((d * 2) & ~3)), sds = *(cptr)((const char*)P.in[8] + ((d * 2) & ~3));
  const float ldt = __uint_as_float((d & 1) ? (sdt & 0xffff0000u) : (sdt << 16));
  const float dsk = __uint_as_float((d & 1) ? (sds & 0xffff0000u) : (sds << 16));
  const float dt = rbf(__builtin_amdgcn_exp2f(ldt * 1.44269504088896341f));
  u32x4 bnd[6];
  bnd[0] = bandop(j + 16 - 8 * q, 3, 5, 0x3f80); bnd[1] = bandop(j + 16 - 8 * q, 6, 10, 0x3f80); bnd[2] = bandop(j + 16 - 8 * q, 11, 13, 0x3f80);
  bnd[3] = bandop(j + 24 - 8 * q, 14, 18, 0x3f80); bnd[4] = bandop(j + 16 - 8 * q, 5, 11, 0x3f80); bnd[5] = bandop(j + 24 - 8 * q, 12, 20, 0x3f80);
  u32x4 afx = bandop(j + 16 - 8 * q, 3, 4, 1);
  const u32 ut = A_UT + wv * UT_STR, kb = A_KB + wv * KB_STR;
  ldsw(ut + lane * 8, u32x2{0u, 0u});
#pragma unroll
  for (int i = 0; i < 6; ++i) asm volatile("" : "+v"(bnd[i]));
  asm volatile("" : "+v"(afx));
  ST(1);
  asm volatile("s_waitcnt vmcnt(4)" : "+v"(pAr), "+v"(pAi), "+v"(pBr), "+v"(pBi), "+v"(pCr), "+v"(pCi) :: "memory");
  ST(2);
  const f32x2 dt2 = {dt, dt};
  const f32x2 are = dt2 * up2(pAr), aim = dt2 * up2(pAi);
  C2 W; { C2 B = {dt2 * up2(pBr), dt2 * up2(pBi)}, Cc = {up2(pCr), up2(pCi)}; W = cm2(Cc, B); W.i = -W.i; }
  const u32 wrow = kb + lane * 8, rfr = kb + j * KP + q * 16;
  u32x4 Yf[8];
  C2 E8 = cexp2(8.f * are, 8.f * aim);
  {
    C2 Y[16];
    Y[1] = E8;
    ldsw(wrow, u32x2{0x3f803f80u, 0u});
    ldsw(wrow + KP, pk4(Y[1]));
#pragma unroll
    for (int i = 2; i < 16; ++i) { Y[i] = (i & 1) ? cm2(Y[i - 1], Y[1]) : cm2(Y[i / 2], Y[i / 2]); ldsw(wrow + i * KP, pk4(Y[i])); }
#pragma unroll
    for (int s = 0; s < 8; ++s) Yf[s] = ldsr<u32x4>(rfr + 64 * s);
  }
  E8.i = -E8.i;
  C2 E[8];
  E[1] = cexp2(are, -aim);
  const C2 G = cexp2(128.f * are, -128.f * aim);
  E[2] = cm2(E[1], E[1]); E[3] = cm2(E[2], E[1]); E[4] = cm2(E[2], E[2]); E[5] = cm2(E[4], E[1]); E[6] = cm2(E[4], E[2]); E[7] = cm2(E[4], E[3]);
  f32x4 T0 = {0.f, 0.f, 0.f, 0.f}, T1 = T0, T2 = T0;
  C2 Pc = W;
#pragma unroll
  for (int h = 0; h < 2; ++h) {
    ldsw(wrow + (8 * h) * KP, pk4(Pc));
#pragma unroll
    for (int r = 1; r < 8; ++r) ldsw(wrow + (8 * h + r) * KP, pk4(cm2(Pc, E[r])));
    Pc = cm2(Pc, G);
  }
#pragma unroll
  for (int s = 0; s < 8; ++s) T0 = mf16(ldsr<u32x4>(rfr + 64 * s), Yf[s], T0);
  u32x2 keep[4];
#pragma unroll
  for (int h = 0; h < 2; ++h) {
    keep[2 * h] = pk4(cm2(Pc, E[4])); keep[2 * h + 1] = pk4(cm2(Pc, E8));
    ldsw(wrow + (8 * h) * KP, pk4(Pc)); ldsw(wrow + (8 * h + 1) * KP, pk4(cm2(Pc, E[2])));
    ldsw(wrow + (8 * h + 4) * KP, keep[2 * h]); ldsw(wrow + (8 * h + 5) * KP, pk4(cm2(Pc, E[6])));
    Pc = cm2(Pc, G);
  }
#pragma unroll
  for (int g = 0; g < 4; ++g) {
    ldsw(wrow + (4 * g + 2) * KP, pk4(Pc)); ldsw(wrow + (4 * g + 3) * KP, pk4(cm2(Pc, E[4])));
    Pc = cm2(Pc, G);
  }
#pragma unroll
  for (int s = 0; s < 8; ++s) T1 = mf16(ldsr<u32x4>(rfr + 64 * s), Yf[s], T1);
#pragma unroll
  for (int g = 0; g < 8; ++g) { ldsw(wrow + (4 * (g >> 1) + (g & 1)) * KP, pk4(Pc)); Pc = cm2(Pc, G); }
  ldsw(wrow + 2 * KP, pk4(Pc));
#pragma unroll
  for (int g = 0; g < 4; ++g) ldsw(wrow + (4 * g + 3) * KP, keep[g]);
#pragma unroll
  for (int s = 0; s < 8; ++s) T2 = mf16(ldsr<u32x4>(rfr + 64 * s), Yf[s], T2);
  ST(3);
  asm volatile("s_waitcnt vmcnt(0)" ::: "memory");
  ST(4);
  {
    const u32 ra = A_RAW + wv * 4096 + (8 * q + 4 * ((j & 3) >> 1) + (j >> 2)) * 16 + (j & 1) * 8;
    const u32 wa = A_UT + (j & 7) * UT_STR + (256 + 256 * wv + 8 * q + 4 * (j >> 3)) * 2;
#pragma unroll
    for (int i = 0; i < 8; ++i) {
      s16x4 v = __builtin_amdgcn_ds_read_tr16_b64_v4i16((LAS s16x4*)(size_t)(ra + i * 512));
      ldsw(wa + i * 64, v);
    }
  }
  const u32 c3 = kb + 3 * CPS;
  ldsw(c3 + lane * 16, u32x4{0u, 0u, 0u, 0u}); ldsw(c3 + 1024 + lane * 16, u32x4{0u, 0u, 0u, 0u});
  {
    const int l0 = 128 * (q >> 1) + 4 * (q & 1) + 8 * j;
    ldsw(c3 + (544 - l0) * 2, u32x2{pack2(T0[3], T0[2]), pack2(T0[1], T0[0])});
    ldsw(c3 + (288 - l0) * 2, u32x2{pack2(T2[3], T1[1]), pack2(T1[0], T1[0])});
    if (lane == 63) ldsw(c3 + 64, u32x2{pack2(0.f, T2[3]), pack2(T2[3], T2[3])});
    const int mu0 = 32 + 8 * q + (j >> 1), od = j & 1;
    u32 p2 = pack2(lane == 0 ? T2[0] : T1[2], T1[3]);
    u32 p3 = pack2(lane == 0 ? T2[2] : T2[0], T2[1]);
    const int ia = lane == 0 ? 647 + 204 - 63 : (od ? 647 + 68 - mu0 : 647 + 204 + 1 - mu0);
    ldsw(c3 + ia * 2, (u16)p2);
    ldsw(c3 + (647 + 136 * od - mu0) * 2, (u16)(p2 >> 16));
    const int i3 = (od ? 967 - 64 : 967 + 84 - 63) - 16 * q - (j >> 1);
    ldsw(c3 + (lane == 0 ? 967 + 84 - 127 : i3) * 2, (u16)p3);
    ldsw(c3 + (i3 - 8) * 2, (u16)(p3 >> 16));
    const u32 k1024 = __builtin_amdgcn_readfirstlane(p2) & 0xffffu;
#pragma unroll
    for (int i = 0; i < 4; ++i) afx[i] *= k1024;
#pragma unroll
    for (int h = 0; h < 2; ++h) {
      u32x4 x = ldsr<u32x4>(c3 + h * 1024 + lane * 16); u32x2 y = ldsr<u32x2>(c3 + h * 1024 + lane * 16 + 16);
      u32 a0 = __builtin_amdgcn_alignbit(x.y, x.x, 16), a1 = __builtin_amdgcn_alignbit(x.z, x.y, 16), a2 = __builtin_amdgcn_alignbit(x.w, x.z, 16),
          a3 = __builtin_amdgcn_alignbit(y.x, x.w, 16), a4 = __builtin_amdgcn_alignbit(y.y, y.x, 16);
      ldsw(kb + 2 * CPS + h * 1024 + lane * 16, u32x4{a0, a1, a2, a3});
      ldsw(kb + 1 * CPS + h * 1024 + lane * 16, u32x4{x.y, x.z, x.w, y.x});
      ldsw(kb + h * 1024 + lane * 16, u32x4{a1, a2, a3, a4});
    }
  }
  u32x4 Af[17], t2[3][2], t3[4];
  {
    const u32 fb = kb + (j & 3) * CPS - 8 * (j >> 2);
    const u32 fa = fb + 16 * q + (544 - 16 - 512) * 2;
#pragma unroll
    for (int n = 0; n < 17; ++n) Af[n] = rd2(fa + (16 - n) * 64, fa + (16 - n) * 64 + 8);
    const u32 ft = fb + 8 * q;
#pragma unroll
    for (int D = 2; D < 5; ++D) { u32 a = ft + (644 - 16 * D) * 2; t2[D - 2][0] = rd2(a, a + 136); t2[D - 2][1] = rd2(a + 272, a + 408); }
#pragma unroll
    for (int D = 4; D < 8; ++D) { u32 a = ft + (964 - 16 * D) * 2; t3[D - 4] = rd2(a, a + 168); }
  }
  ST(5);
  bar();
  ST(6);
  f32x4 acc[8];
#pragma unroll
  for (int i = 0; i < 8; ++i) acc[i] = f32x4{0.f, 0.f, 0.f, 0.f};
  const f32x4 zz = {0.f, 0.f, 0.f, 0.f};
  const u32 ub = ut + (16 * j + 8 * q) * 2;
  const u32 yb = ut + (16 * j + 4 * q) * 2;
#define BADDR(k) (ub + (256 + 256 * ((k) >> 3) - 16 - 32 * ((k) & 7)) * 2)
  constexpr int NB = 4;
  u32x4 Bf[NB], W1;
#pragma unroll
  for (int k = 0; k < NB - 1; ++k) Bf[k] = ldsr<u32x4>(BADDR(k));
  W1 = ldsr<u32x4>(ub + (256 - 24) * 2);
  f32x4 V[6]; u32x4 vp[3]; u32x2 us;
#pragma unroll
  for (int e = 0; e < 8; ++e) {
#pragma unroll
    for (int b = 0; b < 8; ++b) {
      const int k = 8 * e + b;
      if (k + NB - 1 < 64) Bf[(k + NB - 1) % NB] = ldsr<u32x4>(BADDR(k + NB - 1));
      if (b == 5) { us = ldsr<u32x2>(yb + (256 + 256 * e) * 2); }
      if (b == 6 && e < 5) W1 = ldsr<u32x4>(ub + (256 + 256 * (e + 1) - 24) * 2);
      const u32x4 B = Bf[k % NB];
      acc[e] = mf16(Af[b], B, acc[e]);
      if (e < 7) acc[e + 1] = mf16(Af[8 + b], B, acc[e + 1]);
      if (b == 0) {
        if (e < 6) { V[0] = mf16(B, bnd[0], zz); V[1] = mf16(B, bnd[1], zz); V[2] = mf16(B, bnd[2], zz); V[3] = mf16(W1, bnd[3], zz); }
        if (e < 4) { V[4] = mf16(B, bnd[4], zz); V[5] = mf16(W1, bnd[5], zz); }
        if (e < 6) acc[e + 2] = mf16(Af[16], B, acc[e + 2]);
        if (e < 4) acc[e + 4] = mf16(afx, B, acc[e + 4]);
        if (e > 0) {
          f32x2 u01 = up2(us.x), u23 = up2(us.y);
          float o0 = rbf(acc[e - 1][0]) + rbf(u01.x * dsk), o1 = rbf(acc[e - 1][1]) + rbf(u01.y * dsk);
          float o2 = rbf(acc[e - 1][2]) + rbf(u23.x * dsk), o3 = rbf(acc[e - 1][3]) + rbf(u23.y * dsk);
          ldsw(yb + (256 * (e - 1)) * 2, u32x2{pack2(o0, o1), pack2(o2, o3)});
        }
      }
      if (b == 1 && e < 6) {
#pragma unroll
        for (int p = 0; p < 3; ++p) if (p < 2 || e < 4)
          vp[p] = u32x4{pack2(V[2 * p][0], V[2 * p][1]), pack2(V[2 * p][2], V[2 * p][3]), pack2(V[2 * p + 1][0], V[2 * p + 1][1]), pack2(V[2 * p + 1][2], V[2 * p + 1][3])};
      }
      if (b >= 2 && b <= 4 && e + b < 8) { acc[e + b] = mf16(vp[0], t2[b - 2][0], acc[e + b]); acc[e + b] = mf16(vp[1], t2[b - 2][1], acc[e + b]); }
      if (b >= 4 && e + b < 8) acc[e + b] = mf16(vp[2], t3[b - 4], acc[e + b]);
      __builtin_amdgcn_sched_barrier(0);
    }
  }
  {
    f32x2 u01 = up2(us.x), u23 = up2(us.y);
    float o0 = rbf(acc[7][0]) + rbf(u01.x * dsk), o1 = rbf(acc[7][1]) + rbf(u01.y * dsk);
    float o2 = rbf(acc[7][2]) + rbf(u23.x * dsk), o3 = rbf(acc[7][3]) + rbf(u23.y * dsk);
    ldsw(yb + (256 * 7) * 2, u32x2{pack2(o0, o1), pack2(o2, o3)});
  }
  ST(7);
  bar();
  ST(8);
  {
    const u32 ra = A_UT + (4 * (q & 1) + (j >> 2)) * UT_STR + (256 * wv + 16 * (q >> 1) + 4 * (j & 3)) * 2;
    __amdgpu_buffer_rsrc_t orr = __builtin_amdgcn_make_buffer_rsrc(P.out, 0, 2048 * 1152 * 4, 0x00020000);
    const u32 ov = ((256 * wv + 16 * (q >> 1) + j) * 1152 + d0 + 4 * (q & 1)) * 4;
#pragma unroll
    for (int i = 0; i < 8; ++i) {
      u32x2 v = __builtin_bit_cast(u32x2, __builtin_amdgcn_ds_read_tr16_b64_v4i16((LAS s16x4*)(size_t)(ra + i * 64)));
      __builtin_amdgcn_raw_buffer_store_b128(u32x4{v.x << 16, v.x & 0xffff0000u, v.y << 16, v.y & 0xffff0000u}, orr, ov, i * 32 * 4608, OAUXA);
    }
  }
  ST(9);
  STAMP_FLUSH(P);
}

__global__ __launch_bounds__(512, 2) void fused(Params P) {
  const int b = blockIdx.x, xcd = b & 7, slot = b >> 3;
  if (slot < 18) g_role(P, 2 * xcd + (slot >= 9), slot >= 9 ? slot - 9 : slot);
  if (slot >= 18 && slot < 27) a_role(P, 72 * xcd + 8 * (slot - 18));
}

extern "C" void kernel_launch(void* const* d_in, const int* in_sizes, int n_in, void* d_out, int out_size, void* d_ws, size_t ws_size, hipStream_t stream) {
  Params P;
  for (int i = 0; i < 18; ++i) P.in[i] = (const u16*)d_in[i];
  P.out = (float*)d_out; P.ws = (char*)d_ws;
  (void)hipFuncSetAttribute((const void*)fused, hipFuncAttributeMaxDynamicSharedMemorySize, LDS_BYTES);
  fused<<<256, 512, LDS_BYTES, stream>>>(P);
}
```

```cpp
#define OAUX 16
#define OAUXA 0
#include <hip/hip_runtime.h>
#include <stdint.h>

typedef unsigned short u16; typedef unsigned u32; typedef unsigned long long u64;
typedef __bf16 bf16x8 __attribute__((ext_vector_type(8)));
typedef __bf16 bf16x2 __attribute__((ext_vector_type(2)));
typedef short s16x4 __attribute__((ext_vector_type(4)));
typedef float f32x16 __attribute__((ext_vector_type(16)));
typedef float f32x4 __attribute__((ext_vector_type(4)));
typedef float f32x2 __attribute__((ext_vector_type(2)));
typedef u32 u32x4 __attribute__((ext_vector_type(4)));
typedef u32 u32x2 __attribute__((ext_vector_type(2)));
#define LAS __attribute__((address_space(3)))
#define GAS __attribute__((address_space(1)))
#define DEV __device__ __forceinline__

struct Params { const u16* in[18]; float* out; char* ws; };

constexpr u32 WS_EPOCH = 0, WS_FLAG = 4096, WS_GRAN = 65536, WS_H = 262144;
constexpr u32 H_BYTES = 16u * 4u * 72u * 1024u;
constexpr u32 LDS_BYTES = 163840, SCR = 147456;
constexpr int SPIN_MAX = 1 << 16;

#define STAMP_DECL
#define ST(i)
#define STAMP_FLUSH(P)

DEV float bf2f(u16 v) { return __uint_as_float(((u32)v) << 16); }
DEV u32 pack2(float a, float b) { bf16x2 r; r[0] = (__bf16)a; r[1] = (__bf16)b; return __builtin_bit_cast(u32, r); }
DEV float rbf(float f) { return (float)(__bf16)f; }
DEV void bar() { asm volatile("s_waitcnt lgkmcnt(0)\n\ts_barrier" ::: "memory"); }
DEV u32 mix32(u32 x) { x *= 0x9E3779B1u; x ^= x >> 15; x *= 0x85EBCA77u; x ^= x >> 13; return x + 0x632BE5ABu; }
DEV f32x2 cmul(f32x2 a, f32x2 b) { return f32x2{a.x * b.x - a.y * b.y, a.x * b.y + a.y * b.x}; }
DEV f32x2 cfma(f32x2 a, f32x2 b, f32x2 c) { return f32x2{fmaf(a.x, b.x, fmaf(-a.y, b.y, c.x)), fmaf(a.x, b.y, fmaf(a.y, b.x, c.y))}; }
DEV f32x2 cexp(float re, float im) {
  float e = __builtin_amdgcn_exp2f(re * 1.44269504088896341f);
  float r = __builtin_amdgcn_fractf(im * 0.15915494309189535f);
  return f32x2{e * __builtin_amdgcn_cosf(r), e * __builtin_amdgcn_sinf(r)};
}
DEV void glds4(const char* gb, u32 ldsb, u32 v0, u32 v1, u32 v2, u32 v3) {
  u32 keep;
  asm volatile("s_mov_b32 %0, m0\n\ts_mov_b32 m0, %2\n\ts_nop 0\n\t"
               "global_load_lds_dwordx4 %3, %1\n\tglobal_load_lds_dwordx4 %4, %1 offset:1024\n\t"
               "global_load_lds_dwordx4 %5, %1 offset:2048\n\tglobal_load_lds_dwordx4 %6, %1 offset:3072\n\ts_mov_b32 m0, %0"
               : "=&s"(keep) : "s"(gb), "s"(ldsb), "v"(v0), "v"(v1), "v"(v2), "v"(v3) : "memory");
}
template <class T> DEV T ldsr(u32 a) { return *(const LAS T*)(size_t)a; }
template <class T> DEV void ldsw(u32 a, T v) { *(LAS T*)(size_t)a = v; }
DEV f32x2 pr(const f32x16& a, int m) { return f32x2{a[2 * m], a[2 * m + 1]}; }

#define RA 1
DEV void swap16(float x, float& lo, float& hi) { auto r = __builtin_amdgcn_permlane16_swap(__float_as_uint(x), __float_as_uint(x), false, false); lo = __uint_as_float(r[0]); hi = __uint_as_float(r[1]); }
DEV void swap32(float x, float& lo, float& hi) { auto r = __builtin_amdgcn_permlane32_swap(__float_as_uint(x), __float_as_uint(x), false, false); lo = __uint_as_float(r[0]); hi = __uint_as_float(r[1]); }
DEV int wrap9(int v) { return v >= 9 ? v - 9 : v; }

DEV void g_role(const Params& P, int c, int j) {
  const int lane = threadIdx.x & 63, wv = __builtin_amdgcn_readfirstlane(threadIdx.x >> 6);
  STAMP_DECL
  ST(0);
  const u64 ep = *(const __attribute__((address_space(4))) u64*)(P.ws + WS_EPOCH);
  const int rot = wrap9(j + 4 * (c & 1));
  constexpr int NSL = RA + 2;
  if (wv >= 4) {
    const int w = wv - 4;
    const int l31 = lane & 31, hh = lane >> 5;
    u32 vx[4], vb[4];
#pragma unroll
    for (int q = 0; q < 4; ++q) {
      int i = 2 * w + (q >> 1), qq = 2 * (q & 1) + (lane >> 5), r = (lane >> 3) & 3;
      int time = 32 * qq + 16 * (r & 1) + 2 * i + (r >> 1);
      u32 ch = (u32)(((lane & 7) ^ ((4 * (q & 1) + (lane >> 4)) & 7)) << 4) + 3072 - 1024 * q;
      vx[q] = (128 * c + time) * 1152 + ch;
      vb[q] = (64 * j + 32 * (w & 1) + 8 * q + (lane >> 3)) * 1152 + ch;
    }
    u32 dk0, dk1;
    asm volatile("global_load_ushort %0, %2, %3\n\tglobal_load_ushort %1, %2, %3 offset:64" : "=&v"(dk0), "=&v"(dk1) : "v"((64 * j + l31) * 2), "s"(P.in[17]) : "memory");
    const char* xb = (const char*)P.in[9] - 3072;
    const char* bb = (const char*)(w < 2 ? P.in[12] : P.in[13]) - 3072;
    const char* cb = (const char*)(w < 2 ? P.in[14] : P.in[15]) - 3072;
#pragma unroll
    for (int s = 0; s <= RA; ++s) {
      u32 sl = s * 32768 + w * 4096; int ks = wrap9(rot + s);
      glds4(xb + ks * 128, sl, vx[0], vx[1], vx[2], vx[3]);
      glds4(bb + ks * 128, sl + 16384, vb[0], vb[1], vb[2], vb[3]);
    }
    u32 tlo = mix32((u32)ep), thi = mix32((u32)(ep >> 32) ^ 0x5bd1e995u);
    asm volatile("" :: "s"(tlo), "s"(thi));
    ST(1);
    s16x4 xs[8];
    asm volatile("s_waitcnt vmcnt(%2)" : "+v"(dk0), "+v"(dk1) : "n"(8 * RA) : "memory");
    const float dsk0 = __uint_as_float(dk0 << 16), dsk1 = __uint_as_float(dk1 << 16);
    int slot_cur = 0, slot_new = RA + 1 == NSL ? 0 : RA + 1, kn = wrap9(rot + RA + 1);
    int cpc = 0;
#pragma unroll 1
    for (int s = 0; s < 9; ++s) {
      bar();
      u32 sl = slot_new * 32768 + w * 4096;
      if (s + RA + 1 <= 8) {
        glds4(xb + kn * 128, sl, vx[0], vx[1], vx[2], vx[3]);
        glds4(bb + kn * 128, sl + 16384, vb[0], vb[1], vb[2], vb[3]);
      } else {
        glds4(cb + slot_new * 256, sl, vb[0], vb[1], vb[2], vb[3]);
        glds4(cb + slot_new * 256 + 128, sl + 16384, vb[0], vb[1], vb[2], vb[3]);
      }
      if (wrap9(rot + s) == j) {
        const int e = (lane >> 2) & 3, pp = lane & 3;
        u32 sb = slot_cur * 32768 + (32 * hh + 4 * w + 16 * (e >> 1) + 2 * (e & 1)) * 128 + (((2 * ((lane >> 4) & 1) + (pp >> 1)) ^ ((w << 1) | (e & 1))) << 4) + (pp & 1) * 8;
#pragma unroll
        for (int g = 0; g < 4; ++g) {
          xs[g] = __builtin_amdgcn_ds_read_tr16_b64_v4i16((LAS s16x4*)(size_t)(sb + (64 * (g & 1) + (g >> 1)) * 128));
          xs[4 + g] = __builtin_amdgcn_ds_read_tr16_b64_v4i16((LAS s16x4*)(size_t)((sb ^ 64) + (64 * (g & 1) + (g >> 1)) * 128));
        }
      }
      slot_cur = slot_cur + 1 == NSL ? 0 : slot_cur + 1; slot_new = slot_new + 1 == NSL ? 0 : slot_new + 1; kn = wrap9(kn + 1);
      asm volatile("s_waitcnt vmcnt(%0)" :: "n"(8 * RA) : "memory");
    }
    bar();
    ST(3);
    for (int q = 2 * NSL; q < 9; ++q) glds4(cb + q * 128, q * 16384 + w * 4096, vb[0], vb[1], vb[2], vb[3]);
    glds4(cb + (8 % NSL) * 256, (8 % NSL) * 32768 + w * 4096, vb[0], vb[1], vb[2], vb[3]);
    glds4(cb + (8 % NSL) * 256 + 128, (8 % NSL) * 32768 + 16384 + w * 4096, vb[0], vb[1], vb[2], vb[3]);
    f32x16 acc0, acc1;
#pragma unroll
    for (int r = 0; r < 16; ++r) {
      acc0[r] = rbf(bf2f((u16)xs[r >> 2][r & 3]) * dsk0);
      acc1[r] = rbf(bf2f((u16)xs[4 + (r >> 2)][r & 3]) * dsk1);
    }
    asm volatile("" : "+v"(acc0), "+v"(acc1));
    asm volatile("s_waitcnt vmcnt(0)" ::: "memory");
    ST(1);
    bar();
    ST(4);
    {
      const u64 tag = ((u64)thi << 32) | tlo;
      GAS u64* fp = (GAS u64*)(P.ws + WS_FLAG + (c * 9 + (lane < 9 ? lane : 0)) * 256);
      for (int it = 0; it < SPIN_MAX; ++it) {
        u64 v = __hip_atomic_load(fp, __ATOMIC_RELAXED, __HIP_MEMORY_SCOPE_AGENT);
        if (__all(v == tag)) break;
        __builtin_amdgcn_s_sleep(1);
      }
      asm volatile("" ::: "memory");
    }
    ST(5);
    if (c == 15 && j == 0 && w == 0 && lane == 0)
      __hip_atomic_store((GAS u64*)(P.ws + WS_EPOCH), ((u64)((u32)(ep >> 32) + 1u) << 32) | (u32)((u32)ep + 1u), __ATOMIC_RELAXED, __HIP_MEMORY_SCOPE_AGENT);
    __amdgpu_buffer_rsrc_t hr = __builtin_amdgcn_make_buffer_rsrc(P.ws + WS_H, 0, H_BYTES, 0x00020000);
    const u32 hs = (c * 4 + w) * 72 * 1024;
    const int r2 = j;
    int pcs[9];
#pragma unroll
    for (int q = 0; q < 9; ++q) pcs[q] = wrap9(r2 + q);
    u32 aoff[4];
#pragma unroll
    for (int s = 0; s < 4; ++s) aoff[s] = l31 * 128 + (((2 * s + hh) ^ ((l31 >> 1) & 7)) << 4);
#define PFD 12
    constexpr int PF = PFD;
    u32x4 hf[PF];
#define HLOAD(k) __builtin_amdgcn_raw_buffer_load_b128(hr, lane * 16 + ((k) & 3) * 1024, hs + pcs[(k) >> 3] * 8192 + (((k) >> 2) & 1) * 4096, 16)
#pragma unroll
    for (int k = 0; k < PF; ++k) hf[k] = HLOAD(k);
    bf16x8 cf[2][4];
#define CREAD(set, k) do { _Pragma("unroll") for (int u = 0; u < 2; ++u) { u32 ca = pcs[((k) + u) >> 3] * 16384 + aoff[((k) + u) & 3]; u32 cbase = ((((k) + u) >> 2) & 1) * 8192; \
      cf[set][2 * u] = ldsr<bf16x8>(ca + cbase); cf[set][2 * u + 1] = ldsr<bf16x8>(ca + cbase + 4096); } } while (0)
    CREAD(0, 0);
    __builtin_amdgcn_sched_barrier(0);
#pragma unroll
    for (int k = 0; k < 72; k += 2) {
      const int set = (k >> 1) & 1;
      if (k + 2 < 72) CREAD(set ^ 1, k + 2);
      __builtin_amdgcn_sched_barrier(0);
#pragma unroll
      for (int u = 0; u < 2; ++u) {
        bf16x8 a = __builtin_bit_cast(bf16x8, hf[(k + u) % PF]);
        acc0 = __builtin_amdgcn_mfma_f32_32x32x16_bf16(a, cf[set][2 * u], acc0, 0, 0, 0);
        acc1 = __builtin_amdgcn_mfma_f32_32x32x16_bf16(a, cf[set][2 * u + 1], acc1, 0, 0, 0);
        if (k + u + PF < 72) hf[(k + u) % PF] = HLOAD(k + u + PF);
      }
      asm volatile("" : "+v"(acc0), "+v"(acc1));
      __builtin_amdgcn_sched_barrier(0);
    }
    ST(6);
    __amdgpu_buffer_rsrc_t orr = __builtin_amdgcn_make_buffer_rsrc(P.out, 0, 2048 * 1152 * 4, 0x00020000);
    const u32 ov = ((128 * c + 32 * w + 4 * hh) * 1152 + 576 + 64 * j + l31) * 4;
#pragma unroll
    for (int r = 0; r < 16; ++r) {
      __builtin_amdgcn_raw_buffer_store_b32(__float_as_uint(acc0[r]), orr, ov, (8 * (r >> 2) + (r & 3)) * 4608, OAUX);
      __builtin_amdgcn_raw_buffer_store_b32(__float_as_uint(acc1[r]), orr, ov + 128, (8 * (r >> 2) + (r & 3)) * 4608, OAUX);
    }
    ST(7);
  } else {
    const int l15 = lane & 15, q4 = lane >> 4;
    const int p = 64 * j + 16 * wv + l15;
    float dt = rbf(__builtin_amdgcn_exp2f(bf2f(P.in[16][p]) * 1.44269504088896341f));
    float Lr = bf2f(P.in[10][p]), Li = bf2f(P.in[11][p]);
    f32x2 lam = cexp(dt * Lr, dt * Li);
    float inv = 2.f / (Lr * Lr + Li * Li);
    f32x2 coef = cmul(f32x2{lam.x - 1.f, lam.y}, f32x2{Lr * inv, -Li * inv});
    f32x2 mu = cmul(lam, lam); mu = cmul(mu, mu); mu = cmul(mu, mu); mu = cmul(mu, mu);
    f32x2 nu = cmul(mu, mu), nu2 = cmul(nu, nu), La = cmul(nu2, nu2);
    f32x2 La2 = cmul(La, La), La4 = cmul(La2, La2);
    f32x2 one = {1.f, 0.f};
    f32x2 Lg = cmul((q4 & 2) ? La2 : one, (q4 & 1) ? La : one);
    f32x2 nuq = (q4 & 1) ? nu : one;
    f32x2 cm0 = cmul(coef, cmul((q4 & 2) ? nu2 : one, nuq)), cm1 = cmul(cm0, mu);
    u32 tlo = mix32((u32)ep), thi = mix32((u32)(ep >> 32) ^ 0x5bd1e995u);
    asm volatile("" :: "s"(tlo), "s"(thi));
    asm volatile("" : "+v"(lam), "+v"(coef), "+v"(mu), "+v"(La4), "+v"(Lg), "+v"(cm0), "+v"(cm1), "+v"(nu), "+v"(nu2), "+v"(nuq));
    f32x4 ar[8], ai[8];
#pragma unroll
    for (int i = 0; i < 8; ++i) { ar[i] = f32x4{0.f, 0.f, 0.f, 0.f}; ai[i] = ar[i]; }
    ST(1);
    u32 xa0 = l15 * 128 + ((q4 ^ (l15 >> 1)) << 4), xa1 = xa0 ^ 64, ba0 = xa0 + 16384 + wv * 2048, ba1 = ba0 ^ 64;
    bf16x8 A[2][8], Bq[2][2];
#define STEP(cs, xa, ba) do { Bq[cs ^ 1][0] = ldsr<bf16x8>(ba); Bq[cs ^ 1][1] = ldsr<bf16x8>(ba + 8192); \
      _Pragma("unroll") for (int i = 0; i < 8; ++i) { A[cs ^ 1][i] = ldsr<bf16x8>(xa + i * 2048); \
        ar[i] = __builtin_amdgcn_mfma_f32_16x16x32_bf16(A[cs][i], Bq[cs][0], ar[i], 0, 0, 0); ai[i] = __builtin_amdgcn_mfma_f32_16x16x32_bf16(A[cs][i], Bq[cs][1], ai[i], 0, 0, 0); \
        asm volatile("" : "+v"(ar[i]), "+v"(ai[i])); __builtin_amdgcn_sched_barrier(0); } } while (0)
    bar();
    ST(2);
    Bq[0][0] = ldsr<bf16x8>(ba0); Bq[0][1] = ldsr<bf16x8>(ba0 + 8192);
#pragma unroll
    for (int i = 0; i < 8; ++i) A[0][i] = ldsr<bf16x8>(xa0 + i * 2048);
    __builtin_amdgcn_sched_barrier(0);
    int slot = 0;
#pragma unroll 1
    for (int s = 0; s < 9; ++s) {
      STEP(0, xa1, ba1);
      const u32 dl = slot + 1 == NSL ? (u32)(-(NSL - 1) * 32768) : 32768u;
      slot = slot + 1 == NSL ? 0 : slot + 1;
      xa0 += dl; xa1 += dl; ba0 += dl; ba1 += dl;
      bar();
      STEP(1, xa0, ba0);
    }
    ST(3);
    const f32x2 lr2 = {lam.x, lam.x}, li2 = {lam.y, lam.y};
    f32x2 Br[16], Bi[16];
#pragma unroll
    for (int i = 0; i < 8; ++i) { Br[2 * i] = f32x2{ar[i][0], ar[i][1]}; Br[2 * i + 1] = f32x2{ar[i][2], ar[i][3]}; Bi[2 * i] = f32x2{ai[i][0], ai[i][1]}; Bi[2 * i + 1] = f32x2{ai[i][2], ai[i][3]}; }
    f32x2 sr = Br[0], si = Bi[0];
#pragma unroll
    for (int k = 1; k < 16; ++k) {
      f32x2 t1 = __builtin_elementwise_fma(lr2, sr, Br[k]), t2 = __builtin_elementwise_fma(lr2, si, Bi[k]);
      f32x2 n1 = __builtin_elementwise_fma(-li2, si, t1), n2 = __builtin_elementwise_fma(li2, sr, t2);
      sr = n1; si = n2;
    }
    const f32x2 S0 = {sr.x, si.x};
    f32x2 T = cfma(mu, S0, f32x2{sr.y, si.y}), lo, hi;
    { float a, b; swap16(T.x, a, b); lo.x = a; hi.x = b; swap16(T.y, a, b); lo.y = a; hi.y = b; }
    f32x2 U = cfma(nu, lo, hi), E = (q4 & 1) ? lo : f32x2{0.f, 0.f};
    { float a, b; swap32(U.x, a, b); lo.x = a; hi.x = b; swap32(U.y, a, b); lo.y = a; hi.y = b; }
    f32x2 tot = cfma(nu2, lo, hi);
    if (q4 == 0 && c < 15) {
      u32x4 gv = {__float_as_uint(tot.x), tlo, __float_as_uint(tot.y), thi};
      __amdgpu_buffer_rsrc_t gr = __builtin_amdgcn_make_buffer_rsrc(P.ws + WS_GRAN, 0, 9 * 16 * 1024, 0x00020000);
      __builtin_amdgcn_raw_buffer_store_b128(gv, gr, l15 * 16, (j * 16 + c) * 1024 + wv * 256, 16);
    }
    ST(4);
    if (q4 & 2) E = cfma(nuq, lo, E);
    f32x2 La_ = cmul(coef, E), Lb_ = cmul(coef, cfma(mu, E, S0));
    const f32x2 cr2 = {coef.x, coef.x}, ci2 = {coef.y, coef.y};
#pragma unroll
    for (int k = 0; k < 16; ++k) {
      f32x2 n1 = __builtin_elementwise_fma(-ci2, Bi[k], cr2 * Br[k]), n2 = __builtin_elementwise_fma(ci2, Br[k], cr2 * Bi[k]);
      Br[k] = n1; Bi[k] = n2;
    }
#pragma unroll
    for (int k = 0; k < 16; ++k) asm volatile("" : "+v"(Br[k]), "+v"(Bi[k]));
    asm volatile("" : "+v"(La_), "+v"(Lb_));
    ST(8);
    f32x2 H = {0.f, 0.f};
    if (c > 0) {
      const char* gb = P.ws + WS_GRAN + j * 16384 + wv * 256;
      u32 go[4]; bool valid[4];
#pragma unroll
      for (int n = 0; n < 4; ++n) { int cs = c - 1 - q4 - 4 * n; valid[n] = cs >= 0; go[n] = (cs < 0 ? 0 : cs) * 1024 + l15 * 16; }
      u32x4 g0, g1, g2, g3;
      for (int it = 0; it < SPIN_MAX; ++it) {
        asm volatile("global_load_dwordx4 %0, %4, %8 sc1\n\tglobal_load_dwordx4 %1, %5, %8 sc1\n\tglobal_load_dwordx4 %2, %6, %8 sc1\n\tglobal_load_dwordx4 %3, %7, %8 sc1\n\ts_waitcnt vmcnt(0)"
                     : "=&v"(g0), "=&v"(g1), "=&v"(g2), "=&v"(g3) : "v"(go[0]), "v"(go[1]), "v"(go[2]), "v"(go[3]), "s"(gb) : "memory");
        bool ok = (!valid[0] || (g0.y == tlo && g0.w == thi)) && (!valid[1] || (g1.y == tlo && g1.w == thi)) &&
                  (!valid[2] || (g2.y == tlo && g2.w == thi)) && (!valid[3] || (g3.y == tlo && g3.w == thi));
        if (__all(ok)) break;
        __builtin_amdgcn_s_sleep(1);
      }
      f32x2 z = {0.f, 0.f};
      f32x2 s3 = valid[3] ? f32x2{__uint_as_float(g3.x), __uint_as_float(g3.z)} : z;
      f32x2 s2 = valid[2] ? f32x2{__uint_as_float(g2.x), __uint_as_float(g2.z)} : z;
      f32x2 s1 = valid[1] ? f32x2{__uint_as_float(g1.x), __uint_as_float(g1.z)} : z;
      f32x2 s0 = valid[0] ? f32x2{__uint_as_float(g0.x), __uint_as_float(g0.z)} : z;
      H = cmul(Lg, cfma(La4, cfma(La4, cfma(La4, s3, s2), s1), s0));
      float a, b;
      swap16(H.x, a, b); H.x = a + b; swap16(H.y, a, b); H.y = a + b;
      swap32(H.x, a, b); H.x = a + b; swap32(H.y, a, b); H.y = a + b;
    }
    ST(5);
    f32x2 sa = cfma(cm0, H, La_), sb_ = cfma(cm1, H, Lb_);
    sr = f32x2{sa.x, sb_.x}; si = f32x2{sa.y, sb_.y};
#pragma unroll
    for (int k = 0; k < 16; ++k) {
      f32x2 t1 = __builtin_elementwise_fma(lr2, sr, Br[k]), t2 = __builtin_elementwise_fma(lr2, si, Bi[k]);
      f32x2 n1 = __builtin_elementwise_fma(-li2, si, t1), n2 = __builtin_elementwise_fma(li2, sr, t2);
      sr = n1; si = n2; Br[k] = sr; Bi[k] = si;
    }
    ST(10);
    __amdgpu_buffer_rsrc_t hr = __builtin_amdgcn_make_buffer_rsrc(P.ws + WS_H, 0, H_BYTES, 0x00020000);
    const u32 my = SCR + wv * 4096;
    const u32 fsw = ((l15 & 3) << 2) | ((l15 >> 2) & 1);
    const u32 wa = my + l15 * 256;
    u32 ra[2];
#pragma unroll
    for (int u = 0; u < 2; ++u) {
      int row = 8 * (q4 >> 1) + 4 * u + (l15 >> 2);
      u32 fs = ((row & 3) << 2) | ((row >> 2) & 1);
      ra[u] = my + row * 256 + (((2 * (q4 & 1) + ((l15 & 3) >> 1)) ^ fs) << 4) + (l15 & 1) * 8;
    }
#pragma unroll
    for (int part = 0; part < 2; ++part) {
#pragma unroll
      for (int e0 = 0; e0 < 2; ++e0)
#pragma unroll
        for (int hf = 0; hf < 2; ++hf) {
          u32x4 v;
#pragma unroll
          for (int q = 0; q < 4; ++q) {
            int k = 8 * hf + 2 * q;
            v[q] = part ? pack2(-Bi[k][e0], -Bi[k + 1][e0]) : pack2(Br[k][e0], Br[k + 1][e0]);
          }
          ldsw(wa + (((4 * q4 + 2 * e0 + hf) ^ fsw) << 4), v);
        }
#pragma unroll
      for (int rt = 0; rt < 4; ++rt) {
        s16x4 lo4 = __builtin_amdgcn_ds_read_tr16_b64_v4i16((LAS s16x4*)(size_t)(ra[0] ^ (rt * 64)));
        s16x4 hi4 = __builtin_amdgcn_ds_read_tr16_b64_v4i16((LAS s16x4*)(size_t)(ra[1] ^ (rt * 64)));
        u32x2 l2 = __builtin_bit_cast(u32x2, lo4), h2 = __builtin_bit_cast(u32x2, hi4);
        u32x4 v = {l2.x, l2.y, h2.x, h2.y};
        __builtin_amdgcn_raw_buffer_store_b128(v, hr, lane * 16, ((c * 4 + rt) * 72 + j * 8 + part * 4 + wv) * 1024, 16);
      }
    }
    ST(6);
    asm volatile("s_waitcnt vmcnt(0)" ::: "memory");
    ST(9);
    bar();
    if (wv == 0 && lane == 0)
      __hip_atomic_store((GAS u64*)(P.ws + WS_FLAG + (c * 9 + j) * 256), ((u64)thi << 32) | tlo, __ATOMIC_RELAXED, __HIP_MEMORY_SCOPE_AGENT);
    ST(7);
  }
  STAMP_FLUSH(P);
}

struct C2 { f32x2 r, i; };
DEV C2 cm2(const C2& a, const C2& b) {
  C2 o; o.r = __builtin_elementwise_fma(-a.i, b.i, a.r * b.r); o.i = __builtin_elementwise_fma(a.i, b.r, a.r * b.i); return o;
}
DEV C2 cexp2(f32x2 re, f32x2 im) { f32x2 a = cexp(re.x, im.x), b = cexp(re.y, im.y); C2 o; o.r = f32x2{a.x, b.x}; o.i = f32x2{a.y, b.y}; return o; }
DEV u32x2 pk4(const C2& a) { return u32x2{pack2(a.r.x, a.r.y), pack2(a.i.x, a.i.y)}; }
DEV f32x2 up2(u32 v) { return f32x2{__uint_as_float(v << 16), __uint_as_float(v & 0xffff0000u)}; }
DEV f32x4 mf16(u32x4 a, u32x4 b, f32x4 c) { return __builtin_amdgcn_mfma_f32_16x16x32_bf16(__builtin_bit_cast(bf16x8, a), __builtin_bit_cast(bf16x8, b), c, 0, 0, 0); }
DEV u32x4 bandop(int x, int lo, int hi, u32 mul) {
  u32 m = (u32)((((1ull << (hi - lo + 1)) - 1) << 32) >> (32 + hi - x)) & 0xffu;
  u32 sp = m | (m << 15);
  u32x4 o;
#pragma unroll
  for (int i = 0; i < 4; ++i) o[i] = ((sp >> (2 * i)) & 0x10001u) * mul;
  return o;
}
DEV u32x4 rd2(u32 a0, u32 a1) { u32x2 x = ldsr<u32x2>(a0), y = ldsr<u32x2>(a1); return u32x4{x.x, x.y, y.x, y.y}; }
constexpr u32 A_RAW = 0, A_UT = 32768, UT_STR = 4640, A_KB = A_UT + 8 * UT_STR, KB_STR = 8704, KP = 544, CPS = 2112;

DEV void a_role(const Params& P, int d0) {
  const int lane = threadIdx.x & 63, wv = __builtin_amdgcn_readfirstlane(threadIdx.x >> 6);
  const int d = d0 + wv, j = lane & 15, q = lane >> 4;
  STAMP_DECL
  ST(0);
  u32 pAr, pAi, pBr, pBi, pCr, pCi;
  {
    const u32 vo = d * 256 + lane * 4;
    asm volatile("global_load_dword %0, %6, %7\n\tglobal_load_dword %1, %6, %8\n\tglobal_load_dword %2, %6, %9\n\t"
                 "global_load_dword %3, %6, %10\n\tglobal_load_dword %4, %6, %11\n\tglobal_load_dword %5, %6, %12"
                 : "=&v"(pAr), "=&v"(pAi), "=&v"(pBr), "=&v"(pBi), "=&v"(pCr), "=&v"(pCi)
                 : "v"(vo), "s"(P.in[1]), "s"(P.in[2]), "s"(P.in[3]), "s"(P.in[4]), "s"(P.in[5]), "s"(P.in[6]) : "memory");
    const u32 uo = (256 * wv + lane) * 1152 + d0 * 2 + 3072;
    glds4((const char*)P.in[0] - 3072, A_RAW + wv * 4096, uo, uo + 64 * 1152 - 1024, uo + 128 * 1152 - 2048, uo + 192 * 1152 - 3072);
  }
  typedef const __attribute__((address_space(4))) u32* cptr;
  const u32 sdt = *(cptr)((const char*)P.in[7] + ((d * 2) & ~3)), sds = *(cptr)((const char*)P.in[8] + ((d * 2) & ~3));
  const float ldt = __uint_as_float((d & 1) ? (sdt & 0xffff0000u) : (sdt << 16));
  const float dsk = __uint_as_float((d & 1) ? (sds & 0xffff0000u) : (sds << 16));
  const float dt = rbf(__builtin_amdgcn_exp2f(ldt * 1.44269504088896341f));
  u32x4 bnd[6];
  bnd[0] = bandop(j + 16 - 8 * q, 3, 5, 0x3f80); bnd[1] = bandop(j + 16 - 8 * q, 6, 10, 0x3f80); bnd[2] = bandop(j + 16 - 8 * q, 11, 13, 0x3f80);
  bnd[3] = bandop(j + 24 - 8 * q, 14, 18, 0x3f80); bnd[4] = bandop(j + 16 - 8 * q, 5, 11, 0x3f80); bnd[5] = bandop(j + 24 - 8 * q, 12, 20, 0x3f80);
  u32x4 afx = bandop(j + 16 - 8 * q, 3, 4, 1);
  const u32 ut = A_UT + wv * UT_STR, kb = A_KB + wv * KB_STR;
  ldsw(ut + lane * 8, u32x2{0u, 0u});
#pragma unroll
  for (int i = 0; i < 6; ++i) asm volatile("" : "+v"(bnd[i]));
  asm volatile("" : "+v"(afx));
  ST(1);
  asm volatile("s_waitcnt vmcnt(4)" : "+v"(pAr), "+v"(pAi), "+v"(pBr), "+v"(pBi), "+v"(pCr), "+v"(pCi) :: "memory");
  ST(2);
  const f32x2 dt2 = {dt, dt};
  const f32x2 are = dt2 * up2(pAr), aim = dt2 * up2(pAi);
  C2 W; { C2 B = {dt2 * up2(pBr), dt2 * up2(pBi)}, Cc = {up2(pCr), up2(pCi)}; W = cm2(Cc, B); W.i = -W.i; }
  const u32 wrow = kb + lane * 8, rfr = kb + j * KP + q * 16;
  u32x4 Yf[8];
  C2 E8 = cexp2(8.f * are, 8.f * aim);
  {
    C2 Y[16];
    Y[1] = E8;
    ldsw(wrow, u32x2{0x3f803f80u, 0u});
    ldsw(wrow + KP, pk4(Y[1]));
#pragma unroll
    for (int i = 2; i < 16; ++i) { Y[i] = (i & 1) ? cm2(Y[i - 1], Y[1]) : cm2(Y[i / 2], Y[i / 2]); ldsw(wrow + i * KP, pk4(Y[i])); }
#pragma unroll
    for (int s = 0; s < 8; ++s) Yf[s] = ldsr<u32x4>(rfr + 64 * s);
  }
  E8.i = -E8.i;
  C2 E[8];
  E[1] = cexp2(are, -aim);
  const C2 G = cexp2(128.f * are, -128.f * aim);
  E[2] = cm2(E[1], E[1]); E[3] = cm2(E[2], E[1]); E[4] = cm2(E[2], E[2]); E[5] = cm2(E[4], E[1]); E[6] = cm2(E[4], E[2]); E[7] = cm2(E[4], E[3]);
  f32x4 T0 = {0.f, 0.f, 0.f, 0.f}, T1 = T0, T2 = T0;
  C2 Pc = W;
#pragma unroll
  for (int h = 0; h < 2; ++h) {
    ldsw(wrow + (8 * h) * KP, pk4(Pc));
#pragma unroll
    for (int r = 1; r < 8; ++r) ldsw(wrow + (8 * h + r) * KP, pk4(cm2(Pc, E[r])));
    Pc = cm2(Pc, G);
  }
#pragma unroll
  for (int s = 0; s < 8; ++s) T0 = mf16(ldsr<u32x4>(rfr + 64 * s), Yf[s], T0);
  u32x2 keep[4];
#pragma unroll
  for (int h = 0; h < 2; ++h) {
    keep[2 * h] = pk4(cm2(Pc, E[4])); keep[2 * h + 1] = pk4(cm2(Pc, E8));
    ldsw(wrow + (8 * h) * KP, pk4(Pc)); ldsw(wrow + (8 * h + 1) * KP, pk4(cm2(Pc, E[2])));
    ldsw(wrow + (8 * h + 4) * KP, keep[2 * h]); ldsw(wrow + (8 * h + 5) * KP, pk4(cm2(Pc, E[6])));
    Pc = cm2(Pc, G);
  }
#pragma unroll
  for (int g = 0; g < 4; ++g) {
    ldsw(wrow + (4 * g + 2) * KP, pk4(Pc)); ldsw(wrow + (4 * g + 3) * KP, pk4(cm2(Pc, E[4])));
    Pc = cm2(Pc, G);
  }
#pragma unroll
  for (int s = 0; s < 8; ++s) T1 = mf16(ldsr<u32x4>(rfr + 64 * s), Yf[s], T1);
#pragma unroll
  for (int g = 0; g < 8; ++g) { ldsw(wrow + (4 * (g >> 1) + (g & 1)) * KP, pk4(Pc)); Pc = cm2(Pc, G); }
  ldsw(wrow + 2 * KP, pk4(Pc));
#pragma unroll
  for (int g = 0; g < 4; ++g) ldsw(wrow + (4 * g + 3) * KP, keep[g]);
#pragma unroll
  for (int s = 0; s < 8; ++s) T2 = mf16(ldsr<u32x4>(rfr + 64 * s), Yf[s], T2);
  ST(3);
  asm volatile("s_waitcnt vmcnt(0)" ::: "memory");
  ST(4);
  {
    const u32 ra = A_RAW + wv * 4096 + (8 * q + 4 * ((j & 3) >> 1) + (j >> 2)) * 16 + (j & 1) * 8;
    const u32 wa = A_UT + (j & 7) * UT_STR + (256 + 256 * wv + 8 * q + 4 * (j >> 3)) * 2;
#pragma unroll
    for (int i = 0; i < 8; ++i) {
      s16x4 v = __builtin_amdgcn_ds_read_tr16_b64_v4i16((LAS s16x4*)(size_t)(ra + i * 512));
      ldsw(wa + i * 64, v);
    }
  }
  const u32 c3 = kb + 3 * CPS;
  ldsw(c3 + lane * 16, u32x4{0u, 0u, 0u, 0u}); ldsw(c3 + 1024 + lane * 16, u32x4{0u, 0u, 0u, 0u});
  {
    const int l0 = 128 * (q >> 1) + 4 * (q & 1) + 8 * j;
    ldsw(c3 + (544 - l0) * 2, u32x2{pack2(T0[3], T0[2]), pack2(T0[1], T0[0])});
    ldsw(c3 + (288 - l0) * 2, u32x2{pack2(T2[3], T1[1]), pack2(T1[0], T1[0])});
    if (lane == 63) ldsw(c3 + 64, u32x2{pack2(0.f, T2[3]), pack2(T2[3], T2[3])});
    const int mu0 = 32 + 8 * q + (j >> 1), od = j & 1;
    u32 p2 = pack2(lane == 0 ? T2[0] : T1[2], T1[3]);
    u32 p3 = pack2(lane == 0 ? T2[2] : T2[0], T2[1]);
    const int ia = lane == 0 ? 647 + 204 - 63 : (od ? 647 + 68 - mu0 : 647 + 204 + 1 - mu0);
    ldsw(c3 + ia * 2, (u16)p2);
    ldsw(c3 + (647 + 136 * od - mu0) * 2, (u16)(p2 >> 16));
    const int i3 = (od ? 967 - 64 : 967 + 84 - 63) - 16 * q - (j >> 1);
    ldsw(c3 + (lane == 0 ? 967 + 84 - 127 : i3) * 2, (u16)p3);
    ldsw(c3 + (i3 - 8) * 2, (u16)(p3 >> 16));
    const u32 k1024 = __builtin_amdgcn_readfirstlane(p2) & 0xffffu;
#pragma unroll
    for (int i = 0; i < 4; ++i) afx[i] *= k1024;
#pragma unroll
    for (int h = 0; h < 2; ++h) {
      u32x4 x = ldsr<u32x4>(c3 + h * 1024 + lane * 16); u32x2 y = ldsr<u32x2>(c3 + h * 1024 + lane * 16 + 16);
      u32 a0 = __builtin_amdgcn_alignbit(x.y, x.x, 16), a1 = __builtin_amdgcn_alignbit(x.z, x.y, 16), a2 = __builtin_amdgcn_alignbit(x.w, x.z, 16),
          a3 = __builtin_amdgcn_alignbit(y.x, x.w, 16), a4 = __builtin_amdgcn_alignbit(y.y, y.x, 16);
      ldsw(kb + 2 * CPS + h * 1024 + lane * 16, u32x4{a0, a1, a2, a3});
      ldsw(kb + 1 * CPS + h * 1024 + lane * 16, u32x4{x.y, x.z, x.w, y.x});
      ldsw(kb + h * 1024 + lane * 16, u32x4{a1, a2, a3, a4});
    }
  }
  u32x4 Af[17], t2[3][2], t3[4];
  {
    const u32 fb = kb + (j & 3) * CPS - 8 * (j >> 2);
    const u32 fa = fb + 16 * q + (544 - 16 - 512) * 2;
#pragma unroll
    for (int n = 0; n < 17; ++n) Af[n] = rd2(fa + (16 - n) * 64, fa + (16 - n) * 64 + 8);
    const u32 ft = fb + 8 * q;
#pragma unroll
    for (int D = 2; D < 5; ++D) { u32 a = ft + (644 - 16 * D) * 2; t2[D - 2][0] = rd2(a, a + 136); t2[D - 2][1] = rd2(a + 272, a + 408); }
#pragma unroll
    for (int D = 4; D < 8; ++D) { u32 a = ft + (964 - 16 * D) * 2; t3[D - 4] = rd2(a, a + 168); }
  }
  ST(5);
  bar();
  ST(6);
  f32x4 acc[8];
#pragma unroll
  for (int i = 0; i < 8; ++i) acc[i] = f32x4{0.f, 0.f, 0.f, 0.f};
  const f32x4 zz = {0.f, 0.f, 0.f, 0.f};
  const u32 ub = ut + (16 * j + 8 * q) * 2;
  const u32 yb = ut + (16 * j + 4 * q) * 2;
#define BADDR(k) (ub + (256 + 256 * ((k) >> 3) - 16 - 32 * ((k) & 7)) * 2)
  constexpr int NB = 4;
  u32x4 Bf[NB], W1;
#pragma unroll
  for (int k = 0; k < NB - 1; ++k) Bf[k] = ldsr<u32x4>(BADDR(k));
  W1 = ldsr<u32x4>(ub + (256 - 24) * 2);
  f32x4 V[6]; u32x4 vp[3]; u32x2 us;
#pragma unroll
  for (int e = 0; e < 8; ++e) {
#pragma unroll
    for (int b = 0; b < 8; ++b) {
      const int k = 8 * e + b;
      if (k + NB - 1 < 64) Bf[(k + NB - 1) % NB] = ldsr<u32x4>(BADDR(k + NB - 1));
      if (b == 5) { us = ldsr<u32x2>(yb + (256 + 256 * e) * 2); }
      if (b == 6 && e < 5) W1 = ldsr<u32x4>(ub + (256 + 256 * (e + 1) - 24) * 2);
      const u32x4 B = Bf[k % NB];
      acc[e] = mf16(Af[b], B, acc[e]);
      if (e < 7) acc[e + 1] = mf16(Af[8 + b], B, acc[e + 1]);
      if (b == 0) {
        if (e < 6) { V[0] = mf16(B, bnd[0], zz); V[1] = mf16(B, bnd[1], zz); V[2] = mf16(B, bnd[2], zz); V[3] = mf16(W1, bnd[3], zz); }
        if (e < 4) { V[4] = mf16(B, bnd[4], zz); V[5] = mf16(W1, bnd[5], zz); }
        if (e < 6) acc[e + 2] = mf16(Af[16], B, acc[e + 2]);
        if (e < 4) acc[e + 4] = mf16(afx, B, acc[e + 4]);
        if (e > 0) {
          f32x2 u01 = up2(us.x), u23 = up2(us.y);
          float o0 = rbf(acc[e - 1][0]) + rbf(u01.x * dsk), o1 = rbf(acc[e - 1][1]) + rbf(u01.y * dsk);
          float o2 = rbf(acc[e - 1][2]) + rbf(u23.x * dsk), o3 = rbf(acc[e - 1][3]) + rbf(u23.y * dsk);
          ldsw(yb + (256 * (e - 1)) * 2, u32x2{pack2(o0, o1), pack2(o2, o3)});
        }
      }
      if (b == 1 && e < 6) {
#pragma unroll
        for (int p = 0; p < 3; ++p) if (p < 2 || e < 4)
          vp[p] = u32x4{pack2(V[2 * p][0], V[2 * p][1]), pack2(V[2 * p][2], V[2 * p][3]), pack2(V[2 * p + 1][0], V[2 * p + 1][1]), pack2(V[2 * p + 1][2], V[2 * p + 1][3])};
      }
      if (b >= 2 && b <= 4 && e + b < 8) { acc[e + b] = mf16(vp[0], t2[b - 2][0], acc[e + b]); acc[e + b] = mf16(vp[1], t2[b - 2][1], acc[e + b]); }
      if (b >= 4 && e + b < 8) acc[e + b] = mf16(vp[2], t3[b - 4], acc[e + b]);
      __builtin_amdgcn_sched_barrier(0);
    }
  }
  {
    f32x2 u01 = up2(us.x), u23 = up2(us.y);
    float o0 = rbf(acc[7][0]) + rbf(u01.x * dsk), o1 = rbf(acc[7][1]) + rbf(u01.y * dsk);
    float o2 = rbf(acc[7][2]) + rbf(u23.x * dsk), o3 = rbf(acc[7][3]) + rbf(u23.y * dsk);
    ldsw(yb + (256 * 7) * 2, u32x2{pack2(o0, o1), pack2(o2, o3)});
  }
  ST(7);
  bar();
  ST(8);
  {
    const u32 ra = A_UT + (4 * (q & 1) + (j >> 2)) * UT_STR + (256 * wv + 16 * (q >> 1) + 4 * (j & 3)) * 2;
    __amdgpu_buffer_rsrc_t orr = __builtin_amdgcn_make_buffer_rsrc(P.out, 0, 2048 * 1152 * 4, 0x00020000);
    const u32 ov = ((256 * wv + 16 * (q >> 1) + j) * 1152 + d0 + 4 * (q & 1)) * 4;
#pragma unroll
    for (int i = 0; i < 8; ++i) {
      u32x2 v = __builtin_bit_cast(u32x2, __builtin_amdgcn_ds_read_tr16_b64_v4i16((LAS s16x4*)(size_t)(ra + i * 64)));
      __builtin_amdgcn_raw_buffer_store_b128(u32x4{v.x << 16, v.x & 0xffff0000u, v.y << 16, v.y & 0xffff0000u}, orr, ov, i * 32 * 4608, OAUXA);
    }
  }
  ST(9);
  STAMP_FLUSH(P);
}

__global__ __launch_bounds__(512, 2) void fused(Params P) {
  const int b = blockIdx.x, xcd = b & 7, slot = b >> 3;
  if (slot < 18) g_role(P, 2 * xcd + (slot >= 9), slot >= 9 ? slot - 9 : slot);
  if (slot >= 18 && slot < 27) a_role(P, 72 * xcd + 8 * (slot - 18));
}

extern "C" void kernel_launch(void* const* d_in, const int* in_sizes, int n_in, void* d_out, int out_size, void* d_ws, size_t ws_size, hipStream_t stream) {
  Params P;
  for (int i = 0; i < 18; ++i) P.in[i] = (const u16*)d_in[i];
  P.out = (float*)d_out; P.ws = (char*)d_ws;
  (void)hipFuncSetAttribute((const void*)fused, hipFuncAttributeMaxDynamicSharedMemorySize, LDS_BYTES);
#define GRID 256
  fused<<<GRID, 512, LDS_BYTES, stream>>>(P);
}
```

```cpp
#define CODEPF 3
#define XROT 2
#define CPF 2
#define CDELAY 200
#include <hip/hip_runtime.h>
#include <stdint.h>

typedef unsigned short u16; typedef unsigned u32; typedef unsigned long long u64;
typedef __bf16 bf16x8 __attribute__((ext_vector_type(8)));
typedef __bf16 bf16x2 __attribute__((ext_vector_type(2)));
typedef short s16x4 __attribute__((ext_vector_type(4)));
typedef float f32x16 __attribute__((ext_vector_type(16)));
typedef float f32x4 __attribute__((ext_vector_type(4)));
typedef float f32x2 __attribute__((ext_vector_type(2)));
typedef u32 u32x4 __attribute__((ext_vector_type(4)));
typedef u32 u32x2 __attribute__((ext_vector_type(2)));
#define LAS __attribute__((address_space(3)))
#define GAS __attribute__((address_space(1)))
#define DEV __device__ __forceinline__

struct Params { const u16* in[18]; float* out; char* ws; };

constexpr u32 WS_EPOCH = 0, WS_FLAG = 4096, WS_GRAN = 65536, WS_H = 262144;
constexpr u32 H_BYTES = 16u * 4u * 72u * 1024u;
constexpr u32 LDS_BYTES = 163840, SCR = 147456;
constexpr int SPIN_MAX = 1 << 16;
#define OAUX 0
#define OAUXA OAUX

#define STAMP_DECL
#define ST(i)
#define STAMP_FLUSH(P)

DEV float bf2f(u16 v) { return __uint_as_float(((u32)v) << 16); }
DEV u32 pack2(float a, float b) { bf16x2 r; r[0] = (__bf16)a; r[1] = (__bf16)b; return __builtin_bit_cast(u32, r); }
DEV float rbf(float f) { return (float)(__bf16)f; }
DEV void bar() { asm volatile("s_waitcnt lgkmcnt(0)\n\ts_barrier" ::: "memory"); }
DEV u32 mix32(u32 x) { x *= 0x9E3779B1u; x ^= x >> 15; x *= 0x85EBCA77u; x ^= x >> 13; return x + 0x632BE5ABu; }
DEV f32x2 cmul(f32x2 a, f32x2 b) { return f32x2{a.x * b.x - a.y * b.y, a.x * b.y + a.y * b.x}; }
DEV f32x2 cfma(f32x2 a, f32x2 b, f32x2 c) { return f32x2{fmaf(a.x, b.x, fmaf(-a.y, b.y, c.x)), fmaf(a.x, b.y, fmaf(a.y, b.x, c.y))}; }
DEV f32x2 cexp(float re, float im) {
  float e = __builtin_amdgcn_exp2f(re * 1.44269504088896341f);
  float r = __builtin_amdgcn_fractf(im * 0.15915494309189535f);
  return f32x2{e * __builtin_amdgcn_cosf(r), e * __builtin_amdgcn_sinf(r)};
}
DEV void glds4(const char* gb, u32 ldsb, u32 v0, u32 v1, u32 v2, u32 v3) {
  u32 keep;
  asm volatile("s_mov_b32 %0, m0\n\ts_mov_b32 m0, %2\n\ts_nop 0\n\t"
               "global_load_lds_dwordx4 %3, %1\n\tglobal_load_lds_dwordx4 %4, %1 offset:1024\n\t"
               "global_load_lds_dwordx4 %5, %1 offset:2048\n\tglobal_load_lds_dwordx4 %6, %1 offset:3072\n\ts_mov_b32 m0, %0"
               : "=&s"(keep) : "s"(gb), "s"(ldsb), "v"(v0), "v"(v1), "v"(v2), "v"(v3) : "memory");
}
DEV void glds1(const char* gb, u32 ldsb, u32 v0) {
  u32 keep;
  asm volatile("s_mov_b32 %0, m0\n\ts_mov_b32 m0, %2\n\ts_nop 0\n\tglobal_load_lds_dwordx4 %3, %1\n\ts_mov_b32 m0, %0"
               : "=&s"(keep) : "s"(gb), "s"(ldsb), "v"(v0) : "memory");
}
template <class T> DEV T ldsr(u32 a) { return *(const LAS T*)(size_t)a; }
template <class T> DEV void ldsw(u32 a, T v) { *(LAS T*)(size_t)a = v; }
DEV f32x2 pr(const f32x16& a, int m) { return f32x2{a[2 * m], a[2 * m + 1]}; }

#define RA 1
DEV void swap16(float x, float& lo, float& hi) { auto r = __builtin_amdgcn_permlane16_swap(__float_as_uint(x), __float_as_uint(x), false, false); lo = __uint_as_float(r[0]); hi = __uint_as_float(r[1]); }
DEV void swap32(float x, float& lo, float& hi) { auto r = __builtin_amdgcn_permlane32_swap(__float_as_uint(x), __float_as_uint(x), false, false); lo = __uint_as_float(r[0]); hi = __uint_as_float(r[1]); }
DEV int wrap9(int v) { return v >= 9 ? v - 9 : v; }
DEV u64 load_epoch(const char* ws) { u64 ep; asm volatile("s_load_dwordx2 %0, %1, 0x0\n\ts_waitcnt lgkmcnt(0)" : "=s"(ep) : "s"(ws) : "memory"); return ep; }
DEV void touch64(const char* gb, u32 ldsb, u32 v0) {
  u32 keep;
  asm volatile("s_mov_b32 %0, m0\n\ts_mov_b32 m0, %2\n\ts_nop 0\n\tglobal_load_lds_dword %3, %1\n\ts_mov_b32 m0, %0"
               : "=&s"(keep) : "s"(gb), "s"(ldsb), "v"(v0) : "memory");
}

DEV void g_role(const Params& P, int c, int j, u64 pc) {
  u32 pl = 0, pr_ = 0, pi_ = 0;
  const int lane = threadIdx.x & 63, wv = __builtin_amdgcn_readfirstlane(threadIdx.x >> 6);
  STAMP_DECL
  ST(0);
  const int rot = (j + 4 * (c & 1) + XROT * (c >> 1)) % 9;
  constexpr int NSL = RA + 2;
  if (wv < 4)
    asm volatile("global_load_ushort %0, %3, %4\n\tglobal_load_ushort %1, %3, %5\n\tglobal_load_ushort %2, %3, %6"
                 : "=&v"(pl), "=&v"(pr_), "=&v"(pi_) : "v"((64 * j + 16 * wv + (lane & 15)) * 2), "s"(P.in[16]), "s"(P.in[10]), "s"(P.in[11]) : "memory");
  if (__builtin_expect(wv >= 4, 1)) {
    const int w = wv - 4;
    const int l31 = lane & 31, hh = lane >> 5;
    u32 vx[4], vb[4];
#pragma unroll
    for (int q = 0; q < 4; ++q) {
      int i = 2 * w + (q >> 1), qq = 2 * (q & 1) + (lane >> 5), r = (lane >> 3) & 3;
      int time = 32 * qq + 16 * (r & 1) + 2 * i + (r >> 1);
      u32 ch = (u32)(((lane & 7) ^ ((4 * (q & 1) + (lane >> 4)) & 7)) << 4) + 3072 - 1024 * q;
      vx[q] = (128 * c + time) * 1152 + ch;
      vb[q] = (64 * j + 32 * (w & 1) + 8 * q + (lane >> 3)) * 1152 + ch;
    }
    const char* xb = (const char*)P.in[9] - 3072;
    const char* bb = (const char*)(w < 2 ? P.in[12] : P.in[13]) - 3072;
    const char* cb = (const char*)(w < 2 ? P.in[14] : P.in[15]) - 3072;
#pragma unroll
    for (int s = 0; s <= RA; ++s) {
      u32 sl = s * 32768 + w * 4096; int ks = wrap9(rot + s);
      glds4(xb + ks * 128, sl, vx[0], vx[1], vx[2], vx[3]);
      glds4(bb + ks * 128, sl + 16384, vb[0], vb[1], vb[2], vb[3]);
    }
    ST(8);
    u32 dk0, dk1;
    asm volatile("global_load_ushort %0, %2, %3\n\tglobal_load_ushort %1, %2, %3 offset:64" : "=&v"(dk0), "=&v"(dk1) : "v"((64 * j + l31) * 2), "s"(P.in[17]) : "memory");
    const u64 ep = load_epoch(P.ws);
    u32 tlo = mix32((u32)ep), thi = mix32((u32)(ep >> 32) ^ 0x5bd1e995u);
    asm volatile("" :: "s"(tlo), "s"(thi));
    ST(9);
    s16x4 xs[8];
    asm volatile("s_waitcnt vmcnt(%0)" :: "n"(8 * RA + 2) : "memory");
    int slot_cur = 0, slot_new = RA + 1 == NSL ? 0 : RA + 1, kn = wrap9(rot + RA + 1);
    int cpc = 0;
#pragma unroll 1
    for (int s = 0; s < 9; ++s) {
      bar();
      u32 sl = slot_new * 32768 + w * 4096;
      if (s + RA + 1 <= 8) {
        glds4(xb + kn * 128, sl, vx[0], vx[1], vx[2], vx[3]);
        glds4(bb + kn * 128, sl + 16384, vb[0], vb[1], vb[2], vb[3]);
      } else {
        glds4(cb + slot_new * 256, sl, vb[0], vb[1], vb[2], vb[3]);
        glds4(cb + slot_new * 256 + 128, sl + 16384, vb[0], vb[1], vb[2], vb[3]);
      }
      if (wrap9(rot + s) == j) {
        const int e = (lane >> 2) & 3, pp = lane & 3;
        u32 sb = slot_cur * 32768 + (32 * hh + 4 * w + 16 * (e >> 1) + 2 * (e & 1)) * 128 + (((2 * ((lane >> 4) & 1) + (pp >> 1)) ^ ((w << 1) | (e & 1))) << 4) + (pp & 1) * 8;
#pragma unroll
        for (int g = 0; g < 4; ++g) {
          xs[g] = __builtin_amdgcn_ds_read_tr16_b64_v4i16((LAS s16x4*)(size_t)(sb + (64 * (g & 1) + (g >> 1)) * 128));
          xs[4 + g] = __builtin_amdgcn_ds_read_tr16_b64_v4i16((LAS s16x4*)(size_t)((sb ^ 64) + (64 * (g & 1) + (g >> 1)) * 128));
        }
      }
      slot_cur = slot_cur + 1 == NSL ? 0 : slot_cur + 1; slot_new = slot_new + 1 == NSL ? 0 : slot_new + 1; kn = wrap9(kn + 1);
      asm volatile("s_waitcnt vmcnt(%0)" :: "n"(8 * RA) : "memory");
    }
    bar();
    ST(3);
    asm volatile("" : "+v"(dk0), "+v"(dk1));
    const float dsk0 = __uint_as_float(dk0 << 16), dsk1 = __uint_as_float(dk1 << 16);
    for (int q = 2 * NSL; q < 9; ++q) glds4(cb + q * 128, q * 16384 + w * 4096, vb[0], vb[1], vb[2], vb[3]);
    glds4(cb + (8 % NSL) * 256, (8 % NSL) * 32768 + w * 4096, vb[0], vb[1], vb[2], vb[3]);
    glds4(cb + (8 % NSL) * 256 + 128, (8 % NSL) * 32768 + 16384 + w * 4096, vb[0], vb[1], vb[2], vb[3]);
    f32x16 acc0, acc1;
#pragma unroll
    for (int r = 0; r < 16; ++r) {
      acc0[r] = rbf(bf2f((u16)xs[r >> 2][r & 3]) * dsk0);
      acc1[r] = rbf(bf2f((u16)xs[4 + (r >> 2)][r & 3]) * dsk1);
    }
    asm volatile("" : "+v"(acc0), "+v"(acc1));
    asm volatile("s_waitcnt vmcnt(0)" ::: "memory");
    ST(1);
    bar();
    ST(4);
    {
      const u64 tag = ((u64)thi << 32) | tlo;
      GAS u64* fp = (GAS u64*)(P.ws + WS_FLAG + (c * 9 + (lane < 9 ? lane : 0)) * 256);
      for (int it = 0; it < SPIN_MAX; ++it) {
        u64 v = __hip_atomic_load(fp, __ATOMIC_RELAXED, __HIP_MEMORY_SCOPE_AGENT);
        if (__all(v == tag)) break;
        __builtin_amdgcn_s_sleep(1);
      }
      asm volatile("" ::: "memory");
    }
    ST(5);
    if (c == 15 && j == 0 && w == 0 && lane == 0)
      __hip_atomic_store((GAS u64*)(P.ws + WS_EPOCH), ((u64)((u32)(ep >> 32) + 1u) << 32) | (u32)((u32)ep + 1u), __ATOMIC_RELAXED, __HIP_MEMORY_SCOPE_AGENT);
    __amdgpu_buffer_rsrc_t hr = __builtin_amdgcn_make_buffer_rsrc(P.ws + WS_H, 0, H_BYTES, 0x00020000);
    const u32 hs = (c * 4 + w) * 72 * 1024;
    const int r2 = j;
    int pcs[9];
#pragma unroll
    for (int q = 0; q < 9; ++q) pcs[q] = wrap9(r2 + q);
    u32 aoff[4];
#pragma unroll
    for (int s = 0; s < 4; ++s) aoff[s] = l31 * 128 + (((2 * s + hh) ^ ((l31 >> 1) & 7)) << 4);
#define PFD 12
    constexpr int PF = PFD;
    u32x4 hf[PF];
#define HLOAD(k) __builtin_amdgcn_raw_buffer_load_b128(hr, lane * 16 + ((k) & 3) * 1024, hs + pcs[(k) >> 3] * 8192 + (((k) >> 2) & 1) * 4096, 16)
#pragma unroll
    for (int k = 0; k < PF; ++k) hf[k] = HLOAD(k);
    bf16x8 cf[2][4];
#define CREAD(set, k) do { _Pragma("unroll") for (int u = 0; u < 2; ++u) { u32 ca = pcs[((k) + u) >> 3] * 16384 + aoff[((k) + u) & 3]; u32 cbase = ((((k) + u) >> 2) & 1) * 8192; \
      cf[set][2 * u] = ldsr<bf16x8>(ca + cbase); cf[set][2 * u + 1] = ldsr<bf16x8>(ca + cbase + 4096); } } while (0)
    CREAD(0, 0);
    __builtin_amdgcn_sched_barrier(0);
#pragma unroll
    for (int k = 0; k < 72; k += 2) {
      const int set = (k >> 1) & 1;
      if (k + 2 < 72) CREAD(set ^ 1, k + 2);
      __builtin_amdgcn_sched_barrier(0);
#pragma unroll
      for (int u = 0; u < 2; ++u) {
        bf16x8 a = __builtin_bit_cast(bf16x8, hf[(k + u) % PF]);
        acc0 = __builtin_amdgcn_mfma_f32_32x32x16_bf16(a, cf[set][2 * u], acc0, 0, 0, 0);
        acc1 = __builtin_amdgcn_mfma_f32_32x32x16_bf16(a, cf[set][2 * u + 1], acc1, 0, 0, 0);
        if (k + u + PF < 72) hf[(k + u) % PF] = HLOAD(k + u + PF);
      }
      asm volatile("" : "+v"(acc0), "+v"(acc1));
      __builtin_amdgcn_sched_barrier(0);
    }
    ST(6);
    __amdgpu_buffer_rsrc_t orr = __builtin_amdgcn_make_buffer_rsrc(P.out, 0, 2048 * 1152 * 4, 0x00020000);
    const u32 ov = ((128 * c + 32 * w + 4 * hh) * 1152 + 576 + 64 * j + l31) * 4;
#pragma unroll
    for (int r = 0; r < 16; ++r) {
      __builtin_amdgcn_raw_buffer_store_b32(__float_as_uint(acc0[r]), orr, ov, (8 * (r >> 2) + (r & 3)) * 4608, OAUX);
      __builtin_amdgcn_raw_buffer_store_b32(__float_as_uint(acc1[r]), orr, ov + 128, (8 * (r >> 2) + (r & 3)) * 4608, OAUX);
    }
    ST(7);
  } else {
    const int l15 = lane & 15, q4 = lane >> 4;
    const int p = 64 * j + 16 * wv + l15;
    if (!(wv == 0 && (blockIdx.x >> 3) < CODEPF)) asm volatile("s_waitcnt vmcnt(0)" ::: "memory");
    else {
      touch64((const char*)(pc & ~(u64)127), SCR, (blockIdx.x >> 3) * 8192 + lane * 128);
    }
    const u64 ep = load_epoch(P.ws);
    asm volatile("s_waitcnt vmcnt(1)" : "+v"(pl), "+v"(pr_), "+v"(pi_) :: "memory");
    float dt = rbf(__builtin_amdgcn_exp2f(__uint_as_float(pl << 16) * 1.44269504088896341f));
    float Lr = __uint_as_float(pr_ << 16), Li = __uint_as_float(pi_ << 16);
    f32x2 lam = cexp(dt * Lr, dt * Li);
    float inv = 2.f / (Lr * Lr + Li * Li);
    f32x2 coef = cmul(f32x2{lam.x - 1.f, lam.y}, f32x2{Lr * inv, -Li * inv});
    f32x2 mu = cmul(lam, lam); mu = cmul(mu, mu); mu = cmul(mu, mu); mu = cmul(mu, mu);
    f32x2 nu = cmul(mu, mu), nu2 = cmul(nu, nu), La = cmul(nu2, nu2);
    f32x2 La2 = cmul(La, La), La4 = cmul(La2, La2);
    f32x2 one = {1.f, 0.f};
    f32x2 Lg = cmul((q4 & 2) ? La2 : one, (q4 & 1) ? La : one);
    f32x2 nuq = (q4 & 1) ? nu : one;
    f32x2 cm0 = cmul(coef, cmul((q4 & 2) ? nu2 : one, nuq)), cm1 = cmul(cm0, mu);
    u32 tlo = mix32((u32)ep), thi = mix32((u32)(ep >> 32) ^ 0x5bd1e995u);
    asm volatile("" :: "s"(tlo), "s"(thi));
    asm volatile("" : "+v"(lam), "+v"(coef), "+v"(mu), "+v"(La4), "+v"(Lg), "+v"(cm0), "+v"(cm1), "+v"(nu), "+v"(nu2), "+v"(nuq));
    f32x4 ar[8], ai[8];
#pragma unroll
    for (int i = 0; i < 8; ++i) { ar[i] = f32x4{0.f, 0.f, 0.f, 0.f}; ai[i] = ar[i]; }
    ST(1);
    u32 xa0 = l15 * 128 + ((q4 ^ (l15 >> 1)) << 4), xa1 = xa0 ^ 64, ba0 = xa0 + 16384 + wv * 2048, ba1 = ba0 ^ 64;
    bf16x8 A[2][8], Bq[2][2];
#define STEP(cs, xa, ba) do { Bq[cs ^ 1][0] = ldsr<bf16x8>(ba); Bq[cs ^ 1][1] = ldsr<bf16x8>(ba + 8192); \
      _Pragma("unroll") for (int i = 0; i < 8; ++i) { A[cs ^ 1][i] = ldsr<bf16x8>(xa + i * 2048); \
        ar[i] = __builtin_amdgcn_mfma_f32_16x16x32_bf16(A[cs][i], Bq[cs][0], ar[i], 0, 0, 0); ai[i] = __builtin_amdgcn_mfma_f32_16x16x32_bf16(A[cs][i], Bq[cs][1], ai[i], 0, 0, 0); \
        asm volatile("" : "+v"(ar[i]), "+v"(ai[i])); __builtin_amdgcn_sched_barrier(0); } } while (0)
    bar();
    ST(2);
    Bq[0][0] = ldsr<bf16x8>(ba0); Bq[0][1] = ldsr<bf16x8>(ba0 + 8192);
#pragma unroll
    for (int i = 0; i < 8; ++i) A[0][i] = ldsr<bf16x8>(xa0 + i * 2048);
    __builtin_amdgcn_sched_barrier(0);
    int slot = 0;
#pragma unroll 1
    for (int s = 0; s < 9; ++s) {
      STEP(0, xa1, ba1);
      const u32 dl = slot + 1 == NSL ? (u32)(-(NSL - 1) * 32768) : 32768u;
      slot = slot + 1 == NSL ? 0 : slot + 1;
      xa0 += dl; xa1 += dl; ba0 += dl; ba1 += dl;
      bar();
      STEP(1, xa0, ba0);
    }
    ST(3);
    const f32x2 lr2 = {lam.x, lam.x}, li2 = {lam.y, lam.y};
    f32x2 Br[16], Bi[16];
#pragma unroll
    for (int i = 0; i < 8; ++i) { Br[2 * i] = f32x2{ar[i][0], ar[i][1]}; Br[2 * i + 1] = f32x2{ar[i][2], ar[i][3]}; Bi[2 * i] = f32x2{ai[i][0], ai[i][1]}; Bi[2 * i + 1] = f32x2{ai[i][2], ai[i][3]}; }
    f32x2 sr = Br[0], si = Bi[0];
#pragma unroll
    for (int k = 1; k < 16; ++k) {
      f32x2 t1 = __builtin_elementwise_fma(lr2, sr, Br[k]), t2 = __builtin_elementwise_fma(lr2, si, Bi[k]);
      f32x2 n1 = __builtin_elementwise_fma(-li2, si, t1), n2 = __builtin_elementwise_fma(li2, sr, t2);
      sr = n1; si = n2;
    }
    const f32x2 S0 = {sr.x, si.x};
    f32x2 T = cfma(mu, S0, f32x2{sr.y, si.y}), lo, hi;
    { float a, b; swap16(T.x, a, b); lo.x = a; hi.x = b; swap16(T.y, a, b); lo.y = a; hi.y = b; }
    f32x2 U = cfma(nu, lo, hi), E = (q4 & 1) ? lo : f32x2{0.f, 0.f};
    { float a, b; swap32(U.x, a, b); lo.x = a; hi.x = b; swap32(U.y, a, b); lo.y = a; hi.y = b; }
    f32x2 tot = cfma(nu2, lo, hi);
    if (q4 == 0 && c < 15) {
      u32x4 gv = {__float_as_uint(tot.x), tlo, __float_as_uint(tot.y), thi};
      __amdgpu_buffer_rsrc_t gr = __builtin_amdgcn_make_buffer_rsrc(P.ws + WS_GRAN, 0, 9 * 16 * 1024, 0x00020000);
      __builtin_amdgcn_raw_buffer_store_b128(gv, gr, l15 * 16, (j * 16 + c) * 1024 + wv * 256, 16);
    }
    ST(4);
    if (q4 & 2) E = cfma(nuq, lo, E);
    f32x2 La_ = cmul(coef, E), Lb_ = cmul(coef, cfma(mu, E, S0));
    const f32x2 cr2 = {coef.x, coef.x}, ci2 = {coef.y, coef.y};
#pragma unroll
    for (int k = 0; k < 16; ++k) {
      f32x2 n1 = __builtin_elementwise_fma(-ci2, Bi[k], cr2 * Br[k]), n2 = __builtin_elementwise_fma(ci2, Br[k], cr2 * Bi[k]);
      Br[k] = n1; Bi[k] = n2;
    }
#pragma unroll
    for (int k = 0; k < 16; ++k) asm volatile("" : "+v"(Br[k]), "+v"(Bi[k]));
    asm volatile("" : "+v"(La_), "+v"(Lb_));
    ST(8);
    f32x2 H = {0.f, 0.f};
    if (c > 0) {
      const char* gb = P.ws + WS_GRAN + j * 16384 + wv * 256;
      u32 go[4]; bool valid[4];
#pragma unroll
      for (int n = 0; n < 4; ++n) { int cs = c - 1 - q4 - 4 * n; valid[n] = cs >= 0; go[n] = (cs < 0 ? 0 : cs) * 1024 + l15 * 16; }
      u32x4 g0, g1, g2, g3;
      for (int it = 0; it < SPIN_MAX; ++it) {
        asm volatile("global_load_dwordx4 %0, %4, %8 sc1\n\tglobal_load_dwordx4 %1, %5, %8 sc1\n\tglobal_load_dwordx4 %2, %6, %8 sc1\n\tglobal_load_dwordx4 %3, %7, %8 sc1\n\ts_waitcnt vmcnt(0)"
                     : "=&v"(g0), "=&v"(g1), "=&v"(g2), "=&v"(g3) : "v"(go[0]), "v"(go[1]), "v"(go[2]), "v"(go[3]), "s"(gb) : "memory");
        bool ok = (!valid[0] || (g0.y == tlo && g0.w == thi)) && (!valid[1] || (g1.y == tlo && g1.w == thi)) &&
                  (!valid[2] || (g2.y == tlo && g2.w == thi)) && (!valid[3] || (g3.y == tlo && g3.w == thi));
        if (__all(ok)) break;
        __builtin_amdgcn_s_sleep(1);
      }
      f32x2 z = {0.f, 0.f};
      f32x2 s3 = valid[3] ? f32x2{__uint_as_float(g3.x), __uint_as_float(g3.z)} : z;
      f32x2 s2 = valid[2] ? f32x2{__uint_as_float(g2.x), __uint_as_float(g2.z)} : z;
      f32x2 s1 = valid[1] ? f32x2{__uint_as_float(g1.x), __uint_as_float(g1.z)} : z;
      f32x2 s0 = valid[0] ? f32x2{__uint_as_float(g0.x), __uint_as_float(g0.z)} : z;
      H = cmul(Lg, cfma(La4, cfma(La4, cfma(La4, s3, s2), s1), s0));
      float a, b;
      swap16(H.x, a, b); H.x = a + b; swap16(H.y, a, b); H.y = a + b;
      swap32(H.x, a, b); H.x = a + b; swap32(H.y, a, b); H.y = a + b;
    }
    ST(5);
    f32x2 sa = cfma(cm0, H, La_), sb_ = cfma(cm1, H, Lb_);
    sr = f32x2{sa.x, sb_.x}; si = f32x2{sa.y, sb_.y};
#pragma unroll
    for (int k = 0; k < 16; ++k) {
      f32x2 t1 = __builtin_elementwise_fma(lr2, sr, Br[k]), t2 = __builtin_elementwise_fma(lr2, si, Bi[k]);
      f32x2 n1 = __builtin_elementwise_fma(-li2, si, t1), n2 = __builtin_elementwise_fma(li2, sr, t2);
      sr = n1; si = n2; Br[k] = sr; Bi[k] = si;
    }
    ST(10);
    __amdgpu_buffer_rsrc_t hr = __builtin_amdgcn_make_buffer_rsrc(P.ws + WS_H, 0, H_BYTES, 0x00020000);
    const u32 my = SCR + wv * 4096;
    const u32 fsw = ((l15 & 3) << 2) | ((l15 >> 2) & 1);
    const u32 wa = my + l15 * 256;
    u32 ra[2];
#pragma unroll
    for (int u = 0; u < 2; ++u) {
      int row = 8 * (q4 >> 1) + 4 * u + (l15 >> 2);
      u32 fs = ((row & 3) << 2) | ((row >> 2) & 1);
      ra[u] = my + row * 256 + (((2 * (q4 & 1) + ((l15 & 3) >> 1)) ^ fs) << 4) + (l15 & 1) * 8;
    }
#pragma unroll
    for (int part = 0; part < 2; ++part) {
#pragma unroll
      for (int e0 = 0; e0 < 2; ++e0)
#pragma unroll
        for (int hf = 0; hf < 2; ++hf) {
          u32x4 v;
#pragma unroll
          for (int q = 0; q < 4; ++q) {
            int k = 8 * hf + 2 * q;
            v[q] = part ? pack2(-Bi[k][e0], -Bi[k + 1][e0]) : pack2(Br[k][e0], Br[k + 1][e0]);
          }
          ldsw(wa + (((4 * q4 + 2 * e0 + hf) ^ fsw) << 4), v);
        }
#pragma unroll
      for (int rt = 0; rt < 4; ++rt) {
        s16x4 lo4 = __builtin_amdgcn_ds_read_tr16_b64_v4i16((LAS s16x4*)(size_t)(ra[0] ^ (rt * 64)));
        s16x4 hi4 = __builtin_amdgcn_ds_read_tr16_b64_v4i16((LAS s16x4*)(size_t)(ra[1] ^ (rt * 64)));
        u32x2 l2 = __builtin_bit_cast(u32x2, lo4), h2 = __builtin_bit_cast(u32x2, hi4);
        u32x4 v = {l2.x, l2.y, h2.x, h2.y};
        __builtin_amdgcn_raw_buffer_store_b128(v, hr, lane * 16, ((c * 4 + rt) * 72 + j * 8 + part * 4 + wv) * 1024, 16);
      }
    }
    ST(6);
    asm volatile("s_waitcnt vmcnt(0)" ::: "memory");
    ST(9);
    bar();
    if (wv == 0 && lane == 0)
      __hip_atomic_store((GAS u64*)(P.ws + WS_FLAG + (c * 9 + j) * 256), ((u64)thi << 32) | tlo, __ATOMIC_RELAXED, __HIP_MEMORY_SCOPE_AGENT);
    ST(7);
  }
  STAMP_FLUSH(P);
}

struct C2 { f32x2 r, i; };
DEV C2 cm2(const C2& a, const C2& b) {
  C2 o; o.r = __builtin_elementwise_fma(-a.i, b.i, a.r * b.r); o.i = __builtin_elementwise_fma(a.i, b.r, a.r * b.i); return o;
}
DEV C2 cexp2(f32x2 re, f32x2 im) { f32x2 a = cexp(re.x, im.x), b = cexp(re.y, im.y); C2 o; o.r = f32x2{a.x, b.x}; o.i = f32x2{a.y, b.y}; return o; }
DEV u32x2 pk4(const C2& a) { return u32x2{pack2(a.r.x, a.r.y), pack2(a.i.x, a.i.y)}; }
DEV f32x2 up2(u32 v) { return f32x2{__uint_as_float(v << 16), __uint_as_float(v & 0xffff0000u)}; }
DEV f32x4 mf16(u32x4 a, u32x4 b, f32x4 c) { return __builtin_amdgcn_mfma_f32_16x16x32_bf16(__builtin_bit_cast(bf16x8, a), __builtin_bit_cast(bf16x8, b), c, 0, 0, 0); }
DEV u32x4 bandop(int x, int lo, int hi, u32 mul) {
  u32 m = (u32)((((1ull << (hi - lo + 1)) - 1) << 32) >> (32 + hi - x)) & 0xffu;
  u32 sp = m | (m << 15);
  u32x4 o;
#pragma unroll
  for (int i = 0; i < 4; ++i) o[i] = ((sp >> (2 * i)) & 0x10001u) * mul;
  return o;
}
DEV u32x4 rd2(u32 a0, u32 a1) { u32x2 x = ldsr<u32x2>(a0), y = ldsr<u32x2>(a1); return u32x4{x.x, x.y, y.x, y.y}; }
constexpr u32 A_RAW = 0, A_UT = 32768, UT_STR = 4640, A_KB = A_UT + 8 * UT_STR, KB_STR = 8704, KP = 544, CPS = 2112;

DEV void a_role(const Params& P, int d0) {
  const int lane = threadIdx.x & 63, wv = __builtin_amdgcn_readfirstlane(threadIdx.x >> 6);
  const int d = d0 + wv, j = lane & 15, q = lane >> 4;
  STAMP_DECL
  ST(0);
  u32 pAr, pAi, pBr, pBi, pCr, pCi;
  {
    const u32 vo = d * 256 + lane * 4;
    asm volatile("global_load_dword %0, %6, %7\n\tglobal_load_dword %1, %6, %8\n\tglobal_load_dword %2, %6, %9\n\t"
                 "global_load_dword %3, %6, %10\n\tglobal_load_dword %4, %6, %11\n\tglobal_load_dword %5, %6, %12"
                 : "=&v"(pAr), "=&v"(pAi), "=&v"(pBr), "=&v"(pBi), "=&v"(pCr), "=&v"(pCi)
                 : "v"(vo), "s"(P.in[1]), "s"(P.in[2]), "s"(P.in[3]), "s"(P.in[4]), "s"(P.in[5]), "s"(P.in[6]) : "memory");
  }
  const u32 uo = (256 * wv + lane) * 1152 + d0 * 2;
  const char* ug = (const char*)P.in[0];
  glds1(ug, A_RAW + wv * 4096, uo);
  glds1(ug, A_RAW + wv * 4096 + 1024, uo + 64 * 1152);
  typedef const __attribute__((address_space(4))) u32* cptr;
  const u32 sdt = *(cptr)((const char*)P.in[7] + ((d * 2) & ~3)), sds = *(cptr)((const char*)P.in[8] + ((d * 2) & ~3));
  const float ldt = __uint_as_float((d & 1) ? (sdt & 0xffff0000u) : (sdt << 16));
  const float dsk = __uint_as_float((d & 1) ? (sds & 0xffff0000u) : (sds << 16));
  const float dt = rbf(__builtin_amdgcn_exp2f(ldt * 1.44269504088896341f));
  u32x4 bnd[6];
  bnd[0] = bandop(j + 16 - 8 * q, 3, 5, 0x3f80); bnd[1] = bandop(j + 16 - 8 * q, 6, 10, 0x3f80); bnd[2] = bandop(j + 16 - 8 * q, 11, 13, 0x3f80);
  bnd[3] = bandop(j + 24 - 8 * q, 14, 18, 0x3f80); bnd[4] = bandop(j + 16 - 8 * q, 5, 11, 0x3f80); bnd[5] = bandop(j + 24 - 8 * q, 12, 20, 0x3f80);
  u32x4 afx = bandop(j + 16 - 8 * q, 3, 4, 1);
  const u32 ut = A_UT + wv * UT_STR, kb = A_KB + wv * KB_STR;
  ldsw(ut + lane * 8, u32x2{0u, 0u});
#pragma unroll
  for (int i = 0; i < 6; ++i) asm volatile("" : "+v"(bnd[i]));
  asm volatile("" : "+v"(afx));
  ST(1);
  asm volatile("s_waitcnt vmcnt(2)" : "+v"(pAr), "+v"(pAi), "+v"(pBr), "+v"(pBi), "+v"(pCr), "+v"(pCi) :: "memory");
  ST(2);
  const f32x2 dt2 = {dt, dt};
  const f32x2 are = dt2 * up2(pAr), aim = dt2 * up2(pAi);
  C2 W; { C2 B = {dt2 * up2(pBr), dt2 * up2(pBi)}, Cc = {up2(pCr), up2(pCi)}; W = cm2(Cc, B); W.i = -W.i; }
  const u32 wrow = kb + lane * 8, rfr = kb + j * KP + q * 16;
  u32x4 Yf[8];
  C2 E8 = cexp2(8.f * are, 8.f * aim);
  {
    C2 Y[16];
    Y[1] = E8;
    ldsw(wrow, u32x2{0x3f803f80u, 0u});
    ldsw(wrow + KP, pk4(Y[1]));
#pragma unroll
    for (int i = 2; i < 16; ++i) { Y[i] = (i & 1) ? cm2(Y[i - 1], Y[1]) : cm2(Y[i / 2], Y[i / 2]); ldsw(wrow + i * KP, pk4(Y[i])); }
#pragma unroll
    for (int s = 0; s < 8; ++s) Yf[s] = ldsr<u32x4>(rfr + 64 * s);
  }
  glds1(ug, A_RAW + wv * 4096 + 2048, uo + 128 * 1152);
  E8.i = -E8.i;
  C2 E[8];
  E[1] = cexp2(are, -aim);
  const C2 G = cexp2(128.f * are, -128.f * aim);
  E[2] = cm2(E[1], E[1]); E[3] = cm2(E[2], E[1]); E[4] = cm2(E[2], E[2]); E[5] = cm2(E[4], E[1]); E[6] = cm2(E[4], E[2]); E[7] = cm2(E[4], E[3]);
  f32x4 T0 = {0.f, 0.f, 0.f, 0.f}, T1 = T0, T2 = T0;
  C2 Pc = W;
#pragma unroll
  for (int h = 0; h < 2; ++h) {
    ldsw(wrow + (8 * h) * KP, pk4(Pc));
#pragma unroll
    for (int r = 1; r < 8; ++r) ldsw(wrow + (8 * h + r) * KP, pk4(cm2(Pc, E[r])));
    Pc = cm2(Pc, G);
  }
#pragma unroll
  for (int s = 0; s < 8; ++s) T0 = mf16(ldsr<u32x4>(rfr + 64 * s), Yf[s], T0);
  glds1(ug, A_RAW + wv * 4096 + 3072, uo + 192 * 1152);
  u32x2 keep[4];
#pragma unroll
  for (int h = 0; h < 2; ++h) {
    keep[2 * h] = pk4(cm2(Pc, E[4])); keep[2 * h + 1] = pk4(cm2(Pc, E8));
    ldsw(wrow + (8 * h) * KP, pk4(Pc)); ldsw(wrow + (8 * h + 1) * KP, pk4(cm2(Pc, E[2])));
    ldsw(wrow + (8 * h + 4) * KP, keep[2 * h]); ldsw(wrow + (8 * h + 5) * KP, pk4(cm2(Pc, E[6])));
    Pc = cm2(Pc, G);
  }
#pragma unroll
  for (int g = 0; g < 4; ++g) {
    ldsw(wrow + (4 * g + 2) * KP, pk4(Pc)); ldsw(wrow + (4 * g + 3) * KP, pk4(cm2(Pc, E[4])));
    Pc = cm2(Pc, G);
  }
#pragma unroll
  for (int s = 0; s < 8; ++s) T1 = mf16(ldsr<u32x4>(rfr + 64 * s), Yf[s], T1);
#pragma unroll
  for (int g = 0; g < 8; ++g) { ldsw(wrow + (4 * (g >> 1) + (g & 1)) * KP, pk4(Pc)); Pc = cm2(Pc, G); }
  ldsw(wrow + 2 * KP, pk4(Pc));
#pragma unroll
  for (int g = 0; g < 4; ++g) ldsw(wrow + (4 * g + 3) * KP, keep[g]);
#pragma unroll
  for (int s = 0; s < 8; ++s) T2 = mf16(ldsr<u32x4>(rfr + 64 * s), Yf[s], T2);
  ST(3);
  asm volatile("s_waitcnt vmcnt(0)" ::: "memory");
  ST(4);
  {
    const u32 ra = A_RAW + wv * 4096 + (8 * q + 4 * ((j & 3) >> 1) + (j >> 2)) * 16 + (j & 1) * 8;
    const u32 wa = A_UT + (j & 7) * UT_STR + (256 + 256 * wv + 8 * q + 4 * (j >> 3)) * 2;
#pragma unroll
    for (int i = 0; i < 8; ++i) {
      s16x4 v = __builtin_amdgcn_ds_read_tr16_b64_v4i16((LAS s16x4*)(size_t)(ra + i * 512));
      ldsw(wa + i * 64, v);
    }
  }
  const u32 c3 = kb + 3 * CPS;
  ldsw(c3 + lane * 16, u32x4{0u, 0u, 0u, 0u}); ldsw(c3 + 1024 + lane * 16, u32x4{0u, 0u, 0u, 0u});
  {
    const int l0 = 128 * (q >> 1) + 4 * (q & 1) + 8 * j;
    ldsw(c3 + (544 - l0) * 2, u32x2{pack2(T0[3], T0[2]), pack2(T0[1], lane == 0 ? T0[0] + dsk : T0[0])});
    ldsw(c3 + (288 - l0) * 2, u32x2{pack2(T2[3], T1[1]), pack2(T1[0], T1[0])});
    if (lane == 63) ldsw(c3 + 64, u32x2{pack2(0.f, T2[3]), pack2(T2[3], T2[3])});
    const int mu0 = 32 + 8 * q + (j >> 1), od = j & 1;
    u32 p2 = pack2(lane == 0 ? T2[0] : T1[2], T1[3]);
    u32 p3 = pack2(lane == 0 ? T2[2] : T2[0], T2[1]);
    const int ia = lane == 0 ? 647 + 204 - 63 : (od ? 647 + 68 - mu0 : 647 + 204 + 1 - mu0);
    ldsw(c3 + ia * 2, (u16)p2);
    ldsw(c3 + (647 + 136 * od - mu0) * 2, (u16)(p2 >> 16));
    const int i3 = (od ? 967 - 64 : 967 + 84 - 63) - 16 * q - (j >> 1);
    ldsw(c3 + (lane == 0 ? 967 + 84 - 127 : i3) * 2, (u16)p3);
    ldsw(c3 + (i3 - 8) * 2, (u16)(p3 >> 16));
    const u32 k1024 = __builtin_amdgcn_readfirstlane(p2) & 0xffffu;
#pragma unroll
    for (int i = 0; i < 4; ++i) afx[i] *= k1024;
#pragma unroll
    for (int h = 0; h < 2; ++h) {
      u32x4 x = ldsr<u32x4>(c3 + h * 1024 + lane * 16); u32x2 y = ldsr<u32x2>(c3 + h * 1024 + lane * 16 + 16);
      u32 a0 = __builtin_amdgcn_alignbit(x.y, x.x, 16), a1 = __builtin_amdgcn_alignbit(x.z, x.y, 16), a2 = __builtin_amdgcn_alignbit(x.w, x.z, 16),
          a3 = __builtin_amdgcn_alignbit(y.x, x.w, 16), a4 = __builtin_amdgcn_alignbit(y.y, y.x, 16);
      ldsw(kb + 2 * CPS + h * 1024 + lane * 16, u32x4{a0, a1, a2, a3});
      ldsw(kb + 1 * CPS + h * 1024 + lane * 16, u32x4{x.y, x.z, x.w, y.x});
      ldsw(kb + h * 1024 + lane * 16, u32x4{a1, a2, a3, a4});
    }
  }
  u32x4 Af[17], t2[3][2], t3[4];
  {
    const u32 fb = kb + (j & 3) * CPS - 8 * (j >> 2);
    const u32 fa = fb + 16 * q + (544 - 16 - 512) * 2;
#pragma unroll
    for (int n = 0; n < 17; ++n) Af[n] = rd2(fa + (16 - n) * 64, fa + (16 - n) * 64 + 8);
    const u32 ft = fb + 8 * q;
#pragma unroll
    for (int D = 2; D < 5; ++D) { u32 a = ft + (644 - 16 * D) * 2; t2[D - 2][0] = rd2(a, a + 136); t2[D - 2][1] = rd2(a + 272, a + 408); }
#pragma unroll
    for (int D = 4; D < 8; ++D) { u32 a = ft + (964 - 16 * D) * 2; t3[D - 4] = rd2(a, a + 168); }
  }
  ST(5);
  bar();
  ST(6);
  f32x4 acc[8];
#pragma unroll
  for (int i = 0; i < 8; ++i) acc[i] = f32x4{0.f, 0.f, 0.f, 0.f};
  const f32x4 zz = {0.f, 0.f, 0.f, 0.f};
  const u32 ub = ut + (16 * j + 8 * q) * 2;
  const u32 yb = ut + (16 * j + 4 * q) * 2;
#define BADDR(k) (ub + (256 + 256 * ((k) >> 3) - 16 - 32 * ((k) & 7)) * 2)
  constexpr int NB = 4;
  u32x4 Bf[NB], W1;
#pragma unroll
  for (int k = 0; k < NB - 1; ++k) Bf[k] = ldsr<u32x4>(BADDR(k));
  W1 = ldsr<u32x4>(ub + (256 - 24) * 2);
  f32x4 V[6]; u32x4 vp[3];
  __amdgpu_buffer_rsrc_t orr = __builtin_amdgcn_make_buffer_rsrc(P.out, 0, 2048 * 1152 * 4, 0x00020000);
  const u32 ora = A_UT + (4 * (q & 1) + (j >> 2)) * UT_STR + (32 * wv + 16 * (q >> 1) + 4 * (j & 3)) * 2;
  const u32 ov = ((32 * wv + 16 * (q >> 1) + j) * 1152 + d0 + 4 * (q & 1)) * 4;
  u32x2 ovv;
#define YSTAGE(t) ldsw(yb + (256 * (t)) * 2, u32x2{pack2(acc[t][0], acc[t][1]), pack2(acc[t][2], acc[t][3])})
#define YREAD(t) ovv = __builtin_bit_cast(u32x2, __builtin_amdgcn_ds_read_tr16_b64_v4i16((LAS s16x4*)(size_t)(ora + (t) * 512)))
#define YSTORE(t) __builtin_amdgcn_raw_buffer_store_b128(u32x4{ovv.x << 16, ovv.x & 0xffff0000u, ovv.y << 16, ovv.y & 0xffff0000u}, orr, ov, (t) * 256 * 4608, OAUXA)
#pragma unroll
  for (int e = 0; e < 8; ++e) {
#pragma unroll
    for (int b = 0; b < 8; ++b) {
      const int k = 8 * e + b;
      if (b == 3 && e > 0) { bar(); YREAD(e - 1); }
      if (k + NB - 1 < 64) Bf[(k + NB - 1) % NB] = ldsr<u32x4>(BADDR(k + NB - 1));
      if (b == 6 && e < 5) W1 = ldsr<u32x4>(ub + (256 + 256 * (e + 1) - 24) * 2);
      const u32x4 B = Bf[k % NB];
      acc[e] = mf16(Af[b], B, acc[e]);
      if (e < 7) acc[e + 1] = mf16(Af[8 + b], B, acc[e + 1]);
      if (b == 0) {
        if (e < 6) { V[0] = mf16(B, bnd[0], zz); V[1] = mf16(B, bnd[1], zz); V[2] = mf16(B, bnd[2], zz); V[3] = mf16(W1, bnd[3], zz); }
        if (e < 4) { V[4] = mf16(B, bnd[4], zz); V[5] = mf16(W1, bnd[5], zz); }
        if (e < 6) acc[e + 2] = mf16(Af[16], B, acc[e + 2]);
        if (e < 4) acc[e + 4] = mf16(afx, B, acc[e + 4]);
        if (e > 0) YSTAGE(e - 1);
      }
      if (b == 1 && e < 6) {
#pragma unroll
        for (int p = 0; p < 3; ++p) if (p < 2 || e < 4)
          vp[p] = u32x4{pack2(V[2 * p][0], V[2 * p][1]), pack2(V[2 * p][2], V[2 * p][3]), pack2(V[2 * p + 1][0], V[2 * p + 1][1]), pack2(V[2 * p + 1][2], V[2 * p + 1][3])};
      }
      if (b >= 2 && b <= 4 && e + b < 8) { acc[e + b] = mf16(vp[0], t2[b - 2][0], acc[e + b]); acc[e + b] = mf16(vp[1], t2[b - 2][1], acc[e + b]); }
      if (b >= 4 && e + b < 8) acc[e + b] = mf16(vp[2], t3[b - 4], acc[e + b]);
      if (b == 4 && e > 0) YSTORE(e - 1);
      __builtin_amdgcn_sched_barrier(0);
    }
  }
  YSTAGE(7);
  ST(7);
  bar();
  ST(8);
  YREAD(7); YSTORE(7);
  ST(9);
  STAMP_FLUSH(P);
}

DEV void pf_mat(const u16* m0, const u16* m1, int id, int xr, int lane, int wv, int n) {
#pragma unroll 1
  for (int i = id; i < n; i += 40) {
    int pc = i + xr; pc = pc >= 162 ? pc - 162 : pc;
    const char* g = (const char*)(pc < 81 ? m0 : m1);
    touch64(g + (pc < 81 ? pc : pc - 81) * 8192, wv * 256, lane * 128);
    asm volatile("s_waitcnt vmcnt(%0)" :: "n"(CPF - 1) : "memory");
  }
}
DEV void pf_role(const Params& P, int k) {
  const int lane = threadIdx.x & 63, wv = __builtin_amdgcn_readfirstlane(threadIdx.x >> 6);
  const int id = k * 8 + wv, xr = (blockIdx.x & 7) * 20;
  { u64 t0 = __builtin_amdgcn_s_memrealtime(); for (int it = 0; it < 4000 && __builtin_amdgcn_s_memrealtime() - t0 < CDELAY; ++it) __builtin_amdgcn_s_sleep(2); }
  pf_mat(P.in[14], P.in[15], id, xr, lane, wv, 162);
  asm volatile("s_waitcnt vmcnt(0)" ::: "memory");
}

__global__ __launch_bounds__(512, 2) void fused(Params P) {
  const int b = blockIdx.x, xcd = b & 7, slot = b >> 3;
  u64 pc = 0;
  asm volatile("s_getpc_b64 %0" : "=s"(pc));
  if (__builtin_expect(slot < 18, 1)) {
    g_role(P, 2 * xcd + (slot >= 9), slot >= 9 ? slot - 9 : slot, pc);
  } else if (slot < 27) {
    a_role(P, 72 * xcd + 8 * (slot - 18));
  } else pf_role(P, slot - 27);
}

extern "C" void kernel_launch(void* const* d_in, const int* in_sizes, int n_in, void* d_out, int out_size, void* d_ws, size_t ws_size, hipStream_t stream) {
  Params P;
  for (int i = 0; i < 18; ++i) P.in[i] = (const u16*)d_in[i];
  P.out = (float*)d_out; P.ws = (char*)d_ws;
  (void)hipFuncSetAttribute((const void*)fused, hipFuncAttributeMaxDynamicSharedMemorySize, LDS_BYTES);
#define GRID 256
  fused<<<GRID, 512, LDS_BYTES, stream>>>(P);
}
```

```cpp
#define CODEPF 3 XROT=2 CPF=2 CDELAY=200 OAUX=16 OAUXA=0
#include <hip/hip_runtime.h>
#include <stdint.h>

typedef unsigned short u16; typedef unsigned u32; typedef unsigned long long u64;
typedef __bf16 bf16x8 __attribute__((ext_vector_type(8)));
typedef __bf16 bf16x2 __attribute__((ext_vector_type(2)));
typedef short s16x4 __attribute__((ext_vector_type(4)));
typedef float f32x16 __attribute__((ext_vector_type(16)));
typedef float f32x4 __attribute__((ext_vector_type(4)));
typedef float f32x2 __attribute__((ext_vector_type(2)));
typedef u32 u32x4 __attribute__((ext_vector_type(4)));
typedef u32 u32x2 __attribute__((ext_vector_type(2)));
#define LAS __attribute__((address_space(3)))
#define GAS __attribute__((address_space(1)))
#define DEV __device__ __forceinline__

struct Params { const u16* in[18]; float* out; char* ws; };

constexpr u32 WS_EPOCH = 0, WS_FLAG = 4096, WS_GRAN = 65536, WS_H = 262144;
constexpr u32 H_BYTES = 16u * 4u * 72u * 1024u;
constexpr u32 LDS_BYTES = 163840, SCR = 147456;
constexpr int SPIN_MAX = 1 << 16;
#define OAUX 0
#define OAUXA OAUX

#define STAMP_DECL
#define ST(i)
#define STAMP_FLUSH(P)

DEV float bf2f(u16 v) { return __uint_as_float(((u32)v) << 16); }
DEV u32 pack2(float a, float b) { bf16x2 r; r[0] = (__bf16)a; r[1] = (__bf16)b; return __builtin_bit_cast(u32, r); }
DEV float rbf(float f) { return (float)(__bf16)f; }
DEV void bar() { asm volatile("s_waitcnt lgkmcnt(0)\n\ts_barrier" ::: "memory"); }
DEV u32 mix32(u32 x) { x *= 0x9E3779B1u; x ^= x >> 15; x *= 0x85EBCA77u; x ^= x >> 13; return x + 0x632BE5ABu; }
DEV f32x2 cmul(f32x2 a, f32x2 b) { return f32x2{a.x * b.x - a.y * b.y, a.x * b.y + a.y * b.x}; }
DEV f32x2 cfma(f32x2 a, f32x2 b, f32x2 c) { return f32x2{fmaf(a.x, b.x, fmaf(-a.y, b.y, c.x)), fmaf(a.x, b.y, fmaf(a.y, b.x, c.y))}; }
DEV f32x2 cexp(float re, float im) {
  float e = __builtin_amdgcn_exp2f(re * 1.44269504088896341f);
  float r = __builtin_amdgcn_fractf(im * 0.15915494309189535f);
  return f32x2{e * __builtin_amdgcn_cosf(r), e * __builtin_amdgcn_sinf(r)};
}
DEV void glds4(const char* gb, u32 ldsb, u32 v0, u32 v1, u32 v2, u32 v3) {
  u32 keep;
  asm volatile("s_mov_b32 %0, m0\n\ts_mov_b32 m0, %2\n\ts_nop 0\n\t"
               "global_load_lds_dwordx4 %3, %1\n\tglobal_load_lds_dwordx4 %4, %1 offset:1024\n\t"
               "global_load_lds_dwordx4 %5, %1 offset:2048\n\tglobal_load_lds_dwordx4 %6, %1 offset:3072\n\ts_mov_b32 m0, %0"
               : "=&s"(keep) : "s"(gb), "s"(ldsb), "v"(v0), "v"(v1), "v"(v2), "v"(v3) : "memory");
}
DEV void glds1(const char* gb, u32 ldsb, u32 v0) {
  u32 keep;
  asm volatile("s_mov_b32 %0, m0\n\ts_mov_b32 m0, %2\n\ts_nop 0\n\tglobal_load_lds_dwordx4 %3, %1\n\ts_mov_b32 m0, %0"
               : "=&s"(keep) : "s"(gb), "s"(ldsb), "v"(v0) : "memory");
}
template <class T> DEV T ldsr(u32 a) { return *(const LAS T*)(size_t)a; }
template <class T> DEV void ldsw(u32 a, T v) { *(LAS T*)(size_t)a = v; }
DEV f32x2 pr(const f32x16& a, int m) { return f32x2{a[2 * m], a[2 * m + 1]}; }

#define RA 1
DEV void swap16(float x, float& lo, float& hi) { auto r = __builtin_amdgcn_permlane16_swap(__float_as_uint(x), __float_as_uint(x), false, false); lo = __uint_as_float(r[0]); hi = __uint_as_float(r[1]); }
DEV void swap32(float x, float& lo, float& hi) { auto r = __builtin_amdgcn_permlane32_swap(__float_as_uint(x), __float_as_uint(x), false, false); lo = __uint_as_float(r[0]); hi = __uint_as_float(r[1]); }
DEV int wrap9(int v) { return v >= 9 ? v - 9 : v; }
DEV u64 load_epoch(const char* ws) { u64 ep; asm volatile("s_load_dwordx2 %0, %1, 0x0\n\ts_waitcnt lgkmcnt(0)" : "=s"(ep) : "s"(ws) : "memory"); return ep; }
DEV void touch64(const char* gb, u32 ldsb, u32 v0) {
  u32 keep;
  asm volatile("s_mov_b32 %0, m0\n\ts_mov_b32 m0, %2\n\ts_nop 0\n\tglobal_load_lds_dword %3, %1\n\ts_mov_b32 m0, %0"
               : "=&s"(keep) : "s"(gb), "s"(ldsb), "v"(v0) : "memory");
}

DEV void g_role(const Params& P, int c, int j, u64 pc) {
  u32 pl = 0, pr_ = 0, pi_ = 0;
  const int lane = threadIdx.x & 63, wv = __builtin_amdgcn_readfirstlane(threadIdx.x >> 6);
  STAMP_DECL
  ST(0);
  const int rot = wrap9(j + 4 * (c & 1));
  constexpr int NSL = RA + 2;
  if (wv < 4)
    asm volatile("global_load_ushort %0, %3, %4\n\tglobal_load_ushort %1, %3, %5\n\tglobal_load_ushort %2, %3, %6"
                 : "=&v"(pl), "=&v"(pr_), "=&v"(pi_) : "v"((64 * j + 16 * wv + (lane & 15)) * 2), "s"(P.in[16]), "s"(P.in[10]), "s"(P.in[11]) : "memory");
  if (__builtin_expect(wv >= 4, 1)) {
    const int w = wv - 4;
    const int l31 = lane & 31, hh = lane >> 5;
    u32 vx[4], vb[4];
#pragma unroll
    for (int q = 0; q < 4; ++q) {
      int i = 2 * w + (q >> 1), qq = 2 * (q & 1) + (lane >> 5), r = (lane >> 3) & 3;
      int time = 32 * qq + 16 * (r & 1) + 2 * i + (r >> 1);
      u32 ch = (u32)(((lane & 7) ^ ((4 * (q & 1) + (lane >> 4)) & 7)) << 4) + 3072 - 1024 * q;
      vx[q] = (128 * c + time) * 1152 + ch;
      vb[q] = (64 * j + 32 * (w & 1) + 8 * q + (lane >> 3)) * 1152 + ch;
    }
    const char* xb = (const char*)P.in[9] - 3072;
    const char* bb = (const char*)(w < 2 ? P.in[12] : P.in[13]) - 3072;
    const char* cb = (const char*)(w < 2 ? P.in[14] : P.in[15]) - 3072;
#pragma unroll
    for (int s = 0; s <= RA; ++s) {
      u32 sl = s * 32768 + w * 4096; int ks = wrap9(rot + s);
      glds4(xb + ks * 128, sl, vx[0], vx[1], vx[2], vx[3]);
      glds4(bb + ks * 128, sl + 16384, vb[0], vb[1], vb[2], vb[3]);
    }
    ST(8);
    u32 dk0, dk1;
    asm volatile("global_load_ushort %0, %2, %3\n\tglobal_load_ushort %1, %2, %3 offset:64" : "=&v"(dk0), "=&v"(dk1) : "v"((64 * j + l31) * 2), "s"(P.in[17]) : "memory");
    const u64 ep = load_epoch(P.ws);
    u32 tlo = mix32((u32)ep), thi = mix32((u32)(ep >> 32) ^ 0x5bd1e995u);
    asm volatile("" :: "s"(tlo), "s"(thi));
    ST(9);
    s16x4 xs[8];
    asm volatile("s_waitcnt vmcnt(%0)" :: "n"(8 * RA + 2) : "memory");
    int slot_cur = 0, slot_new = RA + 1 == NSL ? 0 : RA + 1, kn = wrap9(rot + RA + 1);
    int cpc = 0;
#pragma unroll 1
    for (int s = 0; s < 9; ++s) {
      bar();
      u32 sl = slot_new * 32768 + w * 4096;
      if (s + RA + 1 <= 8) {
        glds4(xb + kn * 128, sl, vx[0], vx[1], vx[2], vx[3]);
        glds4(bb + kn * 128, sl + 16384, vb[0], vb[1], vb[2], vb[3]);
      } else {
        glds4(cb + slot_new * 256, sl, vb[0], vb[1], vb[2], vb[3]);
        glds4(cb + slot_new * 256 + 128, sl + 16384, vb[0], vb[1], vb[2], vb[3]);
      }
      if (wrap9(rot + s) == j) {
        const int e = (lane >> 2) & 3, pp = lane & 3;
        u32 sb = slot_cur * 32768 + (32 * hh + 4 * w + 16 * (e >> 1) + 2 * (e & 1)) * 128 + (((2 * ((lane >> 4) & 1) + (pp >> 1)) ^ ((w << 1) | (e & 1))) << 4) + (pp & 1) * 8;
#pragma unroll
        for (int g = 0; g < 4; ++g) {
          xs[g] = __builtin_amdgcn_ds_read_tr16_b64_v4i16((LAS s16x4*)(size_t)(sb + (64 * (g & 1) + (g >> 1)) * 128));
          xs[4 + g] = __builtin_amdgcn_ds_read_tr16_b64_v4i16((LAS s16x4*)(size_t)((sb ^ 64) + (64 * (g & 1) + (g >> 1)) * 128));
        }
      }
      slot_cur = slot_cur + 1 == NSL ? 0 : slot_cur + 1; slot_new = slot_new + 1 == NSL ? 0 : slot_new + 1; kn = wrap9(kn + 1);
      asm volatile("s_waitcnt vmcnt(%0)" :: "n"(8 * RA) : "memory");
    }
    bar();
    ST(3);
    asm volatile("" : "+v"(dk0), "+v"(dk1));
    const float dsk0 = __uint_as_float(dk0 << 16), dsk1 = __uint_as_float(dk1 << 16);
    for (int q = 2 * NSL; q < 9; ++q) glds4(cb + q * 128, q * 16384 + w * 4096, vb[0], vb[1], vb[2], vb[3]);
    glds4(cb + (8 % NSL) * 256, (8 % NSL) * 32768 + w * 4096, vb[0], vb[1], vb[2], vb[3]);
    glds4(cb + (8 % NSL) * 256 + 128, (8 % NSL) * 32768 + 16384 + w * 4096, vb[0], vb[1], vb[2], vb[3]);
    f32x16 acc0, acc1;
#pragma unroll
    for (int r = 0; r < 16; ++r) {
      acc0[r] = rbf(bf2f((u16)xs[r >> 2][r & 3]) * dsk0);
      acc1[r] = rbf(bf2f((u16)xs[4 + (r >> 2)][r & 3]) * dsk1);
    }
    asm volatile("" : "+v"(acc0), "+v"(acc1));
    asm volatile("s_waitcnt vmcnt(0)" ::: "memory");
    ST(1);
    bar();
    ST(4);
    {
      const u64 tag = ((u64)thi << 32) | tlo;
      GAS u64* fp = (GAS u64*)(P.ws + WS_FLAG + (c * 9 + (lane < 9 ? lane : 0)) * 256);
      for (int it = 0; it < SPIN_MAX; ++it) {
        u64 v = __hip_atomic_load(fp, __ATOMIC_RELAXED, __HIP_MEMORY_SCOPE_AGENT);
        if (__all(v == tag)) break;
        __builtin_amdgcn_s_sleep(1);
      }
      asm volatile("" ::: "memory");
    }
    ST(5);
    if (c == 15 && j == 0 && w == 0 && lane == 0)
      __hip_atomic_store((GAS u64*)(P.ws + WS_EPOCH), ((u64)((u32)(ep >> 32) + 1u) << 32) | (u32)((u32)ep + 1u), __ATOMIC_RELAXED, __HIP_MEMORY_SCOPE_AGENT);
    __amdgpu_buffer_rsrc_t hr = __builtin_amdgcn_make_buffer_rsrc(P.ws + WS_H, 0, H_BYTES, 0x00020000);
    const u32 hs = (c * 4 + w) * 72 * 1024;
    const int r2 = j;
    int pcs[9];
#pragma unroll
    for (int q = 0; q < 9; ++q) pcs[q] = wrap9(r2 + q);
    u32 aoff[4];
#pragma unroll
    for (int s = 0; s < 4; ++s) aoff[s] = l31 * 128 + (((2 * s + hh) ^ ((l31 >> 1) & 7)) << 4);
#define PFD 12
    constexpr int PF = PFD;
    u32x4 hf[PF];
#define HLOAD(k) __builtin_amdgcn_raw_buffer_load_b128(hr, lane * 16 + ((k) & 3) * 1024, hs + pcs[(k) >> 3] * 8192 + (((k) >> 2) & 1) * 4096, 16)
#pragma unroll
    for (int k = 0; k < PF; ++k) hf[k] = HLOAD(k);
    bf16x8 cf[2][4];
#define CREAD(set, k) do { _Pragma("unroll") for (int u = 0; u < 2; ++u) { u32 ca = pcs[((k) + u) >> 3] * 16384 + aoff[((k) + u) & 3]; u32 cbase = ((((k) + u) >> 2) & 1) * 8192; \
      cf[set][2 * u] = ldsr<bf16x8>(ca + cbase); cf[set][2 * u + 1] = ldsr<bf16x8>(ca + cbase + 4096); } } while (0)
    CREAD(0, 0);
    __builtin_amdgcn_sched_barrier(0);
#pragma unroll
    for (int k = 0; k < 72; k += 2) {
      const int set = (k >> 1) & 1;
      if (k + 2 < 72) CREAD(set ^ 1, k + 2);
      __builtin_amdgcn_sched_barrier(0);
#pragma unroll
      for (int u = 0; u < 2; ++u) {
        bf16x8 a = __builtin_bit_cast(bf16x8, hf[(k + u) % PF]);
        acc0 = __builtin_amdgcn_mfma_f32_32x32x16_bf16(a, cf[set][2 * u], acc0, 0, 0, 0);
        acc1 = __builtin_amdgcn_mfma_f32_32x32x16_bf16(a, cf[set][2 * u + 1], acc1, 0, 0, 0);
        if (k + u + PF < 72) hf[(k + u) % PF] = HLOAD(k + u + PF);
      }
      asm volatile("" : "+v"(acc0), "+v"(acc1));
      __builtin_amdgcn_sched_barrier(0);
    }
    ST(6);
    __amdgpu_buffer_rsrc_t orr = __builtin_amdgcn_make_buffer_rsrc(P.out, 0, 2048 * 1152 * 4, 0x00020000);
    const u32 ov = ((128 * c + 32 * w + 4 * hh) * 1152 + 576 + 64 * j + l31) * 4;
#pragma unroll
    for (int r = 0; r < 16; ++r) {
      __builtin_amdgcn_raw_buffer_store_b32(__float_as_uint(acc0[r]), orr, ov, (8 * (r >> 2) + (r & 3)) * 4608, OAUX);
      __builtin_amdgcn_raw_buffer_store_b32(__float_as_uint(acc1[r]), orr, ov + 128, (8 * (r >> 2) + (r & 3)) * 4608, OAUX);
    }
    ST(7);
  } else {
    const int l15 = lane & 15, q4 = lane >> 4;
    const int p = 64 * j + 16 * wv + l15;
    if (!(wv == 0 && (blockIdx.x >> 3) < CODEPF)) asm volatile("s_waitcnt vmcnt(0)" ::: "memory");
    else {
      touch64((const char*)(pc & ~(u64)127), SCR, (blockIdx.x >> 3) * 8192 + lane * 128);
    }
    const u64 ep = load_epoch(P.ws);
    asm volatile("s_waitcnt vmcnt(1)" : "+v"(pl), "+v"(pr_), "+v"(pi_) :: "memory");
    float dt = rbf(__builtin_amdgcn_exp2f(__uint_as_float(pl << 16) * 1.44269504088896341f));
    float Lr = __uint_as_float(pr_ << 16), Li = __uint_as_float(pi_ << 16);
    f32x2 lam = cexp(dt * Lr, dt * Li);
    float inv = 2.f / (Lr * Lr + Li * Li);
    f32x2 coef = cmul(f32x2{lam.x - 1.f, lam.y}, f32x2{Lr * inv, -Li * inv});
    f32x2 mu = cmul(lam, lam); mu = cmul(mu, mu); mu = cmul(mu, mu); mu = cmul(mu, mu);
    f32x2 nu = cmul(mu, mu), nu2 = cmul(nu, nu), La = cmul(nu2, nu2);
    f32x2 La2 = cmul(La, La), La4 = cmul(La2, La2);
    f32x2 one = {1.f, 0.f};
    f32x2 Lg = cmul((q4 & 2) ? La2 : one, (q4 & 1) ? La : one);
    f32x2 nuq = (q4 & 1) ? nu : one;
    f32x2 cm0 = cmul(coef, cmul((q4 & 2) ? nu2 : one, nuq)), cm1 = cmul(cm0, mu);
    u32 tlo = mix32((u32)ep), thi = mix32((u32)(ep >> 32) ^ 0x5bd1e995u);
    asm volatile("" :: "s"(tlo), "s"(thi));
    asm volatile("" : "+v"(lam), "+v"(coef), "+v"(mu), "+v"(La4), "+v"(Lg), "+v"(cm0), "+v"(cm1), "+v"(nu), "+v"(nu2), "+v"(nuq));
    f32x4 ar[8], ai[8];
#pragma unroll
    for (int i = 0; i < 8; ++i) { ar[i] = f32x4{0.f, 0.f, 0.f, 0.f}; ai[i] = ar[i]; }
    ST(1);
    u32 xa0 = l15 * 128 + ((q4 ^ (l15 >> 1)) << 4), xa1 = xa0 ^ 64, ba0 = xa0 + 16384 + wv * 2048, ba1 = ba0 ^ 64;
    bf16x8 A[2][8], Bq[2][2];
#define STEP(cs, xa, ba) do { Bq[cs ^ 1][0] = ldsr<bf16x8>(ba); Bq[cs ^ 1][1] = ldsr<bf16x8>(ba + 8192); \
      _Pragma("unroll") for (int i = 0; i < 8; ++i) { A[cs ^ 1][i] = ldsr<bf16x8>(xa + i * 2048); \
        ar[i] = __builtin_amdgcn_mfma_f32_16x16x32_bf16(A[cs][i], Bq[cs][0], ar[i], 0, 0, 0); ai[i] = __builtin_amdgcn_mfma_f32_16x16x32_bf16(A[cs][i], Bq[cs][1], ai[i], 0, 0, 0); \
        asm volatile("" : "+v"(ar[i]), "+v"(ai[i])); __builtin_amdgcn_sched_barrier(0); } } while (0)
    bar();
    ST(2);
    Bq[0][0] = ldsr<bf16x8>(ba0); Bq[0][1] = ldsr<bf16x8>(ba0 + 8192);
#pragma unroll
    for (int i = 0; i < 8; ++i) A[0][i] = ldsr<bf16x8>(xa0 + i * 2048);
    __builtin_amdgcn_sched_barrier(0);
    int slot = 0;
#pragma unroll 1
    for (int s = 0; s < 9; ++s) {
      STEP(0, xa1, ba1);
      const u32 dl = slot + 1 == NSL ? (u32)(-(NSL - 1) * 32768) : 32768u;
      slot = slot + 1 == NSL ? 0 : slot + 1;
      xa0 += dl; xa1 += dl; ba0 += dl; ba1 += dl;
      bar();
      STEP(1, xa0, ba0);
    }
    ST(3);
    const f32x2 lr2 = {lam.x, lam.x}, li2 = {lam.y, lam.y};
    f32x2 Br[16], Bi[16];
#pragma unroll
    for (int i = 0; i < 8; ++i) { Br[2 * i] = f32x2{ar[i][0], ar[i][1]}; Br[2 * i + 1] = f32x2{ar[i][2], ar[i][3]}; Bi[2 * i] = f32x2{ai[i][0], ai[i][1]}; Bi[2 * i + 1] = f32x2{ai[i][2], ai[i][3]}; }
    f32x2 sr = Br[0], si = Bi[0];
#pragma unroll
    for (int k = 1; k < 16; ++k) {
      f32x2 t1 = __builtin_elementwise_fma(lr2, sr, Br[k]), t2 = __builtin_elementwise_fma(lr2, si, Bi[k]);
      f32x2 n1 = __builtin_elementwise_fma(-li2, si, t1), n2 = __builtin_elementwise_fma(li2, sr, t2);
      sr = n1; si = n2;
    }
    const f32x2 S0 = {sr.x, si.x};
    f32x2 T = cfma(mu, S0, f32x2{sr.y, si.y}), lo, hi;
    { float a, b; swap16(T.x, a, b); lo.x = a; hi.x = b; swap16(T.y, a, b); lo.y = a; hi.y = b; }
    f32x2 U = cfma(nu, lo, hi), E = (q4 & 1) ? lo : f32x2{0.f, 0.f};
    { float a, b; swap32(U.x, a, b); lo.x = a; hi.x = b; swap32(U.y, a, b); lo.y = a; hi.y = b; }
    f32x2 tot = cfma(nu2, lo, hi);
    if (q4 == 0 && c < 15) {
      u32x4 gv = {__float_as_uint(tot.x), tlo, __float_as_uint(tot.y), thi};
      __amdgpu_buffer_rsrc_t gr = __builtin_amdgcn_make_buffer_rsrc(P.ws + WS_GRAN, 0, 9 * 16 * 1024, 0x00020000);
      __builtin_amdgcn_raw_buffer_store_b128(gv, gr, l15 * 16, (j * 16 + c) * 1024 + wv * 256, 16);
    }
    ST(4);
    if (q4 & 2) E = cfma(nuq, lo, E);
    f32x2 La_ = cmul(coef, E), Lb_ = cmul(coef, cfma(mu, E, S0));
    const f32x2 cr2 = {coef.x, coef.x}, ci2 = {coef.y, coef.y};
#pragma unroll
    for (int k = 0; k < 16; ++k) {
      f32x2 n1 = __builtin_elementwise_fma(-ci2, Bi[k], cr2 * Br[k]), n2 = __builtin_elementwise_fma(ci2, Br[k], cr2 * Bi[k]);
      Br[k] = n1; Bi[k] = n2;
    }
#pragma unroll
    for (int k = 0; k < 16; ++k) asm volatile("" : "+v"(Br[k]), "+v"(Bi[k]));
    asm volatile("" : "+v"(La_), "+v"(Lb_));
    ST(8);
    f32x2 H = {0.f, 0.f};
    if (c > 0) {
      const char* gb = P.ws + WS_GRAN + j * 16384 + wv * 256;
      u32 go[4]; bool valid[4];
#pragma unroll
      for (int n = 0; n < 4; ++n) { int cs = c - 1 - q4 - 4 * n; valid[n] = cs >= 0; go[n] = (cs < 0 ? 0 : cs) * 1024 + l15 * 16; }
      u32x4 g0, g1, g2, g3;
      for (int it = 0; it < SPIN_MAX; ++it) {
        asm volatile("global_load_dwordx4 %0, %4, %8 sc1\n\tglobal_load_dwordx4 %1, %5, %8 sc1\n\tglobal_load_dwordx4 %2, %6, %8 sc1\n\tglobal_load_dwordx4 %3, %7, %8 sc1\n\ts_waitcnt vmcnt(0)"
                     : "=&v"(g0), "=&v"(g1), "=&v"(g2), "=&v"(g3) : "v"(go[0]), "v"(go[1]), "v"(go[2]), "v"(go[3]), "s"(gb) : "memory");
        bool ok = (!valid[0] || (g0.y == tlo && g0.w == thi)) && (!valid[1] || (g1.y == tlo && g1.w == thi)) &&
                  (!valid[2] || (g2.y == tlo && g2.w == thi)) && (!valid[3] || (g3.y == tlo && g3.w == thi));
        if (__all(ok)) break;
        __builtin_amdgcn_s_sleep(1);
      }
      f32x2 z = {0.f, 0.f};
      f32x2 s3 = valid[3] ? f32x2{__uint_as_float(g3.x), __uint_as_float(g3.z)} : z;
      f32x2 s2 = valid[2] ? f32x2{__uint_as_float(g2.x), __uint_as_float(g2.z)} : z;
      f32x2 s1 = valid[1] ? f32x2{__uint_as_float(g1.x), __uint_as_float(g1.z)} : z;
      f32x2 s0 = valid[0] ? f32x2{__uint_as_float(g0.x), __uint_as_float(g0.z)} : z;
      H = cmul(Lg, cfma(La4, cfma(La4, cfma(La4, s3, s2), s1), s0));
      float a, b;
      swap16(H.x, a, b); H.x = a + b; swap16(H.y, a, b); H.y = a + b;
      swap32(H.x, a, b); H.x = a + b; swap32(H.y, a, b); H.y = a + b;
    }
    ST(5);
    f32x2 sa = cfma(cm0, H, La_), sb_ = cfma(cm1, H, Lb_);
    sr = f32x2{sa.x, sb_.x}; si = f32x2{sa.y, sb_.y};
#pragma unroll
    for (int k = 0; k < 16; ++k) {
      f32x2 t1 = __builtin_elementwise_fma(lr2, sr, Br[k]), t2 = __builtin_elementwise_fma(lr2, si, Bi[k]);
      f32x2 n1 = __builtin_elementwise_fma(-li2, si, t1), n2 = __builtin_elementwise_fma(li2, sr, t2);
      sr = n1; si = n2; Br[k] = sr; Bi[k] = si;
    }
    ST(10);
    __amdgpu_buffer_rsrc_t hr = __builtin_amdgcn_make_buffer_rsrc(P.ws + WS_H, 0, H_BYTES, 0x00020000);
    const u32 my = SCR + wv * 4096;
    const u32 fsw = ((l15 & 3) << 2) | ((l15 >> 2) & 1);
    const u32 wa = my + l15 * 256;
    u32 ra[2];
#pragma unroll
    for (int u = 0; u < 2; ++u) {
      int row = 8 * (q4 >> 1) + 4 * u + (l15 >> 2);
      u32 fs = ((row & 3) << 2) | ((row >> 2) & 1);
      ra[u] = my + row * 256 + (((2 * (q4 & 1) + ((l15 & 3) >> 1)) ^ fs) << 4) + (l15 & 1) * 8;
    }
#pragma unroll
    for (int part = 0; part < 2; ++part) {
#pragma unroll
      for (int e0 = 0; e0 < 2; ++e0)
#pragma unroll
        for (int hf = 0; hf < 2; ++hf) {
          u32x4 v;
#pragma unroll
          for (int q = 0; q < 4; ++q) {
            int k = 8 * hf + 2 * q;
            v[q] = part ? pack2(-Bi[k][e0], -Bi[k + 1][e0]) : pack2(Br[k][e0], Br[k + 1][e0]);
          }
          ldsw(wa + (((4 * q4 + 2 * e0 + hf) ^ fsw) << 4), v);
        }
#pragma unroll
      for (int rt = 0; rt < 4; ++rt) {
        s16x4 lo4 = __builtin_amdgcn_ds_read_tr16_b64_v4i16((LAS s16x4*)(size_t)(ra[0] ^ (rt * 64)));
        s16x4 hi4 = __builtin_amdgcn_ds_read_tr16_b64_v4i16((LAS s16x4*)(size_t)(ra[1] ^ (rt * 64)));
        u32x2 l2 = __builtin_bit_cast(u32x2, lo4), h2 = __builtin_bit_cast(u32x2, hi4);
        u32x4 v = {l2.x, l2.y, h2.x, h2.y};
        __builtin_amdgcn_raw_buffer_store_b128(v, hr, lane * 16, ((c * 4 + rt) * 72 + j * 8 + part * 4 + wv) * 1024, 16);
      }
    }
    ST(6);
    asm volatile("s_waitcnt vmcnt(0)" ::: "memory");
    ST(9);
    bar();
    if (wv == 0 && lane == 0)
      __hip_atomic_store((GAS u64*)(P.ws + WS_FLAG + (c * 9 + j) * 256), ((u64)thi << 32) | tlo, __ATOMIC_RELAXED, __HIP_MEMORY_SCOPE_AGENT);
    ST(7);
  }
  STAMP_FLUSH(P);
}

struct C2 { f32x2 r, i; };
DEV C2 cm2(const C2& a, const C2& b) {
  C2 o; o.r = __builtin_elementwise_fma(-a.i, b.i, a.r * b.r); o.i = __builtin_elementwise_fma(a.i, b.r, a.r * b.i); return o;
}
DEV C2 cexp2(f32x2 re, f32x2 im) { f32x2 a = cexp(re.x, im.x), b = cexp(re.y, im.y); C2 o; o.r = f32x2{a.x, b.x}; o.i = f32x2{a.y, b.y}; return o; }
DEV u32x2 pk4(const C2& a) { return u32x2{pack2(a.r.x, a.r.y), pack2(a.i.x, a.i.y)}; }
DEV f32x2 up2(u32 v) { return f32x2{__uint_as_float(v << 16), __uint_as_float(v & 0xffff0000u)}; }
DEV f32x4 mf16(u32x4 a, u32x4 b, f32x4 c) { return __builtin_amdgcn_mfma_f32_16x16x32_bf16(__builtin_bit_cast(bf16x8, a), __builtin_bit_cast(bf16x8, b), c, 0, 0, 0); }
DEV u32x4 bandop(int x, int lo, int hi, u32 mul) {
  u32 m = (u32)((((1ull << (hi - lo + 1)) - 1) << 32) >> (32 + hi - x)) & 0xffu;
  u32 sp = m | (m << 15);
  u32x4 o;
#pragma unroll
  for (int i = 0; i < 4; ++i) o[i] = ((sp >> (2 * i)) & 0x10001u) * mul;
  return o;
}
DEV u32x4 rd2(u32 a0, u32 a1) { u32x2 x = ldsr<u32x2>(a0), y = ldsr<u32x2>(a1); return u32x4{x.x, x.y, y.x, y.y}; }
constexpr u32 A_RAW = 0, A_UT = 32768, UT_STR = 4640, A_KB = A_UT + 8 * UT_STR, KB_STR = 8704, KP = 544, CPS = 2112;

DEV void a_role(const Params& P, int d0) {
  const int lane = threadIdx.x & 63, wv = __builtin_amdgcn_readfirstlane(threadIdx.x >> 6);
  const int d = d0 + wv, j = lane & 15, q = lane >> 4;
  STAMP_DECL
  ST(0);
  u32 pAr, pAi, pBr, pBi, pCr, pCi;
  {
    const u32 vo = d * 256 + lane * 4;
    asm volatile("global_load_dword %0, %6, %7\n\tglobal_load_dword %1, %6, %8\n\tglobal_load_dword %2, %6, %9\n\t"
                 "global_load_dword %3, %6, %10\n\tglobal_load_dword %4, %6, %11\n\tglobal_load_dword %5, %6, %12"
                 : "=&v"(pAr), "=&v"(pAi), "=&v"(pBr), "=&v"(pBi), "=&v"(pCr), "=&v"(pCi)
                 : "v"(vo), "s"(P.in[1]), "s"(P.in[2]), "s"(P.in[3]), "s"(P.in[4]), "s"(P.in[5]), "s"(P.in[6]) : "memory");
  }
  const u32 uo = (256 * wv + lane) * 1152 + d0 * 2;
  const char* ug = (const char*)P.in[0];
  glds1(ug, A_RAW + wv * 4096, uo);
  glds1(ug, A_RAW + wv * 4096 + 1024, uo + 64 * 1152);
  typedef const __attribute__((address_space(4))) u32* cptr;
  const u32 sdt = *(cptr)((const char*)P.in[7] + ((d * 2) & ~3)), sds = *(cptr)((const char*)P.in[8] + ((d * 2) & ~3));
  const float ldt = __uint_as_float((d & 1) ? (sdt & 0xffff0000u) : (sdt << 16));
  const float dsk = __uint_as_float((d & 1) ? (sds & 0xffff0000u) : (sds << 16));
  const float dt = rbf(__builtin_amdgcn_exp2f(ldt * 1.44269504088896341f));
  u32x4 bnd[6];
  bnd[0] = bandop(j + 16 - 8 * q, 3, 5, 0x3f80); bnd[1] = bandop(j + 16 - 8 * q, 6, 10, 0x3f80); bnd[2] = bandop(j + 16 - 8 * q, 11, 13, 0x3f80);
  bnd[3] = bandop(j + 24 - 8 * q, 14, 18, 0x3f80); bnd[4] = bandop(j + 16 - 8 * q, 5, 11, 0x3f80); bnd[5] = bandop(j + 24 - 8 * q, 12, 20, 0x3f80);
  u32x4 afx = bandop(j + 16 - 8 * q, 3, 4, 1);
  const u32 ut = A_UT + wv * UT_STR, kb = A_KB + wv * KB_STR;
  ldsw(ut + lane * 8, u32x2{0u, 0u});
#pragma unroll
  for (int i = 0; i < 6; ++i) asm volatile("" : "+v"(bnd[i]));
  asm volatile("" : "+v"(afx));
  ST(1);
  asm volatile("s_waitcnt vmcnt(2)" : "+v"(pAr), "+v"(pAi), "+v"(pBr), "+v"(pBi), "+v"(pCr), "+v"(pCi) :: "memory");
  ST(2);
  const f32x2 dt2 = {dt, dt};
  const f32x2 are = dt2 * up2(pAr), aim = dt2 * up2(pAi);
  C2 W; { C2 B = {dt2 * up2(pBr), dt2 * up2(pBi)}, Cc = {up2(pCr), up2(pCi)}; W = cm2(Cc, B); W.i = -W.i; }
  const u32 wrow = kb + lane * 8, rfr = kb + j * KP + q * 16;
  u32x4 Yf[8];
  C2 E8 = cexp2(8.f * are, 8.f * aim);
  {
    C2 Y[16];
    Y[1] = E8;
    ldsw(wrow, u32x2{0x3f803f80u, 0u});
    ldsw(wrow + KP, pk4(Y[1]));
#pragma unroll
    for (int i = 2; i < 16; ++i) { Y[i] = (i & 1) ? cm2(Y[i - 1], Y[1]) : cm2(Y[i / 2], Y[i / 2]); ldsw(wrow + i * KP, pk4(Y[i])); }
#pragma unroll
    for (int s = 0; s < 8; ++s) Yf[s] = ldsr<u32x4>(rfr + 64 * s);
  }
  glds1(ug, A_RAW + wv * 4096 + 2048, uo + 128 * 1152);
  E8.i = -E8.i;
  C2 E[8];
  E[1] = cexp2(are, -aim);
  const C2 G = cexp2(128.f * are, -128.f * aim);
  E[2] = cm2(E[1], E[1]); E[3] = cm2(E[2], E[1]); E[4] = cm2(E[2], E[2]); E[5] = cm2(E[4], E[1]); E[6] = cm2(E[4], E[2]); E[7] = cm2(E[4], E[3]);
  f32x4 T0 = {0.f, 0.f, 0.f, 0.f}, T1 = T0, T2 = T0;
  C2 Pc = W;
#pragma unroll
  for (int h = 0; h < 2; ++h) {
    ldsw(wrow + (8 * h) * KP, pk4(Pc));
#pragma unroll
    for (int r = 1; r < 8; ++r) ldsw(wrow + (8 * h + r) * KP, pk4(cm2(Pc, E[r])));
    Pc = cm2(Pc, G);
  }
#pragma unroll
  for (int s = 0; s < 8; ++s) T0 = mf16(ldsr<u32x4>(rfr + 64 * s), Yf[s], T0);
  glds1(ug, A_RAW + wv * 4096 + 3072, uo + 192 * 1152);
  u32x2 keep[4];
#pragma unroll
  for (int h = 0; h < 2; ++h) {
    keep[2 * h] = pk4(cm2(Pc, E[4])); keep[2 * h + 1] = pk4(cm2(Pc, E8));
    ldsw(wrow + (8 * h) * KP, pk4(Pc)); ldsw(wrow + (8 * h + 1) * KP, pk4(cm2(Pc, E[2])));
    ldsw(wrow + (8 * h + 4) * KP, keep[2 * h]); ldsw(wrow + (8 * h + 5) * KP, pk4(cm2(Pc, E[6])));
    Pc = cm2(Pc, G);
  }
#pragma unroll
  for (int g = 0; g < 4; ++g) {
    ldsw(wrow + (4 * g + 2) * KP, pk4(Pc)); ldsw(wrow + (4 * g + 3) * KP, pk4(cm2(Pc, E[4])));
    Pc = cm2(Pc, G);
  }
#pragma unroll
  for (int s = 0; s < 8; ++s) T1 = mf16(ldsr<u32x4>(rfr + 64 * s), Yf[s], T1);
#pragma unroll
  for (int g = 0; g < 8; ++g) { ldsw(wrow + (4 * (g >> 1) + (g & 1)) * KP, pk4(Pc)); Pc = cm2(Pc, G); }
  ldsw(wrow + 2 * KP, pk4(Pc));
#pragma unroll
  for (int g = 0; g < 4; ++g) ldsw(wrow + (4 * g + 3) * KP, keep[g]);
#pragma unroll
  for (int s = 0; s < 8; ++s) T2 = mf16(ldsr<u32x4>(rfr + 64 * s), Yf[s], T2);
  ST(3);
  asm volatile("s_waitcnt vmcnt(0)" ::: "memory");
  ST(4);
  {
    const u32 ra = A_RAW + wv * 4096 + (8 * q + 4 * ((j & 3) >> 1) + (j >> 2)) * 16 + (j & 1) * 8;
    const u32 wa = A_UT + (j & 7) * UT_STR + (256 + 256 * wv + 8 * q + 4 * (j >> 3)) * 2;
#pragma unroll
    for (int i = 0; i < 8; ++i) {
      s16x4 v = __builtin_amdgcn_ds_read_tr16_b64_v4i16((LAS s16x4*)(size_t)(ra + i * 512));
      ldsw(wa + i * 64, v);
    }
  }
  const u32 c3 = kb + 3 * CPS;
  ldsw(c3 + lane * 16, u32x4{0u, 0u, 0u, 0u}); ldsw(c3 + 1024 + lane * 16, u32x4{0u, 0u, 0u, 0u});
  {
    const int l0 = 128 * (q >> 1) + 4 * (q & 1) + 8 * j;
    ldsw(c3 + (544 - l0) * 2, u32x2{pack2(T0[3], T0[2]), pack2(T0[1], lane == 0 ? T0[0] + dsk : T0[0])});
    ldsw(c3 + (288 - l0) * 2, u32x2{pack2(T2[3], T1[1]), pack2(T1[0], T1[0])});
    if (lane == 63) ldsw(c3 + 64, u32x2{pack2(0.f, T2[3]), pack2(T2[3], T2[3])});
    const int mu0 = 32 + 8 * q + (j >> 1), od = j & 1;
    u32 p2 = pack2(lane == 0 ? T2[0] : T1[2], T1[3]);
    u32 p3 = pack2(lane == 0 ? T2[2] : T2[0], T2[1]);
    const int ia = lane == 0 ? 647 + 204 - 63 : (od ? 647 + 68 - mu0 : 647 + 204 + 1 - mu0);
    ldsw(c3 + ia * 2, (u16)p2);
    ldsw(c3 + (647 + 136 * od - mu0) * 2, (u16)(p2 >> 16));
    const int i3 = (od ? 967 - 64 : 967 + 84 - 63) - 16 * q - (j >> 1);
    ldsw(c3 + (lane == 0 ? 967 + 84 - 127 : i3) * 2, (u16)p3);
    ldsw(c3 + (i3 - 8) * 2, (u16)(p3 >> 16));
    const u32 k1024 = __builtin_amdgcn_readfirstlane(p2) & 0xffffu;
#pragma unroll
    for (int i = 0; i < 4; ++i) afx[i] *= k1024;
#pragma unroll
    for (int h = 0; h < 2; ++h) {
      u32x4 x = ldsr<u32x4>(c3 + h * 1024 + lane * 16); u32x2 y = ldsr<u32x2>(c3 + h * 1024 + lane * 16 + 16);
      u32 a0 = __builtin_amdgcn_alignbit(x.y, x.x, 16), a1 = __builtin_amdgcn_alignbit(x.z, x.y, 16), a2 = __builtin_amdgcn_alignbit(x.w, x.z, 16),
          a3 = __builtin_amdgcn_alignbit(y.x, x.w, 16), a4 = __builtin_amdgcn_alignbit(y.y, y.x, 16);
      ldsw(kb + 2 * CPS + h * 1024 + lane * 16, u32x4{a0, a1, a2, a3});
      ldsw(kb + 1 * CPS + h * 1024 + lane * 16, u32x4{x.y, x.z, x.w, y.x});
      ldsw(kb + h * 1024 + lane * 16, u32x4{a1, a2, a3, a4});
    }
  }
  u32x4 Af[17], t2[3][2], t3[4];
  {
    const u32 fb = kb + (j & 3) * CPS - 8 * (j >> 2);
    const u32 fa = fb + 16 * q + (544 - 16 - 512) * 2;
#pragma unroll
    for (int n = 0; n < 17; ++n) Af[n] = rd2(fa + (16 - n) * 64, fa + (16 - n) * 64 + 8);
    const u32 ft = fb + 8 * q;
#pragma unroll
    for (int D = 2; D < 5; ++D) { u32 a = ft + (644 - 16 * D) * 2; t2[D - 2][0] = rd2(a, a + 136); t2[D - 2][1] = rd2(a + 272, a + 408); }
#pragma unroll
    for (int D = 4; D < 8; ++D) { u32 a = ft + (964 - 16 * D) * 2; t3[D - 4] = rd2(a, a + 168); }
  }
  ST(5);
  bar();
  ST(6);
  f32x4 acc[8];
#pragma unroll
  for (int i = 0; i < 8; ++i) acc[i] = f32x4{0.f, 0.f, 0.f, 0.f};
  const f32x4 zz = {0.f, 0.f, 0.f, 0.f};
  const u32 ub = ut + (16 * j + 8 * q) * 2;
  const u32 yb = ut + (16 * j + 4 * q) * 2;
#define BADDR(k) (ub + (256 + 256 * ((k) >> 3) - 16 - 32 * ((k) & 7)) * 2)
  constexpr int NB = 4;
  u32x4 Bf[NB], W1;
#pragma unroll
  for (int k = 0; k < NB - 1; ++k) Bf[k] = ldsr<u32x4>(BADDR(k));
  W1 = ldsr<u32x4>(ub + (256 - 24) * 2);
  f32x4 V[6]; u32x4 vp[3];
  __amdgpu_buffer_rsrc_t orr = __builtin_amdgcn_make_buffer_rsrc(P.out, 0, 2048 * 1152 * 4, 0x00020000);
  const u32 ora = A_UT + (4 * (q & 1) + (j >> 2)) * UT_STR + (32 * wv + 16 * (q >> 1) + 4 * (j & 3)) * 2;
  const u32 ov = ((32 * wv + 16 * (q >> 1) + j) * 1152 + d0 + 4 * (q & 1)) * 4;
  u32x2 ovv;
#define YSTAGE(t) ldsw(yb + (256 * (t)) * 2, u32x2{pack2(acc[t][0], acc[t][1]), pack2(acc[t][2], acc[t][3])})
#define YREAD(t) ovv = __builtin_bit_cast(u32x2, __builtin_amdgcn_ds_read_tr16_b64_v4i16((LAS s16x4*)(size_t)(ora + (t) * 512)))
#define YSTORE(t) __builtin_amdgcn_raw_buffer_store_b128(u32x4{ovv.x << 16, ovv.x & 0xffff0000u, ovv.y << 16, ovv.y & 0xffff0000u}, orr, ov, (t) * 256 * 4608, OAUXA)
#pragma unroll
  for (int e = 0; e < 8; ++e) {
#pragma unroll
    for (int b = 0; b < 8; ++b) {
      const int k = 8 * e + b;
      if (b == 3 && e > 0) { bar(); YREAD(e - 1); }
      if (k + NB - 1 < 64) Bf[(k + NB - 1) % NB] = ldsr<u32x4>(BADDR(k + NB - 1));
      if (b == 6 && e < 5) W1 = ldsr<u32x4>(ub + (256 + 256 * (e + 1) - 24) * 2);
      const u32x4 B = Bf[k % NB];
      acc[e] = mf16(Af[b], B, acc[e]);
      if (e < 7) acc[e + 1] = mf16(Af[8 + b], B, acc[e + 1]);
      if (b == 0) {
        if (e < 6) { V[0] = mf16(B, bnd[0], zz); V[1] = mf16(B, bnd[1], zz); V[2] = mf16(B, bnd[2], zz); V[3] = mf16(W1, bnd[3], zz); }
        if (e < 4) { V[4] = mf16(B, bnd[4], zz); V[5] = mf16(W1, bnd[5], zz); }
        if (e < 6) acc[e + 2] = mf16(Af[16], B, acc[e + 2]);
        if (e < 4) acc[e + 4] = mf16(afx, B, acc[e + 4]);
        if (e > 0) YSTAGE(e - 1);
      }
      if (b == 1 && e < 6) {
#pragma unroll
        for (int p = 0; p < 3; ++p) if (p < 2 || e < 4)
          vp[p] = u32x4{pack2(V[2 * p][0], V[2 * p][1]), pack2(V[2 * p][2], V[2 * p][3]), pack2(V[2 * p + 1][0], V[2 * p + 1][1]), pack2(V[2 * p + 1][2], V[2 * p + 1][3])};
      }
      if (b >= 2 && b <= 4 && e + b < 8) { acc[e + b] = mf16(vp[0], t2[b - 2][0], acc[e + b]); acc[e + b] = mf16(vp[1], t2[b - 2][1], acc[e + b]); }
      if (b >= 4 && e + b < 8) acc[e + b] = mf16(vp[2], t3[b - 4], acc[e + b]);
      if (b == 4 && e > 0) YSTORE(e - 1);
      __builtin_amdgcn_sched_barrier(0);
    }
  }
  YSTAGE(7);
  ST(7);
  bar();
  ST(8);
  YREAD(7); YSTORE(7);
  ST(9);
  STAMP_FLUSH(P);
}

DEV void pf_role(const Params& P, int k) {
}

__global__ __launch_bounds__(512, 2) void fused(Params P) {
  const int b = blockIdx.x, xcd = b & 7, slot = b >> 3;
  u64 pc = 0;
  asm volatile("s_getpc_b64 %0" : "=s"(pc));
  if (__builtin_expect(slot < 18, 1)) {
    g_role(P, 2 * xcd + (slot >= 9), slot >= 9 ? slot - 9 : slot, pc);
  } else if (slot < 27) {
    a_role(P, 72 * xcd + 8 * (slot - 18));
  } else pf_role(P, slot - 27);
}

extern "C" void kernel_launch(void* const* d_in, const int* in_sizes, int n_in, void* d_out, int out_size, void* d_ws, size_t ws_size, hipStream_t stream) {
  Params P;
  for (int i = 0; i < 18; ++i) P.in[i] = (const u16*)d_in[i];
  P.out = (float*)d_out; P.ws = (char*)d_ws;
  (void)hipFuncSetAttribute((const void*)fused, hipFuncAttributeMaxDynamicSharedMemorySize, LDS_BYTES);
#define GRID 256
  fused<<<GRID, 512, LDS_BYTES, stream>>>(P);
}
```

```cpp
#define CODEPF 3 XROT=2 OAUX=16 OAUXA=0 ASLP=14 BSLP=3 HS0=8 HS=3 CPF=4 CNS=9 PBAR=1
#define WDELAY 200
#define DEEP 8
#include <hip/hip_runtime.h>
#include <stdint.h>

typedef unsigned short u16; typedef unsigned u32; typedef unsigned long long u64;
typedef __bf16 bf16x8 __attribute__((ext_vector_type(8)));
typedef __bf16 bf16x2 __attribute__((ext_vector_type(2)));
typedef short s16x4 __attribute__((ext_vector_type(4)));
typedef float f32x16 __attribute__((ext_vector_type(16)));
typedef float f32x4 __attribute__((ext_vector_type(4)));
typedef float f32x2 __attribute__((ext_vector_type(2)));
typedef u32 u32x4 __attribute__((ext_vector_type(4)));
typedef u32 u32x2 __attribute__((ext_vector_type(2)));
#define LAS __attribute__((address_space(3)))
#define GAS __attribute__((address_space(1)))
#define DEV __device__ __forceinline__

struct Params { const u16* in[18]; float* out; char* ws; };
typedef u64 u64x8 __attribute__((ext_vector_type(8)));
typedef u64 u64x4 __attribute__((ext_vector_type(4)));
struct KA { u64x8 a, b; u64x4 c; };
#define KWAIT(k) asm volatile("s_waitcnt lgkmcnt(0)" :: "s"(k.a), "s"(k.b), "s"(k.c) : "memory")
__device__ __forceinline__ u64 kraw(const KA& k, int i) { return i < 8 ? k.a[i & 7] : i < 16 ? k.b[i & 7] : k.c[i & 3]; }
__device__ __forceinline__ char* kptr(const KA& k, int i) { char* p; asm volatile("s_mov_b64 %0, %1" : "=s"(p) : "s"(kraw(k, i))); return p; }

constexpr u32 WS_EPOCH = 0, WS_FLAG = 4096, WS_GRAN = 65536, WS_H = 262144;
constexpr u32 H_BYTES = 16u * 4u * 72u * 1024u;
constexpr u32 LDS_BYTES = 163840, SCR = 147456;
constexpr int SPIN_MAX = 1 << 16;
#define NWG 5
#define HS 1
#define LAUX 16
#define WAUX 16
#define OAUX 0
#define OAUXA OAUX

#define STAMP_DECL
#define ST(i)
#define ST0()
#define ST0F()
#define STAMP_FLUSH(P)

DEV float bf2f(u16 v) { return __uint_as_float(((u32)v) << 16); }
DEV u32 pack2(float a, float b) { bf16x2 r; r[0] = (__bf16)a; r[1] = (__bf16)b; return __builtin_bit_cast(u32, r); }
DEV float rbf(float f) { return (float)(__bf16)f; }
DEV void bar() { asm volatile("s_waitcnt lgkmcnt(0)\n\ts_barrier" ::: "memory"); }
DEV u32 mix32(u32 x) { x *= 0x9E3779B1u; x ^= x >> 15; x *= 0x85EBCA77u; x ^= x >> 13; return x + 0x632BE5ABu; }
DEV f32x2 cmul(f32x2 a, f32x2 b) { return f32x2{a.x * b.x - a.y * b.y, a.x * b.y + a.y * b.x}; }
DEV f32x2 cfma(f32x2 a, f32x2 b, f32x2 c) { return f32x2{fmaf(a.x, b.x, fmaf(-a.y, b.y, c.x)), fmaf(a.x, b.y, fmaf(a.y, b.x, c.y))}; }
DEV f32x2 cexp(float re, float im) {
  float e = __builtin_amdgcn_exp2f(re * 1.44269504088896341f);
  float r = __builtin_amdgcn_fractf(im * 0.15915494309189535f);
  return f32x2{e * __builtin_amdgcn_cosf(r), e * __builtin_amdgcn_sinf(r)};
}
DEV void glds4(const char* gb, u32 ldsb, u32 v0, u32 v1, u32 v2, u32 v3) {
  u32 keep;
  asm volatile("s_mov_b32 %0, m0\n\ts_mov_b32 m0, %2\n\ts_nop 0\n\t"
               "global_load_lds_dwordx4 %3, %1\n\tglobal_load_lds_dwordx4 %4, %1 offset:1024\n\t"
               "global_load_lds_dwordx4 %5, %1 offset:2048\n\tglobal_load_lds_dwordx4 %6, %1 offset:3072\n\ts_mov_b32 m0, %0"
               : "=&s"(keep) : "s"(gb), "s"(ldsb), "v"(v0), "v"(v1), "v"(v2), "v"(v3) : "memory");
}
DEV void glds1(const char* gb, u32 ldsb, u32 v0) {
  u32 keep;
  asm volatile("s_mov_b32 %0, m0\n\ts_mov_b32 m0, %2\n\ts_nop 0\n\tglobal_load_lds_dwordx4 %3, %1\n\ts_mov_b32 m0, %0"
               : "=&s"(keep) : "s"(gb), "s"(ldsb), "v"(v0) : "memory");
}
template <class T> DEV T ldsr(u32 a) { return *(const LAS T*)(size_t)a; }
template <class T> DEV void ldsw(u32 a, T v) { *(LAS T*)(size_t)a = v; }
DEV f32x2 pr(const f32x16& a, int m) { return f32x2{a[2 * m], a[2 * m + 1]}; }

#define RA 1
DEV void swap16(float x, float& lo, float& hi) { auto r = __builtin_amdgcn_permlane16_swap(__float_as_uint(x), __float_as_uint(x), false, false); lo = __uint_as_float(r[0]); hi = __uint_as_float(r[1]); }
DEV void swap32(float x, float& lo, float& hi) { auto r = __builtin_amdgcn_permlane32_swap(__float_as_uint(x), __float_as_uint(x), false, false); lo = __uint_as_float(r[0]); hi = __uint_as_float(r[1]); }
DEV int wrap9(int v) { return v >= 9 ? v - 9 : v; }
DEV u64 load_epoch(const char* ws) { u64 ep; asm volatile("s_load_dwordx2 %0, %1, 0x0\n\ts_waitcnt lgkmcnt(0)" : "=s"(ep) : "s"(ws) : "memory"); return ep; }
DEV void touch64(const char* gb, u32 ldsb, u32 v0) {
  u32 keep;
  asm volatile("s_mov_b32 %0, m0\n\ts_mov_b32 m0, %2\n\ts_nop 0\n\tglobal_load_lds_dword %3, %1\n\ts_mov_b32 m0, %0"
               : "=&s"(keep) : "s"(gb), "s"(ldsb), "v"(v0) : "memory");
}

DEV void touch64s(const char* gb, u32 ldsb, u32 v0) {
  u32 keep;
  asm volatile("s_mov_b32 %0, m0\n\ts_mov_b32 m0, %2\n\ts_nop 0\n\tglobal_load_lds_dword %3, %1 sc1\n\ts_mov_b32 m0, %0"
               : "=&s"(keep) : "s"(gb), "s"(ldsb), "v"(v0) : "memory");
}
#define TOUCH touch64
#define MM(h, cc, acc) __builtin_amdgcn_mfma_f32_32x32x16_bf16(h, cc, acc, 0, 0, 0)
#define SB __builtin_amdgcn_sched_barrier(0)
#define WL(n) __builtin_amdgcn_s_waitcnt(0xC07F | ((n) << 8))
constexpr u32 HREG = 131072, MARK = 0x80000000u;
DEV void g_role(KA K, int c, int j, u64 pc) {
  u32 pl = 0, pr_ = 0, pi_ = 0;
  const int lane = threadIdx.x & 63, wv = __builtin_amdgcn_readfirstlane(threadIdx.x >> 6);
  STAMP_DECL
  ST0();
  const int rot = (j + 4 * (c & 1) + XROT * (c >> 1)) % 9;
  const int l15 = lane & 15, q4 = lane >> 4;
  const int w = wv & 3, l31 = lane & 31, hh = lane >> 5;
  f32x16 acc0, acc1;
  u32 vb[4], aoff[4], ca[4];
  const char* c8;
  u64 ep;
  constexpr int PF = 8, CD = 3;
  u32x4 hd[32];
  u32 ht[4], csn[4];
  u32x4 hf[PF];
  bf16x8 cf[CD + 1][2];
#define CADDR(k, h) (ca[(k) & 3] + (((k) >> 2) & 1) * 8192 + (h) * 4096)
#define PIN(x) asm volatile("" : "+s"(x))
#define HLOAD(o, k) __builtin_amdgcn_raw_buffer_load_b128(hr, ho + ((k) & 3) * 1024, (o) + (((k) >> 2) & 1) * 4096, LAUX)
  const int grp = wv >> 2;
  u32 he, hc, hn, cs, cl, l8, ho, ho4;
#define PINS() do { he = (c * 4 + w) * 72 * 1024; hc = he + wrap9(j + 1 + grp) * 8192; hn = he + wrap9(j + 3 + grp) * 8192; he += 9 * 8192; \
    cs = (3 + grp) * 16384; cl = grp ? HREG : 16384; l8 = HREG + w * 4096; ho = lane * 16; ho4 = ho + 4096; \
    asm volatile("" : "+s"(he), "+s"(hc), "+s"(hn), "+s"(cs), "+s"(cl), "+s"(l8)); asm volatile("" : "+v"(ho), "+v"(ho4)); } while (0)
  u32 vx[4], ko0 = rot * 128, ko1 = wrap9(rot + 1) * 128, sl0 = w * 4096;
  if (wv >= 4) {
#pragma unroll
    for (int q = 0; q < 4; ++q) {
      int i = 2 * w + (q >> 1), qq = 2 * (q & 1) + (lane >> 5), r = (lane >> 3) & 3;
      int time = 32 * qq + 16 * (r & 1) + 2 * i + (r >> 1);
      u32 ch = (u32)(((lane & 7) ^ ((4 * (q & 1) + (lane >> 4)) & 7)) << 4) + 3072 - 1024 * q;
      vx[q] = (128 * c + time) * 1152 + ch;
      vb[q] = (64 * j + 32 * (w & 1) + 8 * q + (lane >> 3)) * 1152 + ch;
    }
  } else {
#pragma unroll
    for (int q = 0; q < 4; ++q) { vx[q] = (64 * j + 16 * wv + l15) * 2; vb[q] = 0; }
  }
  asm volatile("" : "+v"(vx[0]), "+v"(vx[1]), "+v"(vx[2]), "+v"(vx[3]), "+v"(vb[0]), "+v"(vb[1]), "+v"(vb[2]), "+v"(vb[3]));
  asm volatile("" : "+s"(ko0), "+s"(ko1), "+s"(sl0));
  KWAIT(K);
  ST0F();
  char* ws;
  __amdgpu_buffer_rsrc_t hr;
  if (wv < 4)
    asm volatile("global_load_ushort %0, %3, %4\n\tglobal_load_ushort %1, %3, %5\n\tglobal_load_ushort %2, %3, %6"
                 : "=&v"(pl), "=&v"(pr_), "=&v"(pi_) : "v"(vx[0]), "s"(kraw(K, 16)), "s"(kraw(K, 10)), "s"(kraw(K, 11)) : "memory");
  if (__builtin_expect(wv >= 4, 1)) {
    const char* xb = kptr(K, 9) - 3072;
    glds4(xb + ko0, sl0, vx[0], vx[1], vx[2], vx[3]);
    const char *b0 = kptr(K, 12), *b1 = kptr(K, 13), *bb = (w < 2 ? b0 : b1) - 3072;
    glds4(bb + ko0, sl0 + 16384, vb[0], vb[1], vb[2], vb[3]);
    glds4(xb + ko1, sl0 + 32768, vx[0], vx[1], vx[2], vx[3]);
    glds4(bb + ko1, sl0 + 49152, vb[0], vb[1], vb[2], vb[3]);
    const char *c0 = kptr(K, 14), *c1 = kptr(K, 15), *cb = (w < 2 ? c0 : c1) - 3072;
    ws = kptr(K, 19);
    hr = __builtin_amdgcn_make_buffer_rsrc(ws + WS_H, 0, H_BYTES, 0x00020000);
    ST(8);
    u32 dk0, dk1;
    asm volatile("global_load_ushort %0, %2, %3\n\tglobal_load_ushort %1, %2, %3 offset:64" : "=&v"(dk0), "=&v"(dk1) : "v"((64 * j + l31) * 2), "s"(kraw(K, 17)) : "memory");
    ST(9);
    s16x4 xs[8];
    asm volatile("s_waitcnt vmcnt(10)" ::: "memory");
    const char* ce = cb + ((w & 1) ? -32 * 1152 : 32 * 1152);
#define CPIECE(i) glds4((((i) & 1) ? cb : ce) + wrap9(j + (i)) * 128, (((i) + 2) & 7) * 16384 + w * 4096, vb[0], vb[1], vb[2], vb[3])
    int kn = wrap9(rot + 2);
#pragma unroll 1
    for (int s = 0; s < 9; ++s) {
      bar();
      if (s <= 6) {
        u32 sl = ((s + 2) & 3) * 32768 + w * 4096;
        glds4(xb + kn * 128, sl, vx[0], vx[1], vx[2], vx[3]);
        glds4(bb + kn * 128, sl + 16384, vb[0], vb[1], vb[2], vb[3]);
      } else { CPIECE(2 * s - 14); CPIECE(2 * s - 13); }
      if (wrap9(rot + s) == j) {
        const int e = (lane >> 2) & 3, pp = lane & 3;
        u32 sb = (s & 3) * 32768 + (32 * hh + 4 * w + 16 * (e >> 1) + 2 * (e & 1)) * 128 + (((2 * ((lane >> 4) & 1) + (pp >> 1)) ^ ((w << 1) | (e & 1))) << 4) + (pp & 1) * 8;
#pragma unroll
        for (int g = 0; g < 4; ++g) {
          xs[g] = __builtin_amdgcn_ds_read_tr16_b64_v4i16((LAS s16x4*)(size_t)(sb + (64 * (g & 1) + (g >> 1)) * 128));
          xs[4 + g] = __builtin_amdgcn_ds_read_tr16_b64_v4i16((LAS s16x4*)(size_t)((sb ^ 64) + (64 * (g & 1) + (g >> 1)) * 128));
        }
      }
      kn = wrap9(kn + 1);
      if (s < 8) asm volatile("s_waitcnt vmcnt(8)" ::: "memory");
    }
    bar();
    ST(3);
    asm volatile("" : "+v"(dk0), "+v"(dk1));
    const float dsk0 = __uint_as_float(dk0 << 16), dsk1 = __uint_as_float(dk1 << 16);
#pragma unroll
    for (int i = 4; i < 8; ++i) CPIECE(i);
#pragma unroll
    for (int r = 0; r < 16; ++r) {
      acc1[r] = rbf(bf2f((u16)xs[r >> 2][r & 3]) * dsk0);
      acc0[r] = rbf(bf2f((u16)xs[4 + (r >> 2)][r & 3]) * dsk1);
    }
#pragma unroll
    for (int s = 0; s < 4; ++s) aoff[s] = l31 * 128 + (((2 * s + hh) ^ ((l31 >> 1) & 7)) << 4);
    u32 ra[2];
#pragma unroll
    for (int u = 0; u < 2; ++u) {
      int row = 8 * (q4 >> 1) + 4 * u + (l15 >> 2);
      ra[u] = HREG + w * 4096 + row * 64 + (((2 * (q4 & 1) + ((l15 & 3) >> 1)) ^ ((row >> 1) & 3)) << 4) + (l15 & 1) * 8;
    }
    asm volatile("" : "+v"(acc0), "+v"(acc1), "+v"(ra[0]), "+v"(ra[1]), "+v"(aoff[0]), "+v"(aoff[1]), "+v"(aoff[2]), "+v"(aoff[3]));
#pragma unroll
    for (int s = 0; s < 4; ++s) ca[s] = aoff[s] + 2 * 16384;
    c8 = ce + wrap9(j + 8) * 128;
    asm volatile("" : "+s"(c8));
    PINS();
    ep = 0;
    if (c == 15 && j == 0 && wv == 4) {
      ep = load_epoch(ws);
      GAS u64* fp = (GAS u64*)(ws + WS_FLAG + ((lane >> 3) * 8 + ((lane & 7) < NWG - 1 ? (lane & 7) : NWG - 1)) * 128);
      const u64 ta = (u64)mix32((u32)ep) << 32, tb = (u64)mix32((u32)(ep >> 32) ^ 0x5bd1e995u) << 32;
#pragma unroll 1
      for (int it = 0; it < SPIN_MAX; ++it) {
        if (__all(__hip_atomic_load(fp, __ATOMIC_RELAXED, __HIP_MEMORY_SCOPE_AGENT) == ta && __hip_atomic_load(fp + 1, __ATOMIC_RELAXED, __HIP_MEMORY_SCOPE_AGENT) == tb)) break;
        __builtin_amdgcn_s_sleep(4);
      }
    }
    asm volatile("s_waitcnt vmcnt(0)" ::: "memory");
    ST(1);
    bar();
    ST(4);
#define hx hf
#pragma unroll
    for (int k = 0; k < 8; ++k) {
      u32x2 l2 = __builtin_bit_cast(u32x2, __builtin_amdgcn_ds_read_tr16_b64_v4i16((LAS s16x4*)(size_t)(ra[0] + (k & 3) * 1024 + (k >> 2) * 16384)));
      u32x2 h2 = __builtin_bit_cast(u32x2, __builtin_amdgcn_ds_read_tr16_b64_v4i16((LAS s16x4*)(size_t)(ra[1] + (k & 3) * 1024 + (k >> 2) * 16384)));
      hx[k] = u32x4{l2.x, l2.y, h2.x, h2.y};
      if (k < CD) { cf[k][0] = ldsr<bf16x8>(ca[k]); cf[k][1] = ldsr<bf16x8>(ca[k] + 4096); }
    }
    SB;
#pragma unroll
    for (int k = 0; k < 8; ++k) {
      bf16x8 a = __builtin_bit_cast(bf16x8, hx[k]);
      acc0 = MM(a, cf[k & 3][0], acc0);
      asm volatile("" : "+v"(acc0)); SB;
      if (k == 4) glds4(c8, l8, vb[0], vb[1], vb[2], vb[3]);
      if (k + CD < 8) cf[(k + CD) & 3][0] = ldsr<bf16x8>(CADDR(k + CD, 0));
      SB;
      acc1 = MM(a, cf[k & 3][1], acc1);
      asm volatile("" : "+v"(acc1)); SB;
      if (k + CD < 8) cf[(k + CD) & 3][1] = ldsr<bf16x8>(CADDR(k + CD, 1));
      SB;
    }
  }
  int wq = wv;
  asm volatile("" : "+s"(wq));
  if (__builtin_expect(wq < 4, 1)) {
    asm volatile("s_load_dwordx2 %0, %1, 0x0" : "=&s"(ep) : "s"(kraw(K, 19)) : "memory");
    ws = kptr(K, 19);
    hr = __builtin_amdgcn_make_buffer_rsrc(ws + WS_H, 0, H_BYTES, 0x00020000);
    if (wv == 0 && (blockIdx.x >> 3) < CODEPF) {
      touch64((const char*)(pc & ~(u64)127), SCR, (blockIdx.x >> 3) * 8192 + lane * 128);
      asm volatile("s_waitcnt vmcnt(1)" ::: "memory");
    } else asm volatile("s_waitcnt vmcnt(0)" ::: "memory");
    asm volatile("" : "+v"(pl), "+v"(pr_), "+v"(pi_) :: "memory");
    u32 hbase = (c * 4 * 72 + j * 8 + wv) * 1024;
    float dt = rbf(__builtin_amdgcn_exp2f(__uint_as_float(pl << 16) * 1.44269504088896341f));
    float Lr = __uint_as_float(pr_ << 16), Li = __uint_as_float(pi_ << 16);
    f32x2 lam = cexp(dt * Lr, dt * Li);
    float inv = 2.f / (Lr * Lr + Li * Li);
    f32x2 coef = cmul(f32x2{lam.x - 1.f, lam.y}, f32x2{Lr * inv, -Li * inv});
    f32x2 mu = cmul(lam, lam); mu = cmul(mu, mu); mu = cmul(mu, mu); mu = cmul(mu, mu);
    f32x2 nu = cmul(mu, mu), nu2 = cmul(nu, nu), La = cmul(nu2, nu2);
    f32x2 La2 = cmul(La, La), La4 = cmul(La2, La2);
    f32x2 one = {1.f, 0.f};
    f32x2 Lg = cmul((q4 & 2) ? La2 : one, (q4 & 1) ? La : one);
    f32x2 nuq = (q4 & 1) ? nu : one;
    f32x2 cm0 = cmul(coef, cmul((q4 & 2) ? nu2 : one, nuq)), cm1 = cmul(cm0, mu);
    asm volatile("s_waitcnt lgkmcnt(0)" : "+s"(ep) :: "memory");
    u32 tlo = mix32((u32)ep), thi = mix32((u32)(ep >> 32) ^ 0x5bd1e995u);
    asm volatile("" :: "s"(tlo), "s"(thi));
    asm volatile("" : "+v"(lam), "+v"(coef), "+v"(mu), "+v"(La4), "+v"(Lg), "+v"(cm0), "+v"(cm1), "+v"(nu), "+v"(nu2), "+v"(nuq));
    f32x4 ar[8], ai[8];
#pragma unroll
    for (int i = 0; i < 8; ++i) { ar[i] = f32x4{0.f, 0.f, 0.f, 0.f}; ai[i] = ar[i]; asm volatile("" : "+v"(ar[i]), "+v"(ai[i])); }
    ST(1);
    u32 xa0 = l15 * 128 + ((q4 ^ (l15 >> 1)) << 4), xa1 = xa0 ^ 64, ba0 = xa0 + 16384 + wv * 2048, ba1 = ba0 ^ 64;
    bf16x8 A[2][8], Bq[2][2];
#define STEP(cs, RD, xa, ba, G10, G11, G12, G13) do { WL(4); SB; \
      _Pragma("unroll") for (int i = 0; i < 8; ++i) { if (i == 4) { if (RD) WL(8); else WL(0); SB; } \
        ar[i] = __builtin_amdgcn_mfma_f32_16x16x32_bf16(A[cs][i], Bq[cs][0], ar[i], 0, 0, 0); asm volatile("" : "+v"(ar[i])); SB; \
        if (RD) { if (i == 0) Bq[cs ^ 1][0] = ldsr<bf16x8>(ba); else if (i < 5) A[cs ^ 1][2 * i - 2] = ldsr<bf16x8>(xa + (2 * i - 2) * 2048); } \
        if (i == 5) { G10; } if (i == 6) { G12; } SB; \
        ai[i] = __builtin_amdgcn_mfma_f32_16x16x32_bf16(A[cs][i], Bq[cs][1], ai[i], 0, 0, 0); asm volatile("" : "+v"(ai[i])); SB; \
        if (RD) { if (i == 0) Bq[cs ^ 1][1] = ldsr<bf16x8>(ba + 8192); else if (i < 5) A[cs ^ 1][2 * i - 1] = ldsr<bf16x8>(xa + (2 * i - 1) * 2048); } \
        if (i == 5) { G11; } if (i == 6) { G13; } SB; } } while (0)
#define ADV(v) do { v = (v + 32768) & 131071; asm volatile("" : "+v"(v)); } while (0)
    bar();
    ST(2);
    Bq[0][0] = ldsr<bf16x8>(ba0); Bq[0][1] = ldsr<bf16x8>(ba0 + 8192);
#pragma unroll
    for (int i = 0; i < 8; ++i) A[0][i] = ldsr<bf16x8>(xa0 + i * 2048);
    SB;
#pragma unroll 1
    for (int s = 0; s < 8; ++s) {
      STEP(0, 1, xa1, ba1, ADV(xa0), ADV(ba0), asm volatile("s_barrier" ::: "memory"), );
      STEP(1, 1, xa0, ba0, ADV(xa1), ADV(ba1), , );
    }
    STEP(0, 1, xa1, ba1, , , , bar());
    STEP(1, 0, xa0, ba0, asm volatile("s_waitcnt vmcnt(0)" ::: "memory"), , , );
    ST(3);
    const f32x2 lr2 = {lam.x, lam.x}, li2 = {lam.y, lam.y};
    f32x2 Br[16], Bi[16];
#pragma unroll
    for (int i = 0; i < 8; ++i) { Br[2 * i] = f32x2{ar[i][0], ar[i][1]}; Br[2 * i + 1] = f32x2{ar[i][2], ar[i][3]}; Bi[2 * i] = f32x2{ai[i][0], ai[i][1]}; Bi[2 * i + 1] = f32x2{ai[i][2], ai[i][3]}; }
    f32x2 sr = Br[0], si = Bi[0];
#pragma unroll
    for (int k = 1; k < 16; ++k) {
      f32x2 t1 = __builtin_elementwise_fma(lr2, sr, Br[k]), t2 = __builtin_elementwise_fma(lr2, si, Bi[k]);
      f32x2 n1 = __builtin_elementwise_fma(-li2, si, t1), n2 = __builtin_elementwise_fma(li2, sr, t2);
      sr = n1; si = n2;
    }
    const f32x2 S0 = {sr.x, si.x};
    f32x2 T = cfma(mu, S0, f32x2{sr.y, si.y}), lo, hi;
    { float a, b; swap16(T.x, a, b); lo.x = a; hi.x = b; swap16(T.y, a, b); lo.y = a; hi.y = b; }
    f32x2 U = cfma(nu, lo, hi), E = (q4 & 1) ? lo : f32x2{0.f, 0.f};
    { float a, b; swap32(U.x, a, b); lo.x = a; hi.x = b; swap32(U.y, a, b); lo.y = a; hi.y = b; }
    f32x2 tot = cfma(nu2, lo, hi);
    if (q4 == 0 && c < 15) {
      u32x4 gv = {__float_as_uint(tot.x), tlo, __float_as_uint(tot.y), thi};
      __amdgpu_buffer_rsrc_t gr = __builtin_amdgcn_make_buffer_rsrc(ws + WS_GRAN, 0, 9 * 16 * 1024, 0x00020000);
      __builtin_amdgcn_raw_buffer_store_b128(gv, gr, l15 * 16, (j * 16 + c) * 1024 + wv * 256, 16);
    }
#pragma unroll
    for (int k = 0; k < 16; ++k) asm volatile("" : "+v"(Br[k]), "+v"(Bi[k]) :: "memory");
    ST(4);
    if (q4 & 2) E = cfma(nuq, lo, E);
    f32x2 La_ = cmul(coef, E), Lb_ = cmul(coef, cfma(mu, E, S0));
    const f32x2 cr2 = {coef.x, coef.x}, ci2 = {coef.y, coef.y};
#pragma unroll
    for (int k = 0; k < 16; ++k) {
      f32x2 n1 = __builtin_elementwise_fma(-ci2, Bi[k], cr2 * Br[k]), n2 = __builtin_elementwise_fma(ci2, Br[k], cr2 * Bi[k]);
      Br[k] = n1; Bi[k] = n2;
    }
#pragma unroll
    for (int k = 0; k < 16; ++k) asm volatile("" : "+v"(Br[k]), "+v"(Bi[k]));
    asm volatile("" : "+v"(La_), "+v"(Lb_));
    const u32 wa = HREG + q4 * 4096 + wv * 1024 + l15 * 64;
    u32 wa4[4];
#pragma unroll
    for (int q = 0; q < 4; ++q) wa4[q] = wa + ((q ^ ((l15 >> 1) & 3)) << 4);
    asm volatile("" : "+v"(wa4[0]), "+v"(wa4[1]), "+v"(wa4[2]), "+v"(wa4[3]));
    u32 ra[2];
#pragma unroll
    for (int u = 0; u < 2; ++u) {
      int row = 8 * (q4 >> 1) + 4 * u + (l15 >> 2);
      ra[u] = HREG + wv * 1024 + row * 64 + (((2 * (q4 & 1) + ((l15 & 3) >> 1)) ^ ((row >> 1) & 3)) << 4) + (l15 & 1) * 8;
    }
#pragma unroll
    for (int s = 0; s < 4; ++s) aoff[s] = l31 * 128 + (((2 * s + hh) ^ ((l31 >> 1) & 7)) << 4);
    c8 = ws;
#pragma unroll
    for (int r = 0; r < 16; ++r) { acc0[r] = 0.f; acc1[r] = 0.f; }
    asm volatile("" : "+v"(ra[0]), "+v"(ra[1]), "+v"(aoff[0]), "+v"(aoff[1]), "+v"(aoff[2]), "+v"(aoff[3]), "+v"(acc0), "+v"(acc1));
    PINS(); PIN(hbase);
    ST(8);
    f32x2 H;
    {
      const char* gb = ws;
      u32 go[4];
      const u32 fo = WS_FLAG + ((c >> 1) * 8 + ((lane & 7) < NWG - 1 ? (lane & 7) : NWG - 1)) * 128;
#pragma unroll
      for (int n = 0; n < 4; ++n) { int cs = c - 1 - q4 - 4 * n; go[n] = cs >= 0 ? WS_GRAN + j * 16384 + wv * 256 + cs * 1024 + l15 * 16 : fo; }
      u32x4 g0, g1, g2, g3;
#pragma unroll 1
      for (int it = 0; it < SPIN_MAX; ++it) {
        asm volatile("global_load_dwordx4 %0, %4, %8 sc1\n\tglobal_load_dwordx4 %1, %5, %8 sc1\n\tglobal_load_dwordx4 %2, %6, %8 sc1\n\tglobal_load_dwordx4 %3, %7, %8 sc1\n\t"
                     "s_waitcnt vmcnt(0)"
                     : "=&v"(g0), "=&v"(g1), "=&v"(g2), "=&v"(g3) : "v"(go[0]), "v"(go[1]), "v"(go[2]), "v"(go[3]), "s"(gb) : "memory");
        u32 bad = (((g0.y ^ tlo) | (g1.y ^ tlo)) | ((g2.y ^ tlo) | (g3.y ^ tlo))) | (((g0.w ^ thi) | (g1.w ^ thi)) | ((g2.w ^ thi) | (g3.w ^ thi)));
        if (!__any(bad != 0)) break;
        __builtin_amdgcn_s_sleep(HS);
      }
      f32x2 s3 = {__uint_as_float(g3.x), __uint_as_float(g3.z)}, s2 = {__uint_as_float(g2.x), __uint_as_float(g2.z)};
      f32x2 s1 = {__uint_as_float(g1.x), __uint_as_float(g1.z)}, s0 = {__uint_as_float(g0.x), __uint_as_float(g0.z)};
      H = cmul(Lg, cfma(La4, cfma(La4, cfma(La4, s3, s2), s1), s0));
      float a, b;
      swap16(H.x, a, b); H.x = a + b; swap16(H.y, a, b); H.y = a + b;
      swap32(H.x, a, b); H.x = a + b; swap32(H.y, a, b); H.y = a + b;
    }
    ST(5);
    f32x2 sa = cfma(cm0, H, La_), sb_ = cfma(cm1, H, Lb_);
    sr = f32x2{sa.x, sb_.x}; si = f32x2{sa.y, sb_.y};
#define HPK(g, hf) u32x4{HP1(g, 8 * (hf)), HP1(g, 8 * (hf) + 2), HP1(g, 8 * (hf) + 4), HP1(g, 8 * (hf) + 6)}
#define HP1(g, k) (((g) >> 1) ? pack2(-Bi[k][(g) & 1], -Bi[(k) + 1][(g) & 1]) : pack2(Br[k][(g) & 1], Br[(k) + 1][(g) & 1]))
    u32x4 hv[8];
#pragma unroll
    for (int k = 0; k < 16; ++k) {
      f32x2 t1 = __builtin_elementwise_fma(lr2, sr, Br[k]), t2 = __builtin_elementwise_fma(lr2, si, Bi[k]);
      f32x2 n1 = __builtin_elementwise_fma(-li2, si, t1), n2 = __builtin_elementwise_fma(li2, sr, t2);
      sr = n1; si = n2; Br[k] = sr; Bi[k] = si;
      if (k >= 8 && !(k & 1)) { const int g = (k - 8) >> 1; SB; hv[g] = HPK(g, 0); ldsw(wa4[2 * (g & 1)] + (g >> 1) * 16384, hv[g]); SB; }
    }
#pragma unroll
    for (int g = 0; g < 4; ++g) { hv[4 + g] = HPK(g, 1); asm volatile("" : "+v"(hv[4 + g])); ldsw(wa4[2 * (g & 1) + 1] + (g >> 1) * 16384, hv[4 + g]); SB; }
    asm volatile("" :: "v"(hv[0]), "v"(hv[1]), "v"(hv[2]), "v"(hv[3]), "v"(hv[4]), "v"(hv[5]), "v"(hv[6]), "v"(hv[7]));
#pragma unroll
    for (int q = 0; q < 8; ++q) {
      u32x2 l2 = __builtin_bit_cast(u32x2, __builtin_amdgcn_ds_read_tr16_b64_v4i16((LAS s16x4*)(size_t)(ra[0] + (q & 3) * 4096 + (q >> 2) * 16384)));
      u32x2 h2 = __builtin_bit_cast(u32x2, __builtin_amdgcn_ds_read_tr16_b64_v4i16((LAS s16x4*)(size_t)(ra[1] + (q & 3) * 4096 + (q >> 2) * 16384)));
      hv[q] = u32x4{l2.x, l2.y, h2.x, h2.y}; SB;
    }
#pragma unroll
    for (int q = 0; q < 8; ++q) {
      hv[q].x = (int)hv[q].x > (int)(MARK + 1) ? hv[q].x : MARK + 1;
      __builtin_amdgcn_raw_buffer_store_b128(hv[q], hr, ho, hbase + ((q & 3) * 72 + (q >> 2) * 4) * 1024, 16); SB;
    }
    ST(6);
    bar();
  }
  ST(10);
  {
#define SLP 4
#define BARI 19
#define NOCHK 0
    u32 hp = 0, cp = 0;
#define CNT()
#define RST()
#define IMIN(a, b) ((int)(a) < (int)(b) ? (a) : (b))
#define ANY7(t, o) __any(IMIN(IMIN(IMIN(t[o].x, t[o + 1].x), IMIN(t[o + 2].x, t[o + 3].x)), IMIN(IMIN(t[o + 4].x, t[o + 5].x), t[o + 6].x)) == MARK)
#define DLOAD(i) __builtin_amdgcn_raw_buffer_load_b128(hr, (((i) & 4) ? ho4 : ho) + ((i) & 3) * 1024, ht[(i) >> 3], LAUX)
    ht[0] = hc; ht[1] = hn;
    ht[2] = hn + 16384; ht[2] = ht[2] >= he ? ht[2] - 9 * 8192 : ht[2];
    ht[3] = ht[2] + 16384; ht[3] = ht[3] >= he ? ht[3] - 9 * 8192 : ht[3];
#pragma unroll
    for (int i = 0; i < DEEP; ++i) { hd[i] = DLOAD(i); SB; }
#pragma unroll
    for (int q = 0; q < CD; ++q) { ca[q] = aoff[q] + cs; cf[q][0] = ldsr<bf16x8>(CADDR(q, 0)); cf[q][1] = ldsr<bf16x8>(CADDR(q, 1)); }
    ca[3] = aoff[3] + cs;
    SB;
    ST(11);
    u32 xa_ = (grp ? HREG + 16384 : 2 * 16384) + w * 4096 + lane * 16, xb_ = (grp ? 2 * 16384 : HREG + 16384) + w * 4096 + lane * 16;
    u32 ov = ((128 * c + 32 * w + 4 * hh) * 1152 + 576 + 64 * j + 32 * grp + l31) * 4;
    asm volatile("" : "+v"(ov), "+v"(xa_), "+v"(xb_));
#define FRESH(i) do { if (!NOCHK && __builtin_expect(__any(hd[i].x == MARK), 0)) { CNT(); \
        _Pragma("unroll 1") for (int it = 0; it < SPIN_MAX; ++it) { __builtin_amdgcn_s_sleep(SLP); hd[i] = DLOAD(i); if (!__any(hd[i].x == MARK)) break; } } } while (0)
    FRESH(0);
#pragma unroll
    for (int i = 0; i < 32; ++i) {
      const int k = i & 7, n = i >> 3, kn2 = k + CD; const bool NX = n < 3;
      bf16x8 a = __builtin_bit_cast(bf16x8, hd[i]);
      acc0 = MM(a, cf[k & 3][0], acc0);
      asm volatile("" : "+v"(acc0)); SB;
      if (kn2 >= 8 && NX) ca[kn2 & 3] = aoff[kn2 & 3] + cs;
      if (k == 0 && n > 0) ca[3] = aoff[3] + cs;
      if (kn2 < 8 || NX) cf[kn2 & 3][0] = ldsr<bf16x8>(CADDR(kn2, 0));
      if (i + DEEP < 32) hd[i + DEEP] = DLOAD(i + DEEP);
      SB;
      acc1 = MM(a, cf[k & 3][1], acc1);
      asm volatile("" : "+v"(acc1)); SB;
      if (kn2 < 8 || NX) cf[kn2 & 3][1] = ldsr<bf16x8>(CADDR(kn2, 1));
      if (NX && k == 1) { cs = n == 2 ? cl : (cs + 32768) & 131071; PIN(cs); }
      if (i == BARI) asm volatile("s_barrier" ::: "memory");
      if (k == 7 && NX) RST();
      SB;
      if (i < 31) FRESH(i + 1);
      SB;
    }
    ST(12);
    __amdgpu_buffer_rsrc_t orr = __builtin_amdgcn_make_buffer_rsrc(kptr(K, 18), 0, 2048 * 1152 * 4, 0x00020000);
#pragma unroll
    for (int q = 0; q < 4; ++q) ldsw(xa_ + q * 1024, f32x4{acc1[4 * q], acc1[4 * q + 1], acc1[4 * q + 2], acc1[4 * q + 3]});
    bar();
    if (c == 15 && j == 0 && threadIdx.x == 256)
      __hip_atomic_store((GAS u64*)(ws + WS_EPOCH), ((u64)((u32)(ep >> 32) + 1u) << 32) | (u32)((u32)ep + 1u), __ATOMIC_RELAXED, __HIP_MEMORY_SCOPE_AGENT);
#pragma unroll
    for (int q = 0; q < 4; ++q) {
      f32x4 o = ldsr<f32x4>(xb_ + q * 1024);
#pragma unroll
      for (int e = 0; e < 4; ++e) acc1[4 * q + e] = acc0[4 * q + e] + o[e];
    }
    asm volatile("" : "+v"(acc1));
#pragma unroll
    for (int r = 0; r < 16; ++r) __builtin_amdgcn_raw_buffer_store_b32(__float_as_uint(acc1[r]), orr, ov, (8 * (r >> 2) + (r & 3)) * 4608, OAUX);
    ST(13);
    ST(7);
  }
  STAMP_FLUSH(P);
}

struct C2 { f32x2 r, i; };
DEV C2 cm2(const C2& a, const C2& b) {
  C2 o; o.r = __builtin_elementwise_fma(-a.i, b.i, a.r * b.r); o.i = __builtin_elementwise_fma(a.i, b.r, a.r * b.i); return o;
}
DEV C2 cexp2(f32x2 re, f32x2 im) { f32x2 a = cexp(re.x, im.x), b = cexp(re.y, im.y); C2 o; o.r = f32x2{a.x, b.x}; o.i = f32x2{a.y, b.y}; return o; }
DEV u32x2 pk4(const C2& a) { return u32x2{pack2(a.r.x, a.r.y), pack2(a.i.x, a.i.y)}; }
DEV f32x2 up2(u32 v) { return f32x2{__uint_as_float(v << 16), __uint_as_float(v & 0xffff0000u)}; }
DEV f32x4 mf16(u32x4 a, u32x4 b, f32x4 c) { return __builtin_amdgcn_mfma_f32_16x16x32_bf16(__builtin_bit_cast(bf16x8, a), __builtin_bit_cast(bf16x8, b), c, 0, 0, 0); }
DEV u32x4 bandop(int x, int lo, int hi, u32 mul) {
  u32 m = (u32)((((1ull << (hi - lo + 1)) - 1) << 32) >> (32 + hi - x)) & 0xffu;
  u32 sp = m | (m << 15);
  u32x4 o;
#pragma unroll
  for (int i = 0; i < 4; ++i) o[i] = ((sp >> (2 * i)) & 0x10001u) * mul;
  return o;
}
DEV u32x4 rd2(u32 a0, u32 a1) {
  u32x2 x = *(volatile const LAS u32x2*)(size_t)a0, y = *(volatile const LAS u32x2*)(size_t)a1; return u32x4{x.x, x.y, y.x, y.y};
}
constexpr u32 A_RAW = 0, A_UT = 32768, UT_STR = 4640, A_KB = A_UT + 8 * UT_STR, KB_STR = 8704, KP = 544, CPS = 2112;

DEV void a_role(KA K, int d0) {
  const int lane = threadIdx.x & 63, wv = __builtin_amdgcn_readfirstlane(threadIdx.x >> 6);
  const int d = d0 + wv, j = lane & 15, q = lane >> 4;
  STAMP_DECL
  ST0();
  u32 pAr, pAi, pBr, pBi, pCr, pCi, pdt, pds;
  u32 vo = d * 256 + lane * 4, vd = d * 2, uo = (512 * wv + lane) * 1152 + d0 * 2, ul = A_RAW + wv * 8192;
  asm volatile("" : "+v"(vo), "+v"(vd), "+v"(uo));
  asm volatile("" : "+s"(ul));
  KWAIT(K);
  ST0F();
  asm volatile("global_load_dword %0, %8, %10\n\tglobal_load_dword %1, %8, %11\n\tglobal_load_ushort %6, %9, %16\n\tglobal_load_dword %2, %8, %12\n\t"
               "global_load_dword %3, %8, %13\n\tglobal_load_dword %4, %8, %14\n\tglobal_load_dword %5, %8, %15\n\tglobal_load_ushort %7, %9, %17"
               : "=&v"(pAr), "=&v"(pAi), "=&v"(pBr), "=&v"(pBi), "=&v"(pCr), "=&v"(pCi), "=&v"(pdt), "=&v"(pds)
               : "v"(vo), "v"(vd), "s"(kraw(K, 1)), "s"(kraw(K, 2)), "s"(kraw(K, 3)), "s"(kraw(K, 4)), "s"(kraw(K, 5)), "s"(kraw(K, 6)), "s"(kraw(K, 7)), "s"(kraw(K, 8)) : "memory");
  const char* ug = kptr(K, 0);
  char* const outp = kptr(K, 18);
#define UGET(i) glds1(ug + (i) * (64 * 1152), ul + (i) * 1024, uo)
#define U0 4
  if (wv < 4) { _Pragma("unroll") for (int i = 0; i < U0; ++i) UGET(i); }
  u32x4 bnd[6];
  bnd[0] = bandop(j + 16 - 8 * q, 3, 5, 0x3f80); bnd[1] = bandop(j + 16 - 8 * q, 6, 10, 0x3f80); bnd[2] = bandop(j + 16 - 8 * q, 11, 13, 0x3f80);
  bnd[3] = bandop(j + 24 - 8 * q, 14, 18, 0x3f80); bnd[4] = bandop(j + 16 - 8 * q, 5, 11, 0x3f80); bnd[5] = bandop(j + 24 - 8 * q, 12, 20, 0x3f80);
  u32x4 afx = bandop(j + 16 - 8 * q, 3, 4, 1);
  const u32 ut = A_UT + wv * UT_STR, kb = A_KB + wv * KB_STR;
  ldsw(ut + lane * 8, u32x2{0u, 0u});
#pragma unroll
  for (int i = 0; i < 6; ++i) asm volatile("" : "+v"(bnd[i]));
  asm volatile("" : "+v"(afx));
  ST(1);
  if (wv < 4) asm volatile("s_waitcnt vmcnt(%0)" :: "n"(U0) : "memory"); else asm volatile("s_waitcnt vmcnt(0)" ::: "memory");
  asm volatile("" : "+v"(pAr), "+v"(pAi), "+v"(pBr), "+v"(pBi), "+v"(pCr), "+v"(pCi), "+v"(pdt), "+v"(pds) :: "memory");
  ST(2);
  const float dsk = __uint_as_float(pds << 16);
  const float dt = rbf(__builtin_amdgcn_exp2f(__uint_as_float(pdt << 16) * 1.44269504088896341f));
  const f32x2 dt2 = {dt, dt};
  const f32x2 are = dt2 * up2(pAr), aim = dt2 * up2(pAi);
  C2 W; { C2 B = {dt2 * up2(pBr), dt2 * up2(pBi)}, Cc = {up2(pCr), up2(pCi)}; W = cm2(Cc, B); W.i = -W.i; }
  const u32 wrow = kb + lane * 8, rfr = kb + j * KP + q * 16;
  u32x4 Yf[8];
  C2 E8 = cexp2(8.f * are, 8.f * aim);
  {
    C2 Y[16];
    Y[1] = E8;
    ldsw(wrow, u32x2{0x3f803f80u, 0u});
    ldsw(wrow + KP, pk4(Y[1]));
#pragma unroll
    for (int i = 2; i < 16; ++i) { Y[i] = (i & 1) ? cm2(Y[i - 1], Y[1]) : cm2(Y[i / 2], Y[i / 2]); ldsw(wrow + i * KP, pk4(Y[i])); }
#pragma unroll
    for (int s = 0; s < 8; ++s) Yf[s] = ldsr<u32x4>(rfr + 64 * s);
  }
#define U1 6
  if (wv < 4) { _Pragma("unroll") for (int i = U0; i < U1; ++i) UGET(i); }
  E8.i = -E8.i;
  C2 E[8];
  E[1] = cexp2(are, -aim);
  const C2 G = cexp2(128.f * are, -128.f * aim);
  E[2] = cm2(E[1], E[1]); E[3] = cm2(E[2], E[1]); E[4] = cm2(E[2], E[2]); E[5] = cm2(E[4], E[1]); E[6] = cm2(E[4], E[2]); E[7] = cm2(E[4], E[3]);
  f32x4 T0 = {0.f, 0.f, 0.f, 0.f}, T1 = T0, T2 = T0;
  C2 Pc = W;
#pragma unroll
  for (int h = 0; h < 2; ++h) {
    ldsw(wrow + (8 * h) * KP, pk4(Pc));
#pragma unroll
    for (int r = 1; r < 8; ++r) ldsw(wrow + (8 * h + r) * KP, pk4(cm2(Pc, E[r])));
    Pc = cm2(Pc, G);
  }
#pragma unroll
  for (int s = 0; s < 8; ++s) T0 = mf16(ldsr<u32x4>(rfr + 64 * s), Yf[s], T0);
  if (wv < 4) { _Pragma("unroll") for (int i = U1; i < 8; ++i) UGET(i); }
  u32x2 keep[4];
#pragma unroll
  for (int h = 0; h < 2; ++h) {
    keep[2 * h] = pk4(cm2(Pc, E[4])); keep[2 * h + 1] = pk4(cm2(Pc, E8));
    ldsw(wrow + (8 * h) * KP, pk4(Pc)); ldsw(wrow + (8 * h + 1) * KP, pk4(cm2(Pc, E[2])));
    ldsw(wrow + (8 * h + 4) * KP, keep[2 * h]); ldsw(wrow + (8 * h + 5) * KP, pk4(cm2(Pc, E[6])));
    Pc = cm2(Pc, G);
  }
#pragma unroll
  for (int g = 0; g < 4; ++g) {
    ldsw(wrow + (4 * g + 2) * KP, pk4(Pc)); ldsw(wrow + (4 * g + 3) * KP, pk4(cm2(Pc, E[4])));
    Pc = cm2(Pc, G);
  }
#pragma unroll
  for (int s = 0; s < 8; ++s) T1 = mf16(ldsr<u32x4>(rfr + 64 * s), Yf[s], T1);
#pragma unroll
  for (int g = 0; g < 8; ++g) { ldsw(wrow + (4 * (g >> 1) + (g & 1)) * KP, pk4(Pc)); Pc = cm2(Pc, G); }
  ldsw(wrow + 2 * KP, pk4(Pc));
#pragma unroll
  for (int g = 0; g < 4; ++g) ldsw(wrow + (4 * g + 3) * KP, keep[g]);
#pragma unroll
  for (int s = 0; s < 8; ++s) T2 = mf16(ldsr<u32x4>(rfr + 64 * s), Yf[s], T2);
  ST(3);
  const u32 c3 = kb + 3 * CPS;
  ldsw(c3 + lane * 16, u32x4{0u, 0u, 0u, 0u}); ldsw(c3 + 1024 + lane * 16, u32x4{0u, 0u, 0u, 0u});
  {
    const int l0 = 128 * (q >> 1) + 4 * (q & 1) + 8 * j;
    ldsw(c3 + (544 - l0) * 2, u32x2{pack2(T0[3], T0[2]), pack2(T0[1], lane == 0 ? T0[0] + dsk : T0[0])});
    ldsw(c3 + (288 - l0) * 2, u32x2{pack2(T2[3], T1[1]), pack2(T1[0], T1[0])});
    if (lane == 63) ldsw(c3 + 64, u32x2{pack2(0.f, T2[3]), pack2(T2[3], T2[3])});
    const int mu0 = 32 + 8 * q + (j >> 1), od = j & 1;
    u32 p2 = pack2(lane == 0 ? T2[0] : T1[2], T1[3]);
    u32 p3 = pack2(lane == 0 ? T2[2] : T2[0], T2[1]);
    const int ia = lane == 0 ? 647 + 204 - 63 : (od ? 647 + 68 - mu0 : 647 + 204 + 1 - mu0);
    ldsw(c3 + ia * 2, (u16)p2);
    ldsw(c3 + (647 + 136 * od - mu0) * 2, (u16)(p2 >> 16));
    const int i3 = (od ? 967 - 64 : 967 + 84 - 63) - 16 * q - (j >> 1);
    ldsw(c3 + (lane == 0 ? 967 + 84 - 127 : i3) * 2, (u16)p3);
    ldsw(c3 + (i3 - 8) * 2, (u16)(p3 >> 16));
    const u32 k1024 = __builtin_amdgcn_readfirstlane(p2) & 0xffffu;
#pragma unroll
    for (int i = 0; i < 4; ++i) afx[i] = __umul24(afx[i], k1024);
    u32x4 x[2]; u32x2 y[2];
#pragma unroll
    for (int h = 0; h < 2; ++h) { x[h] = ldsr<u32x4>(c3 + h * 1024 + lane * 16); y[h] = ldsr<u32x2>(c3 + h * 1024 + lane * 16 + 16); }
#pragma unroll
    for (int h = 0; h < 2; ++h) {
      u32 a0 = __builtin_amdgcn_alignbit(x[h].y, x[h].x, 16), a1 = __builtin_amdgcn_alignbit(x[h].z, x[h].y, 16), a2 = __builtin_amdgcn_alignbit(x[h].w, x[h].z, 16),
          a3 = __builtin_amdgcn_alignbit(y[h].x, x[h].w, 16), a4 = __builtin_amdgcn_alignbit(y[h].y, y[h].x, 16);
      ldsw(kb + 2 * CPS + h * 1024 + lane * 16, u32x4{a0, a1, a2, a3});
      ldsw(kb + 1 * CPS + h * 1024 + lane * 16, u32x4{x[h].y, x[h].z, x[h].w, y[h].x});
      ldsw(kb + h * 1024 + lane * 16, u32x4{a1, a2, a3, a4});
    }
  }
  u32x4 Af[17], t2[3][2], t3[4];
  {
    const u32 fb = kb + (j & 3) * CPS - 8 * (j >> 2);
    const u32 fa = fb + 16 * q + (544 - 16 - 512) * 2;
#pragma unroll
    for (int n = 0; n < 17; ++n) Af[n] = rd2(fa + (16 - n) * 64, fa + (16 - n) * 64 + 8);
    const u32 ft = fb + 8 * q;
#pragma unroll
    for (int D = 2; D < 5; ++D) { u32 a = ft + (644 - 16 * D) * 2; t2[D - 2][0] = rd2(a, a + 136); t2[D - 2][1] = rd2(a + 272, a + 408); }
#pragma unroll
    for (int D = 4; D < 8; ++D) { u32 a = ft + (964 - 16 * D) * 2; t3[D - 4] = rd2(a, a + 168); }
  }
  ST(4);
  if (wv < 4) {
    asm volatile("s_waitcnt vmcnt(0)" ::: "memory");
    ST(10);
    const u32 ra = ul + (8 * q + 4 * ((j & 3) >> 1) + (j >> 2)) * 16 + (j & 1) * 8;
    const u32 wa = A_UT + (j & 7) * UT_STR + (256 + 512 * wv + 8 * q + 4 * (j >> 3)) * 2;
    s16x4 v[16];
#pragma unroll
    for (int i = 0; i < 16; ++i) v[i] = __builtin_amdgcn_ds_read_tr16_b64_v4i16((LAS s16x4*)(size_t)(ra + i * 512));
    SB;
#pragma unroll
    for (int i = 0; i < 16; ++i) ldsw(wa + i * 64, v[i]);
  }
  ST(5);
  bar();
  ST(6);
  f32x4 acc[8];
#pragma unroll
  for (int i = 0; i < 8; ++i) acc[i] = f32x4{0.f, 0.f, 0.f, 0.f};
  const f32x4 zz = {0.f, 0.f, 0.f, 0.f};
  const u32 ub = ut + (16 * j + 8 * q) * 2;
  const u32 yb = ut + (16 * j + 4 * q) * 2;
#define BADDR(k) (ub + (256 + 256 * ((k) >> 3) - 16 - 32 * ((k) & 7)) * 2)
  constexpr int NB = 4;
  u32x4 Bf[NB], W1;
#pragma unroll
  for (int k = 0; k < NB - 1; ++k) Bf[k] = ldsr<u32x4>(BADDR(k));
  W1 = ldsr<u32x4>(ub + (256 - 24) * 2);
  f32x4 V[6]; u32x4 vp[3];
  __amdgpu_buffer_rsrc_t orr = __builtin_amdgcn_make_buffer_rsrc(outp, 0, 2048 * 1152 * 4, 0x00020000);
  const u32 ora = A_UT + (4 * (q & 1) + (j >> 2)) * UT_STR + (32 * wv + 16 * (q >> 1) + 4 * (j & 3)) * 2;
  const u32 ov = ((32 * wv + 16 * (q >> 1) + j) * 1152 + d0 + 4 * (q & 1)) * 4;
  u32x2 ovv;
#define YSTAGE(t) ldsw(yb + (256 * (t)) * 2, u32x2{pack2(acc[t][0], acc[t][1]), pack2(acc[t][2], acc[t][3])})
#define YREAD(t) ovv = __builtin_bit_cast(u32x2, __builtin_amdgcn_ds_read_tr16_b64_v4i16((LAS s16x4*)(size_t)(ora + (t) * 512)))
#define YSTORE(t) __builtin_amdgcn_raw_buffer_store_b128(u32x4{ovv.x << 16, ovv.x & 0xffff0000u, ovv.y << 16, ovv.y & 0xffff0000u}, orr, ov, (t) * 256 * 4608, OAUXA)
#pragma unroll
  for (int e = 0; e < 8; ++e) {
#pragma unroll
    for (int b = 0; b < 8; ++b) {
      const int k = 8 * e + b;
      if (b == 3 && e > 0) { bar(); YREAD(e - 1); }
      if (k + NB - 1 < 64) Bf[(k + NB - 1) % NB] = ldsr<u32x4>(BADDR(k + NB - 1));
      if (b == 6 && e < 5) W1 = ldsr<u32x4>(ub + (256 + 256 * (e + 1) - 24) * 2);
      const u32x4 B = Bf[k % NB];
      acc[e] = mf16(Af[b], B, acc[e]);
      if (e < 7) acc[e + 1] = mf16(Af[8 + b], B, acc[e + 1]);
      if (b == 0) {
        if (e < 6) { V[0] = mf16(B, bnd[0], zz); V[1] = mf16(B, bnd[1], zz); V[2] = mf16(B, bnd[2], zz); V[3] = mf16(W1, bnd[3], zz); }
        if (e < 4) { V[4] = mf16(B, bnd[4], zz); V[5] = mf16(W1, bnd[5], zz); }
        if (e < 6) acc[e + 2] = mf16(Af[16], B, acc[e + 2]);
        if (e < 4) acc[e + 4] = mf16(afx, B, acc[e + 4]);
        if (e > 0) YSTAGE(e - 1);
      }
      if (b == 1 && e < 6) {
#pragma unroll
        for (int p = 0; p < 3; ++p) if (p < 2 || e < 4)
          vp[p] = u32x4{pack2(V[2 * p][0], V[2 * p][1]), pack2(V[2 * p][2], V[2 * p][3]), pack2(V[2 * p + 1][0], V[2 * p + 1][1]), pack2(V[2 * p + 1][2], V[2 * p + 1][3])};
      }
      if (b >= 2 && b <= 4 && e + b < 8) { acc[e + b] = mf16(vp[0], t2[b - 2][0], acc[e + b]); acc[e + b] = mf16(vp[1], t2[b - 2][1], acc[e + b]); }
      if (b >= 4 && e + b < 8) acc[e + b] = mf16(vp[2], t3[b - 4], acc[e + b]);
      if (b == 4 && e > 0) YSTORE(e - 1);
      __builtin_amdgcn_sched_barrier(0);
    }
  }
  YSTAGE(7);
  ST(7);
  bar();
  ST(8);
  YREAD(7); YSTORE(7);
  ST(9);

  STAMP_FLUSH(P);
}

DEV void pf_role(KA K, int k) {
  const u64 tb = __builtin_amdgcn_s_memrealtime();
  const int lane = threadIdx.x & 63, wv = __builtin_amdgcn_readfirstlane(threadIdx.x >> 6);
  const int g = blockIdx.x & 7, id = k * 8 + wv;
  STAMP_DECL
  KWAIT(K);
  char* const ws = kptr(K, 19);
  const char *c0 = kptr(K, 14), *c1 = kptr(K, 15);
  const u64 ep = load_epoch(ws);
#define CNS 9
#define CPULL() _Pragma("unroll 1") for (int i = id; i < 18 * CNS; i += 40) { \
    int pc = i + g * (2 * CNS + 2); pc = pc >= 18 * CNS ? pc - 18 * CNS : pc; \
    int m = pc >= 9 * CNS, q = pc - 9 * CNS * m, jj = q / CNS, e = (q - jj * CNS) * 64 + lane, row = e / CNS, sl = e - row * CNS + jj + 9 - CNS; \
    sl = sl >= 9 ? sl - 9 : sl; \
    TOUCH((m ? c1 : c0) + jj * (64 * 1152), wv * 256, row * 1152 + sl * 128); \
    asm volatile("s_waitcnt vmcnt(%0)" :: "n"(CPF - 1) : "memory"); }
  for (int it = 0; it < 4000 && __builtin_amdgcn_s_memrealtime() - tb < WDELAY; ++it) __builtin_amdgcn_s_sleep(2);
  ST(11);
  __amdgpu_buffer_rsrc_t hr = __builtin_amdgcn_make_buffer_rsrc(ws + WS_H, 0, H_BYTES, 0x00020000);
#pragma unroll 1
  for (int i = id; i < 576; i += 40) __builtin_amdgcn_raw_buffer_store_b128(u32x4{MARK, 0u, 0u, 0u}, hr, lane * 16, (g * 576 + i) * 1024, 16);
  ST(12);
  asm volatile("s_waitcnt vmcnt(0)" ::: "memory");
  ST(13);
  bar();
  if (threadIdx.x == 0) {
    __amdgpu_buffer_rsrc_t fr = __builtin_amdgcn_make_buffer_rsrc(ws + WS_FLAG, 0, 64 * 128, 0x00020000);
    __builtin_amdgcn_raw_buffer_store_b128(u32x4{0u, mix32((u32)ep), 0u, mix32((u32)(ep >> 32) ^ 0x5bd1e995u)}, fr, 0, (g * 8 + k) * 128, 16);
  }
  CPULL()
  asm volatile("s_waitcnt vmcnt(0)" ::: "memory");
  ST(10);
  STAMP_FLUSH(P);
}

__global__ __launch_bounds__(512, 2) void fused(Params P) {
  const int b = blockIdx.x, xcd = b & 7, slot = b >> 3;
  KA K;
  asm volatile("s_load_dwordx16 %1, %3, 0x40\n\ts_load_dwordx8 %2, %3, 0x80\n\ts_load_dwordx16 %0, %3, 0x0"
               : "=&s"(K.a), "=&s"(K.b), "=&s"(K.c) : "s"(__builtin_amdgcn_kernarg_segment_ptr()) : "memory");
  u64 pc = 0;
  asm volatile("s_getpc_b64 %0" : "=s"(pc));
  if (__builtin_expect(slot < 18, 1)) {
    g_role(K, 2 * xcd + (slot >= 9), slot >= 9 ? slot - 9 : slot, pc);
  } else if (slot < 27) {
    a_role(K, 72 * xcd + 8 * (slot - 18));
  } else pf_role(K, slot - 27);
}

extern "C" void kernel_launch(void* const* d_in, const int* in_sizes, int n_in, void* d_out, int out_size, void* d_ws, size_t ws_size, hipStream_t stream) {
  Params P;
  for (int i = 0; i < 18; ++i) P.in[i] = (const u16*)d_in[i];
  P.out = (float*)d_out; P.ws = (char*)d_ws;
  (void)hipFuncSetAttribute((const void*)fused, hipFuncAttributeMaxDynamicSharedMemorySize, LDS_BYTES);
#define GRID 256
  fused<<<GRID, 512, LDS_BYTES, stream>>>(P);
}
```

```cpp
#define XROT 2 OAUX=16 OAUXA=0 HS0=8 HS=3 PBAR=1 DEEP=8
#define WDELAY 100
#define CODEPF 3
#define ASLP 14
#define BSLP 3
#define CPF 4
#define CNS 9
#include <hip/hip_runtime.h>
#include <stdint.h>

typedef unsigned short u16; typedef unsigned u32; typedef unsigned long long u64;
typedef __bf16 bf16x8 __attribute__((ext_vector_type(8)));
typedef __bf16 bf16x2 __attribute__((ext_vector_type(2)));
typedef short s16x4 __attribute__((ext_vector_type(4)));
typedef float f32x16 __attribute__((ext_vector_type(16)));
typedef float f32x4 __attribute__((ext_vector_type(4)));
typedef float f32x2 __attribute__((ext_vector_type(2)));
typedef u32 u32x4 __attribute__((ext_vector_type(4)));
typedef u32 u32x2 __attribute__((ext_vector_type(2)));
#define LAS __attribute__((address_space(3)))
#define GAS __attribute__((address_space(1)))
#define DEV __device__ __forceinline__

struct Params { const u16* in[18]; float* out; char* ws; };
typedef u64 u64x8 __attribute__((ext_vector_type(8)));
typedef u64 u64x4 __attribute__((ext_vector_type(4)));
struct KA { u64x8 a, b; u64x4 c; };
#define KWAIT(k) asm volatile("s_waitcnt lgkmcnt(0)" :: "s"(k.a), "s"(k.b), "s"(k.c) : "memory")
__device__ __forceinline__ u64 kraw(const KA& k, int i) { return i < 8 ? k.a[i & 7] : i < 16 ? k.b[i & 7] : k.c[i & 3]; }
__device__ __forceinline__ char* kptr(const KA& k, int i) { char* p; asm volatile("s_mov_b64 %0, %1" : "=s"(p) : "s"(kraw(k, i))); return p; }

constexpr u32 WS_EPOCH = 0, WS_FLAG = 4096, WS_GRAN = 65536, WS_H = 262144;
constexpr u32 H_BYTES = 16u * 4u * 72u * 1024u;
constexpr u32 LDS_BYTES = 163840, SCR = 147456;
constexpr int SPIN_MAX = 1 << 16;
#define NWG 5
#define HS 1
#define LAUX 16
#define WAUX 16
#define OAUX 0
#define OAUXA OAUX

#define STAMP_DECL
#define ST(i)
#define ST0()
#define ST0F()
#define STAMP_FLUSH(P)

DEV float bf2f(u16 v) { return __uint_as_float(((u32)v) << 16); }
DEV u32 pack2(float a, float b) { bf16x2 r; r[0] = (__bf16)a; r[1] = (__bf16)b; return __builtin_bit_cast(u32, r); }
DEV float rbf(float f) { return (float)(__bf16)f; }
DEV void bar() { asm volatile("s_waitcnt lgkmcnt(0)\n\ts_barrier" ::: "memory"); }
DEV u32 mix32(u32 x) { x *= 0x9E3779B1u; x ^= x >> 15; x *= 0x85EBCA77u; x ^= x >> 13; return x + 0x632BE5ABu; }
DEV f32x2 cmul(f32x2 a, f32x2 b) { return f32x2{a.x * b.x - a.y * b.y, a.x * b.y + a.y * b.x}; }
DEV f32x2 cfma(f32x2 a, f32x2 b, f32x2 c) { return f32x2{fmaf(a.x, b.x, fmaf(-a.y, b.y, c.x)), fmaf(a.x, b.y, fmaf(a.y, b.x, c.y))}; }
DEV f32x2 cexp(float re, float im) {
  float e = __builtin_amdgcn_exp2f(re * 1.44269504088896341f);
  float r = __builtin_amdgcn_fractf(im * 0.15915494309189535f);
  return f32x2{e * __builtin_amdgcn_cosf(r), e * __builtin_amdgcn_sinf(r)};
}
DEV void glds4(const char* gb, u32 ldsb, u32 v0, u32 v1, u32 v2, u32 v3) {
  u32 keep;
  asm volatile("s_mov_b32 %0, m0\n\ts_mov_b32 m0, %2\n\ts_nop 0\n\t"
               "global_load_lds_dwordx4 %3, %1\n\tglobal_load_lds_dwordx4 %4, %1 offset:1024\n\t"
               "global_load_lds_dwordx4 %5, %1 offset:2048\n\tglobal_load_lds_dwordx4 %6, %1 offset:3072\n\ts_mov_b32 m0, %0"
               : "=&s"(keep) : "s"(gb), "s"(ldsb), "v"(v0), "v"(v1), "v"(v2), "v"(v3) : "memory");
}
DEV void glds1(const char* gb, u32 ldsb, u32 v0) {
  u32 keep;
  asm volatile("s_mov_b32 %0, m0\n\ts_mov_b32 m0, %2\n\ts_nop 0\n\tglobal_load_lds_dwordx4 %3, %1\n\ts_mov_b32 m0, %0"
               : "=&s"(keep) : "s"(gb), "s"(ldsb), "v"(v0) : "memory");
}
template <class T> DEV T ldsr(u32 a) { return *(const LAS T*)(size_t)a; }
template <class T> DEV void ldsw(u32 a, T v) { *(LAS T*)(size_t)a = v; }
DEV f32x2 pr(const f32x16& a, int m) { return f32x2{a[2 * m], a[2 * m + 1]}; }

#define RA 1
DEV void swap16(float x, float& lo, float& hi) { auto r = __builtin_amdgcn_permlane16_swap(__float_as_uint(x), __float_as_uint(x), false, false); lo = __uint_as_float(r[0]); hi = __uint_as_float(r[1]); }
DEV void swap32(float x, float& lo, float& hi) { auto r = __builtin_amdgcn_permlane32_swap(__float_as_uint(x), __float_as_uint(x), false, false); lo = __uint_as_float(r[0]); hi = __uint_as_float(r[1]); }
DEV int wrap9(int v) { return v >= 9 ? v - 9 : v; }
DEV u64 load_epoch(const char* ws) { u64 ep; asm volatile("s_load_dwordx2 %0, %1, 0x0\n\ts_waitcnt lgkmcnt(0)" : "=s"(ep) : "s"(ws) : "memory"); return ep; }
DEV void touch64(const char* gb, u32 ldsb, u32 v0) {
  u32 keep;
  asm volatile("s_mov_b32 %0, m0\n\ts_mov_b32 m0, %2\n\ts_nop 0\n\tglobal_load_lds_dword %3, %1\n\ts_mov_b32 m0, %0"
               : "=&s"(keep) : "s"(gb), "s"(ldsb), "v"(v0) : "memory");
}

DEV void touch64s(const char* gb, u32 ldsb, u32 v0) {
  u32 keep;
  asm volatile("s_mov_b32 %0, m0\n\ts_mov_b32 m0, %2\n\ts_nop 0\n\tglobal_load_lds_dword %3, %1 sc1\n\ts_mov_b32 m0, %0"
               : "=&s"(keep) : "s"(gb), "s"(ldsb), "v"(v0) : "memory");
}
#define TOUCH touch64
#define MM(h, cc, acc) __builtin_amdgcn_mfma_f32_32x32x16_bf16(h, cc, acc, 0, 0, 0)
#define SB __builtin_amdgcn_sched_barrier(0)
#define WL(n) __builtin_amdgcn_s_waitcnt(0xC07F | ((n) << 8))
constexpr u32 HREG = 131072, MARK = 0x80000000u;
DEV void g_role(KA K, int c, int j, u64 pc) {
  u32 pl = 0, pr_ = 0, pi_ = 0;
  const int lane = threadIdx.x & 63, wv = __builtin_amdgcn_readfirstlane(threadIdx.x >> 6);
  STAMP_DECL
  ST0();
  const int rot = (j + 4 * (c & 1) + XROT * (c >> 1)) % 9;
  const int l15 = lane & 15, q4 = lane >> 4;
  const int w = wv & 3, l31 = lane & 31, hh = lane >> 5;
  f32x16 acc0, acc1;
  u32 vb[4], aoff[4], ca[4];
  const char* c8;
  u64 ep;
  constexpr int PF = 8, CD = 3;
  u32x4 hf[PF];
  bf16x8 cf[CD + 1][2];
#define CADDR(k, h) (ca[(k) & 3] + (((k) >> 2) & 1) * 8192 + (h) * 4096)
#define PIN(x) asm volatile("" : "+s"(x))
#define HLOAD(o, k) __builtin_amdgcn_raw_buffer_load_b128(hr, ho + ((k) & 3) * 1024, (o) + (((k) >> 2) & 1) * 4096, LAUX)
  const int grp = wv >> 2;
  u32 he, hc, hn, cs, cl, l8, ho, ho4;
#define PINS() do { he = (c * 4 + w) * 72 * 1024; hc = he + wrap9(j + 1 + grp) * 8192; hn = he + wrap9(j + 3 + grp) * 8192; he += 9 * 8192; \
    cs = (3 + grp) * 16384; cl = grp ? HREG : 16384; l8 = HREG + w * 4096; ho = lane * 16; ho4 = ho + 4096; \
    asm volatile("" : "+s"(he), "+s"(hc), "+s"(hn), "+s"(cs), "+s"(cl), "+s"(l8)); asm volatile("" : "+v"(ho), "+v"(ho4)); } while (0)
  u32 vx[4], ko0 = rot * 128, ko1 = wrap9(rot + 1) * 128, sl0 = w * 4096;
  if (wv >= 4) {
#pragma unroll
    for (int q = 0; q < 4; ++q) {
      int i = 2 * w + (q >> 1), qq = 2 * (q & 1) + (lane >> 5), r = (lane >> 3) & 3;
      int time = 32 * qq + 16 * (r & 1) + 2 * i + (r >> 1);
      u32 ch = (u32)(((lane & 7) ^ ((4 * (q & 1) + (lane >> 4)) & 7)) << 4) + 3072 - 1024 * q;
      vx[q] = (128 * c + time) * 1152 + ch;
      vb[q] = (64 * j + 32 * (w & 1) + 8 * q + (lane >> 3)) * 1152 + ch;
    }
  } else {
#pragma unroll
    for (int q = 0; q < 4; ++q) { vx[q] = (64 * j + 16 * wv + l15) * 2; vb[q] = 0; }
  }
  asm volatile("" : "+v"(vx[0]), "+v"(vx[1]), "+v"(vx[2]), "+v"(vx[3]), "+v"(vb[0]), "+v"(vb[1]), "+v"(vb[2]), "+v"(vb[3]));
  asm volatile("" : "+s"(ko0), "+s"(ko1), "+s"(sl0));
  KWAIT(K);
  ST0F();
  char* ws;
  __amdgpu_buffer_rsrc_t hr;
  if (wv < 4)
    asm volatile("global_load_ushort %0, %3, %4\n\tglobal_load_ushort %1, %3, %5\n\tglobal_load_ushort %2, %3, %6"
                 : "=&v"(pl), "=&v"(pr_), "=&v"(pi_) : "v"(vx[0]), "s"(kraw(K, 16)), "s"(kraw(K, 10)), "s"(kraw(K, 11)) : "memory");
  if (__builtin_expect(wv >= 4, 1)) {
    const char* xb = kptr(K, 9) - 3072;
    glds4(xb + ko0, sl0, vx[0], vx[1], vx[2], vx[3]);
    const char *b0 = kptr(K, 12), *b1 = kptr(K, 13), *bb = (w < 2 ? b0 : b1) - 3072;
    glds4(bb + ko0, sl0 + 16384, vb[0], vb[1], vb[2], vb[3]);
    glds4(xb + ko1, sl0 + 32768, vx[0], vx[1], vx[2], vx[3]);
    glds4(bb + ko1, sl0 + 49152, vb[0], vb[1], vb[2], vb[3]);
    const char *c0 = kptr(K, 14), *c1 = kptr(K, 15), *cb = (w < 2 ? c0 : c1) - 3072;
    ws = kptr(K, 19);
    hr = __builtin_amdgcn_make_buffer_rsrc(ws + WS_H, 0, H_BYTES, 0x00020000);
    ST(8);
    u32 dk0, dk1;
    asm volatile("global_load_ushort %0, %2, %3\n\tglobal_load_ushort %1, %2, %3 offset:64" : "=&v"(dk0), "=&v"(dk1) : "v"((64 * j + l31) * 2), "s"(kraw(K, 17)) : "memory");
    ST(9);
    s16x4 xs[8];
    asm volatile("s_waitcnt vmcnt(10)" ::: "memory");
    const char* ce = cb + ((w & 1) ? -32 * 1152 : 32 * 1152);
#define CPIECE(i) glds4((((i) & 1) ? cb : ce) + wrap9(j + (i)) * 128, (((i) + 2) & 7) * 16384 + w * 4096, vb[0], vb[1], vb[2], vb[3])
    int kn = wrap9(rot + 2);
#pragma unroll 1
    for (int s = 0; s < 9; ++s) {
      bar();
      if (s <= 6) {
        u32 sl = ((s + 2) & 3) * 32768 + w * 4096;
        glds4(xb + kn * 128, sl, vx[0], vx[1], vx[2], vx[3]);
        glds4(bb + kn * 128, sl + 16384, vb[0], vb[1], vb[2], vb[3]);
      } else { CPIECE(2 * s - 14); CPIECE(2 * s - 13); }
      if (wrap9(rot + s) == j) {
        const int e = (lane >> 2) & 3, pp = lane & 3;
        u32 sb = (s & 3) * 32768 + (32 * hh + 4 * w + 16 * (e >> 1) + 2 * (e & 1)) * 128 + (((2 * ((lane >> 4) & 1) + (pp >> 1)) ^ ((w << 1) | (e & 1))) << 4) + (pp & 1) * 8;
#pragma unroll
        for (int g = 0; g < 4; ++g) {
          xs[g] = __builtin_amdgcn_ds_read_tr16_b64_v4i16((LAS s16x4*)(size_t)(sb + (64 * (g & 1) + (g >> 1)) * 128));
          xs[4 + g] = __builtin_amdgcn_ds_read_tr16_b64_v4i16((LAS s16x4*)(size_t)((sb ^ 64) + (64 * (g & 1) + (g >> 1)) * 128));
        }
      }
      kn = wrap9(kn + 1);
      if (s < 8) asm volatile("s_waitcnt vmcnt(8)" ::: "memory");
    }
    bar();
    ST(3);
    asm volatile("" : "+v"(dk0), "+v"(dk1));
    const float dsk0 = __uint_as_float(dk0 << 16), dsk1 = __uint_as_float(dk1 << 16);
#pragma unroll
    for (int i = 4; i < 8; ++i) CPIECE(i);
#pragma unroll
    for (int r = 0; r < 16; ++r) {
      acc1[r] = rbf(bf2f((u16)xs[r >> 2][r & 3]) * dsk0);
      acc0[r] = rbf(bf2f((u16)xs[4 + (r >> 2)][r & 3]) * dsk1);
    }
#pragma unroll
    for (int s = 0; s < 4; ++s) aoff[s] = l31 * 128 + (((2 * s + hh) ^ ((l31 >> 1) & 7)) << 4);
    u32 ra[2];
#pragma unroll
    for (int u = 0; u < 2; ++u) {
      int row = 8 * (q4 >> 1) + 4 * u + (l15 >> 2);
      ra[u] = HREG + w * 4096 + row * 64 + (((2 * (q4 & 1) + ((l15 & 3) >> 1)) ^ ((row >> 1) & 3)) << 4) + (l15 & 1) * 8;
    }
    asm volatile("" : "+v"(acc0), "+v"(acc1), "+v"(ra[0]), "+v"(ra[1]), "+v"(aoff[0]), "+v"(aoff[1]), "+v"(aoff[2]), "+v"(aoff[3]));
#pragma unroll
    for (int s = 0; s < 4; ++s) ca[s] = aoff[s] + 2 * 16384;
    c8 = ce + wrap9(j + 8) * 128;
    asm volatile("" : "+s"(c8));
    PINS();
    ep = 0;
    if (c == 15 && j == 0 && wv == 4) {
      ep = load_epoch(ws);
      GAS u64* fp = (GAS u64*)(ws + WS_FLAG + ((lane >> 3) * 8 + ((lane & 7) < NWG - 1 ? (lane & 7) : NWG - 1)) * 128);
      const u64 ta = (u64)mix32((u32)ep) << 32, tb = (u64)mix32((u32)(ep >> 32) ^ 0x5bd1e995u) << 32;
#pragma unroll 1
      for (int it = 0; it < SPIN_MAX; ++it) {
        if (__all(__hip_atomic_load(fp, __ATOMIC_RELAXED, __HIP_MEMORY_SCOPE_AGENT) == ta && __hip_atomic_load(fp + 1, __ATOMIC_RELAXED, __HIP_MEMORY_SCOPE_AGENT) == tb)) break;
        __builtin_amdgcn_s_sleep(4);
      }
    }
    asm volatile("s_waitcnt vmcnt(0)" ::: "memory");
    ST(1);
    bar();
    ST(4);
#define hx hf
#pragma unroll
    for (int k = 0; k < 8; ++k) {
      u32x2 l2 = __builtin_bit_cast(u32x2, __builtin_amdgcn_ds_read_tr16_b64_v4i16((LAS s16x4*)(size_t)(ra[0] + (k & 3) * 1024 + (k >> 2) * 16384)));
      u32x2 h2 = __builtin_bit_cast(u32x2, __builtin_amdgcn_ds_read_tr16_b64_v4i16((LAS s16x4*)(size_t)(ra[1] + (k & 3) * 1024 + (k >> 2) * 16384)));
      hx[k] = u32x4{l2.x, l2.y, h2.x, h2.y};
      if (k < CD) { cf[k][0] = ldsr<bf16x8>(ca[k]); cf[k][1] = ldsr<bf16x8>(ca[k] + 4096); }
    }
    SB;
#pragma unroll
    for (int k = 0; k < 8; ++k) {
      bf16x8 a = __builtin_bit_cast(bf16x8, hx[k]);
      acc0 = MM(a, cf[k & 3][0], acc0);
      asm volatile("" : "+v"(acc0)); SB;
      if (k + CD < 8) cf[(k + CD) & 3][0] = ldsr<bf16x8>(CADDR(k + CD, 0));
      SB;
      acc1 = MM(a, cf[k & 3][1], acc1);
      asm volatile("" : "+v"(acc1)); SB;
      if (k + CD < 8) cf[(k + CD) & 3][1] = ldsr<bf16x8>(CADDR(k + CD, 1));
      SB;
    }
    __builtin_amdgcn_s_sleep(BSLP);
#pragma unroll
    for (int q = 0; q < 7; ++q) { hf[q] = __builtin_amdgcn_raw_buffer_load_b128(hr, (q < 4 ? ho : ho4) + (q & 3) * 1024, hc, LAUX); SB; }
  }
  int wq = wv;
  asm volatile("" : "+s"(wq));
  if (__builtin_expect(wq < 4, 1)) {
    asm volatile("s_load_dwordx2 %0, %1, 0x0" : "=&s"(ep) : "s"(kraw(K, 19)) : "memory");
    ws = kptr(K, 19);
    hr = __builtin_amdgcn_make_buffer_rsrc(ws + WS_H, 0, H_BYTES, 0x00020000);
    if (wv == 0 && (blockIdx.x >> 3) < CODEPF) {
      touch64((const char*)(pc & ~(u64)127), SCR, (blockIdx.x >> 3) * 8192 + lane * 128);
      asm volatile("s_waitcnt vmcnt(1)" ::: "memory");
    } else asm volatile("s_waitcnt vmcnt(0)" ::: "memory");
    asm volatile("" : "+v"(pl), "+v"(pr_), "+v"(pi_) :: "memory");
    u32 hbase = (c * 4 * 72 + j * 8 + wv) * 1024;
    float dt = rbf(__builtin_amdgcn_exp2f(__uint_as_float(pl << 16) * 1.44269504088896341f));
    float Lr = __uint_as_float(pr_ << 16), Li = __uint_as_float(pi_ << 16);
    f32x2 lam = cexp(dt * Lr, dt * Li);
    float inv = 2.f / (Lr * Lr + Li * Li);
    f32x2 coef = cmul(f32x2{lam.x - 1.f, lam.y}, f32x2{Lr * inv, -Li * inv});
    f32x2 mu = cmul(lam, lam); mu = cmul(mu, mu); mu = cmul(mu, mu); mu = cmul(mu, mu);
    f32x2 nu = cmul(mu, mu), nu2 = cmul(nu, nu), La = cmul(nu2, nu2);
    f32x2 La2 = cmul(La, La), La4 = cmul(La2, La2);
    f32x2 one = {1.f, 0.f};
    f32x2 Lg = cmul((q4 & 2) ? La2 : one, (q4 & 1) ? La : one);
    f32x2 nuq = (q4 & 1) ? nu : one;
    f32x2 cm0 = cmul(coef, cmul((q4 & 2) ? nu2 : one, nuq)), cm1 = cmul(cm0, mu);
    asm volatile("s_waitcnt lgkmcnt(0)" : "+s"(ep) :: "memory");
    u32 tlo = mix32((u32)ep), thi = mix32((u32)(ep >> 32) ^ 0x5bd1e995u);
    asm volatile("" :: "s"(tlo), "s"(thi));
    asm volatile("" : "+v"(lam), "+v"(coef), "+v"(mu), "+v"(La4), "+v"(Lg), "+v"(cm0), "+v"(cm1), "+v"(nu), "+v"(nu2), "+v"(nuq));
    f32x4 ar[8], ai[8];
#pragma unroll
    for (int i = 0; i < 8; ++i) { ar[i] = f32x4{0.f, 0.f, 0.f, 0.f}; ai[i] = ar[i]; asm volatile("" : "+v"(ar[i]), "+v"(ai[i])); }
    ST(1);
    u32 xa0 = l15 * 128 + ((q4 ^ (l15 >> 1)) << 4), xa1 = xa0 ^ 64, ba0 = xa0 + 16384 + wv * 2048, ba1 = ba0 ^ 64;
    bf16x8 A[2][8], Bq[2][2];
#define STEP(cs, RD, xa, ba, G10, G11, G12, G13) do { WL(4); SB; \
      _Pragma("unroll") for (int i = 0; i < 8; ++i) { if (i == 4) { if (RD) WL(8); else WL(0); SB; } \
        ar[i] = __builtin_amdgcn_mfma_f32_16x16x32_bf16(A[cs][i], Bq[cs][0], ar[i], 0, 0, 0); asm volatile("" : "+v"(ar[i])); SB; \
        if (RD) { if (i == 0) Bq[cs ^ 1][0] = ldsr<bf16x8>(ba); else if (i < 5) A[cs ^ 1][2 * i - 2] = ldsr<bf16x8>(xa + (2 * i - 2) * 2048); } \
        if (i == 5) { G10; } if (i == 6) { G12; } SB; \
        ai[i] = __builtin_amdgcn_mfma_f32_16x16x32_bf16(A[cs][i], Bq[cs][1], ai[i], 0, 0, 0); asm volatile("" : "+v"(ai[i])); SB; \
        if (RD) { if (i == 0) Bq[cs ^ 1][1] = ldsr<bf16x8>(ba + 8192); else if (i < 5) A[cs ^ 1][2 * i - 1] = ldsr<bf16x8>(xa + (2 * i - 1) * 2048); } \
        if (i == 5) { G11; } if (i == 6) { G13; } SB; } } while (0)
#define ADV(v) do { v = (v + 32768) & 131071; asm volatile("" : "+v"(v)); } while (0)
    bar();
    ST(2);
    Bq[0][0] = ldsr<bf16x8>(ba0); Bq[0][1] = ldsr<bf16x8>(ba0 + 8192);
#pragma unroll
    for (int i = 0; i < 8; ++i) A[0][i] = ldsr<bf16x8>(xa0 + i * 2048);
    SB;
#pragma unroll 1
    for (int s = 0; s < 8; ++s) {
      STEP(0, 1, xa1, ba1, ADV(xa0), ADV(ba0), asm volatile("s_barrier" ::: "memory"), );
      STEP(1, 1, xa0, ba0, ADV(xa1), ADV(ba1), , );
    }
    STEP(0, 1, xa1, ba1, , , , bar());
    STEP(1, 0, xa0, ba0, asm volatile("s_waitcnt vmcnt(0)" ::: "memory"), , , );
    ST(3);
    const f32x2 lr2 = {lam.x, lam.x}, li2 = {lam.y, lam.y};
    f32x2 Br[16], Bi[16];
#pragma unroll
    for (int i = 0; i < 8; ++i) { Br[2 * i] = f32x2{ar[i][0], ar[i][1]}; Br[2 * i + 1] = f32x2{ar[i][2], ar[i][3]}; Bi[2 * i] = f32x2{ai[i][0], ai[i][1]}; Bi[2 * i + 1] = f32x2{ai[i][2], ai[i][3]}; }
    f32x2 sr = Br[0], si = Bi[0];
#pragma unroll
    for (int k = 1; k < 16; ++k) {
      f32x2 t1 = __builtin_elementwise_fma(lr2, sr, Br[k]), t2 = __builtin_elementwise_fma(lr2, si, Bi[k]);
      f32x2 n1 = __builtin_elementwise_fma(-li2, si, t1), n2 = __builtin_elementwise_fma(li2, sr, t2);
      sr = n1; si = n2;
    }
    const f32x2 S0 = {sr.x, si.x};
    f32x2 T = cfma(mu, S0, f32x2{sr.y, si.y}), lo, hi;
    { float a, b; swap16(T.x, a, b); lo.x = a; hi.x = b; swap16(T.y, a, b); lo.y = a; hi.y = b; }
    f32x2 U = cfma(nu, lo, hi), E = (q4 & 1) ? lo : f32x2{0.f, 0.f};
    { float a, b; swap32(U.x, a, b); lo.x = a; hi.x = b; swap32(U.y, a, b); lo.y = a; hi.y = b; }
    f32x2 tot = cfma(nu2, lo, hi);
    if (q4 == 0 && c < 15) {
      u32x4 gv = {__float_as_uint(tot.x), tlo, __float_as_uint(tot.y), thi};
      __amdgpu_buffer_rsrc_t gr = __builtin_amdgcn_make_buffer_rsrc(ws + WS_GRAN, 0, 9 * 16 * 1024, 0x00020000);
      __builtin_amdgcn_raw_buffer_store_b128(gv, gr, l15 * 16, (j * 16 + c) * 1024 + wv * 256, 16);
    }
#pragma unroll
    for (int k = 0; k < 16; ++k) asm volatile("" : "+v"(Br[k]), "+v"(Bi[k]) :: "memory");
    ST(4);
    if (q4 & 2) E = cfma(nuq, lo, E);
    f32x2 La_ = cmul(coef, E), Lb_ = cmul(coef, cfma(mu, E, S0));
    const f32x2 cr2 = {coef.x, coef.x}, ci2 = {coef.y, coef.y};
#pragma unroll
    for (int k = 0; k < 16; ++k) {
      f32x2 n1 = __builtin_elementwise_fma(-ci2, Bi[k], cr2 * Br[k]), n2 = __builtin_elementwise_fma(ci2, Br[k], cr2 * Bi[k]);
      Br[k] = n1; Bi[k] = n2;
    }
#pragma unroll
    for (int k = 0; k < 16; ++k) asm volatile("" : "+v"(Br[k]), "+v"(Bi[k]));
    asm volatile("" : "+v"(La_), "+v"(Lb_));
    const u32 wa = HREG + q4 * 4096 + wv * 1024 + l15 * 64;
    u32 wa4[4];
#pragma unroll
    for (int q = 0; q < 4; ++q) wa4[q] = wa + ((q ^ ((l15 >> 1) & 3)) << 4);
    asm volatile("" : "+v"(wa4[0]), "+v"(wa4[1]), "+v"(wa4[2]), "+v"(wa4[3]));
    u32 ra[2];
#pragma unroll
    for (int u = 0; u < 2; ++u) {
      int row = 8 * (q4 >> 1) + 4 * u + (l15 >> 2);
      ra[u] = HREG + wv * 1024 + row * 64 + (((2 * (q4 & 1) + ((l15 & 3) >> 1)) ^ ((row >> 1) & 3)) << 4) + (l15 & 1) * 8;
    }
#pragma unroll
    for (int s = 0; s < 4; ++s) aoff[s] = l31 * 128 + (((2 * s + hh) ^ ((l31 >> 1) & 7)) << 4);
    c8 = ws;
#pragma unroll
    for (int r = 0; r < 16; ++r) { acc0[r] = 0.f; acc1[r] = 0.f; }
    asm volatile("" : "+v"(ra[0]), "+v"(ra[1]), "+v"(aoff[0]), "+v"(aoff[1]), "+v"(aoff[2]), "+v"(aoff[3]), "+v"(acc0), "+v"(acc1));
    PINS(); PIN(hbase);
    ST(8);
    f32x2 H;
    {
      const char* gb = ws;
      u32 go[4];
      const u32 fo = WS_FLAG + ((c >> 1) * 8 + ((lane & 7) < NWG - 1 ? (lane & 7) : NWG - 1)) * 128;
#pragma unroll
      for (int n = 0; n < 4; ++n) { int cs = c - 1 - q4 - 4 * n; go[n] = cs >= 0 ? WS_GRAN + j * 16384 + wv * 256 + cs * 1024 + l15 * 16 : fo; }
      u32x4 g0, g1, g2, g3;
#pragma unroll 1
      for (int it = 0; it < SPIN_MAX; ++it) {
        asm volatile("global_load_dwordx4 %0, %4, %8 sc1\n\tglobal_load_dwordx4 %1, %5, %8 sc1\n\tglobal_load_dwordx4 %2, %6, %8 sc1\n\tglobal_load_dwordx4 %3, %7, %8 sc1\n\t"
                     "s_waitcnt vmcnt(0)"
                     : "=&v"(g0), "=&v"(g1), "=&v"(g2), "=&v"(g3) : "v"(go[0]), "v"(go[1]), "v"(go[2]), "v"(go[3]), "s"(gb) : "memory");
        u32 bad = (((g0.y ^ tlo) | (g1.y ^ tlo)) | ((g2.y ^ tlo) | (g3.y ^ tlo))) | (((g0.w ^ thi) | (g1.w ^ thi)) | ((g2.w ^ thi) | (g3.w ^ thi)));
        if (!__any(bad != 0)) break;
        __builtin_amdgcn_s_sleep(HS);
      }
      f32x2 s3 = {__uint_as_float(g3.x), __uint_as_float(g3.z)}, s2 = {__uint_as_float(g2.x), __uint_as_float(g2.z)};
      f32x2 s1 = {__uint_as_float(g1.x), __uint_as_float(g1.z)}, s0 = {__uint_as_float(g0.x), __uint_as_float(g0.z)};
      H = cmul(Lg, cfma(La4, cfma(La4, cfma(La4, s3, s2), s1), s0));
      float a, b;
      swap16(H.x, a, b); H.x = a + b; swap16(H.y, a, b); H.y = a + b;
      swap32(H.x, a, b); H.x = a + b; swap32(H.y, a, b); H.y = a + b;
    }
    ST(5);
    f32x2 sa = cfma(cm0, H, La_), sb_ = cfma(cm1, H, Lb_);
    sr = f32x2{sa.x, sb_.x}; si = f32x2{sa.y, sb_.y};
#define HPK(g, hf) u32x4{HP1(g, 8 * (hf)), HP1(g, 8 * (hf) + 2), HP1(g, 8 * (hf) + 4), HP1(g, 8 * (hf) + 6)}
#define HP1(g, k) (((g) >> 1) ? pack2(-Bi[k][(g) & 1], -Bi[(k) + 1][(g) & 1]) : pack2(Br[k][(g) & 1], Br[(k) + 1][(g) & 1]))
    u32x4 hv[8];
#pragma unroll
    for (int k = 0; k < 16; ++k) {
      f32x2 t1 = __builtin_elementwise_fma(lr2, sr, Br[k]), t2 = __builtin_elementwise_fma(lr2, si, Bi[k]);
      f32x2 n1 = __builtin_elementwise_fma(-li2, si, t1), n2 = __builtin_elementwise_fma(li2, sr, t2);
      sr = n1; si = n2; Br[k] = sr; Bi[k] = si;
      if (k >= 8 && !(k & 1)) { const int g = (k - 8) >> 1; SB; hv[g] = HPK(g, 0); ldsw(wa4[2 * (g & 1)] + (g >> 1) * 16384, hv[g]); SB; }
    }
#pragma unroll
    for (int g = 0; g < 4; ++g) { hv[4 + g] = HPK(g, 1); asm volatile("" : "+v"(hv[4 + g])); ldsw(wa4[2 * (g & 1) + 1] + (g >> 1) * 16384, hv[4 + g]); SB; }
    asm volatile("" :: "v"(hv[0]), "v"(hv[1]), "v"(hv[2]), "v"(hv[3]), "v"(hv[4]), "v"(hv[5]), "v"(hv[6]), "v"(hv[7]));
#pragma unroll
    for (int q = 0; q < 8; ++q) {
      u32x2 l2 = __builtin_bit_cast(u32x2, __builtin_amdgcn_ds_read_tr16_b64_v4i16((LAS s16x4*)(size_t)(ra[0] + (q & 3) * 4096 + (q >> 2) * 16384)));
      u32x2 h2 = __builtin_bit_cast(u32x2, __builtin_amdgcn_ds_read_tr16_b64_v4i16((LAS s16x4*)(size_t)(ra[1] + (q & 3) * 4096 + (q >> 2) * 16384)));
      hv[q] = u32x4{l2.x, l2.y, h2.x, h2.y}; SB;
    }
#pragma unroll
    for (int q = 0; q < 8; ++q) {
      hv[q].x = (int)hv[q].x > (int)(MARK + 1) ? hv[q].x : MARK + 1;
      __builtin_amdgcn_raw_buffer_store_b128(hv[q], hr, ho, hbase + ((q & 3) * 72 + (q >> 2) * 4) * 1024, 16); SB;
    }
    ST(6);
    bar();
    __builtin_amdgcn_s_sleep(ASLP);
#pragma unroll
    for (int q = 0; q < 7; ++q) { hf[q] = __builtin_amdgcn_raw_buffer_load_b128(hr, (q < 4 ? ho : ho4) + (q & 3) * 1024, hc, LAUX); SB; }
  }
  ST(10);
  {
#define SLP 4
#define BARI 19
#define NOCHK 0
    u32 hp = 0, cp = 0;
#define CNT()
#define RST()
#define IMIN(a, b) ((int)(a) < (int)(b) ? (a) : (b))
#define ANY7(t, o) __any(IMIN(IMIN(IMIN(t[o].x, t[o + 1].x), IMIN(t[o + 2].x, t[o + 3].x)), IMIN(IMIN(t[o + 4].x, t[o + 5].x), t[o + 6].x)) == MARK)
#pragma unroll
    for (int q = 0; q < CD; ++q) { ca[q] = aoff[q] + cs; cf[q][0] = ldsr<bf16x8>(CADDR(q, 0)); cf[q][1] = ldsr<bf16x8>(CADDR(q, 1)); }
    SB;
    ST(11);
    u64 mk[8], any = 0;
#pragma unroll
    for (int q = 0; q < 8; ++q) mk[q] = 0;
#define MARKED(q) do { mk[q] = __ballot(hf[q].x == MARK); PIN(mk[q]); } while (0)
#define REDO(o, cb_) do { u32x4 t[8]; \
        _Pragma("unroll 1") for (int it = 0; it < SPIN_MAX; ++it) { \
          _Pragma("unroll") for (int q = 1; q < 8; ++q) t[q] = HLOAD(o, q); \
          if (!ANY7(t, 1)) break; \
          __builtin_amdgcn_s_sleep(SLP); } \
        _Pragma("unroll") for (int q = 1; q < 8; ++q) if (mk[q] != 0) { \
          bf16x8 a2 = __builtin_bit_cast(bf16x8, ((mk[q] >> lane) & 1) ? t[q] : u32x4{0u, 0u, 0u, 0u}); \
          const u32 cq = aoff[q & 3] + (cb_) + (q >> 2) * 8192; \
          acc0 = MM(a2, ldsr<bf16x8>(cq), acc0); \
          acc1 = MM(a2, ldsr<bf16x8>(cq + 4096), acc1); } } while (0)
#define FIX0() do { PIN(mk[0]); if (__builtin_expect((any | mk[0]) != 0, 0)) { CNT(); \
        if (any != 0) REDO(hp, cp); \
        if (mk[0] != 0) { \
          _Pragma("unroll 1") for (int it = 0; it < SPIN_MAX; ++it) { hf[0] = HLOAD(hc, 0); if (!__any(hf[0].x == MARK)) break; __builtin_amdgcn_s_sleep(SLP); } \
          bf16x8 a2 = __builtin_bit_cast(bf16x8, ((mk[0] >> lane) & 1) ? hf[0] : u32x4{0u, 0u, 0u, 0u}); \
          acc0 = MM(a2, cf[0][0], acc0); \
          acc1 = MM(a2, cf[0][1], acc1); SB; \
          _Pragma("unroll") for (int r = 1; r < 8; ++r) { hf[r] = HLOAD(hc, r); SB; } } } } while (0)
    u32 xa_ = (grp ? HREG + 16384 : 2 * 16384) + w * 4096 + lane * 16, xb_ = (grp ? 2 * 16384 : HREG + 16384) + w * 4096 + lane * 16;
    u32 ov = ((128 * c + 32 * w + 4 * hh) * 1152 + 576 + 64 * j + 32 * grp + l31) * 4;
    asm volatile("" : "+v"(ov));
#define AS 0
    asm volatile("" : "+v"(xa_), "+v"(xb_));
    f32x4 px[2][4];
#define XK 5
#define PIECE(NX) _Pragma("unroll") for (int k = 0; k < 8; ++k) { \
      bf16x8 a = __builtin_bit_cast(bf16x8, hf[k]); \
      const int kn2 = k + CD; \
      acc0 = MM(a, cf[k & 3][0], acc0); \
      asm volatile("" : "+v"(acc0)); SB; \
      if (!NOCHK) { MARKED(k); SB; } \
      if (kn2 >= 8 && NX) ca[kn2 & 3] = aoff[kn2 & 3] + cs; \
      if (k == 0) ca[3] = aoff[3] + cs; \
      if (kn2 < 8 || NX) cf[kn2 & 3][0] = ldsr<bf16x8>(CADDR(kn2, 0)); \
      if (k == 0) hf[7] = HLOAD(hc, 7); \
      if (k >= 1 && NX) hf[k - 1] = HLOAD(hn, k - 1); \
      SB; \
      acc1 = MM(a, cf[k & 3][1], acc1); \
      asm volatile("" : "+v"(acc1)); SB; \
      if (!NOCHK && k >= 1) { PIN(mk[k]); any = k == 1 ? mk[1] : any | mk[k]; PIN(any); SB; } \
      if (kn2 < 8 || NX) cf[kn2 & 3][1] = ldsr<bf16x8>(CADDR(kn2, 1)); \
      if (NX) { \
        if (k == 1) { cp = cs; cs = n == 2 ? cl : (cs + 32768) & 131071; PIN(cs); PIN(cp); } \
        if (k == 2) { if (n == 0 && grp) glds4(c8, l8, vb[0], vb[1], vb[2], vb[3]); } \
        if (k == 3) { if (n == 2) asm volatile("s_barrier" ::: "memory"); } } \
      if (AS && !NX && k == XK) WL(0); \
      if (AS && !NX && k == XK && grp) { asm volatile("s_barrier" ::: "memory"); \
        _Pragma("unroll") for (int q = 0; q < 4; ++q) { px[0][q] = ldsr<f32x4>(xa_ + q * 1024); px[1][q] = ldsr<f32x4>(xb_ + q * 1024); } } \
      if (!NOCHK && k == 0) FIX0(); \
      if (k == 7 && NX) { RST(); hp = hc; hc = hn; hn += 16384; hn = hn >= he ? hn - 9 * 8192 : hn; PIN(hc); PIN(hn); } \
      SB; }
#define PRI 0
    if (PRI > 0) { if (!grp) __builtin_amdgcn_s_setprio(PRI); } else if (PRI < 0) { if (grp) __builtin_amdgcn_s_setprio(-(PRI)); }
    SB;
#pragma unroll 1
    for (int n = 0; n < 3; ++n) { PIECE(1) }
    { const int n = 3; PIECE(0) }
    if (!NOCHK && __builtin_expect(any != 0, 0)) { CNT(); REDO(hc, cs); }
    ST(12);
    __amdgpu_buffer_rsrc_t orr = __builtin_amdgcn_make_buffer_rsrc(kptr(K, 18), 0, 2048 * 1152 * 4, 0x00020000);
#pragma unroll
    for (int q = 0; q < 4; ++q) ldsw(xa_ + q * 1024, f32x4{acc1[4 * q], acc1[4 * q + 1], acc1[4 * q + 2], acc1[4 * q + 3]});
    bar();
    if (c == 15 && j == 0 && threadIdx.x == 256)
      __hip_atomic_store((GAS u64*)(ws + WS_EPOCH), ((u64)((u32)(ep >> 32) + 1u) << 32) | (u32)((u32)ep + 1u), __ATOMIC_RELAXED, __HIP_MEMORY_SCOPE_AGENT);
#pragma unroll
    for (int q = 0; q < 4; ++q) {
      f32x4 o = ldsr<f32x4>(xb_ + q * 1024);
#pragma unroll
      for (int e = 0; e < 4; ++e) acc1[4 * q + e] = acc0[4 * q + e] + o[e];
    }
    asm volatile("" : "+v"(acc1));
#pragma unroll
    for (int r = 0; r < 16; ++r) __builtin_amdgcn_raw_buffer_store_b32(__float_as_uint(acc1[r]), orr, ov, (8 * (r >> 2) + (r & 3)) * 4608, OAUX);
    ST(13);
    ST(7);
  }
  STAMP_FLUSH(P);
}

struct C2 { f32x2 r, i; };
DEV C2 cm2(const C2& a, const C2& b) {
  C2 o; o.r = __builtin_elementwise_fma(-a.i, b.i, a.r * b.r); o.i = __builtin_elementwise_fma(a.i, b.r, a.r * b.i); return o;
}
DEV C2 cexp2(f32x2 re, f32x2 im) { f32x2 a = cexp(re.x, im.x), b = cexp(re.y, im.y); C2 o; o.r = f32x2{a.x, b.x}; o.i = f32x2{a.y, b.y}; return o; }
DEV u32x2 pk4(const C2& a) { return u32x2{pack2(a.r.x, a.r.y), pack2(a.i.x, a.i.y)}; }
DEV f32x2 up2(u32 v) { return f32x2{__uint_as_float(v << 16), __uint_as_float(v & 0xffff0000u)}; }
DEV f32x4 mf16(u32x4 a, u32x4 b, f32x4 c) { return __builtin_amdgcn_mfma_f32_16x16x32_bf16(__builtin_bit_cast(bf16x8, a), __builtin_bit_cast(bf16x8, b), c, 0, 0, 0); }
DEV u32x4 bandop(int x, int lo, int hi, u32 mul) {
  u32 m = (u32)((((1ull << (hi - lo + 1)) - 1) << 32) >> (32 + hi - x)) & 0xffu;
  u32 sp = m | (m << 15);
  u32x4 o;
#pragma unroll
  for (int i = 0; i < 4; ++i) o[i] = ((sp >> (2 * i)) & 0x10001u) * mul;
  return o;
}
DEV u32x4 rd2(u32 a0, u32 a1) {
  u32x2 x = *(volatile const LAS u32x2*)(size_t)a0, y = *(volatile const LAS u32x2*)(size_t)a1; return u32x4{x.x, x.y, y.x, y.y};
}
constexpr u32 A_RAW = 0, A_UT = 32768, UT_STR = 4640, A_KB = A_UT + 8 * UT_STR, KB_STR = 8704, KP = 544, CPS = 2112;

DEV void a_role(KA K, int d0) {
  const int lane = threadIdx.x & 63, wv = __builtin_amdgcn_readfirstlane(threadIdx.x >> 6);
  const int d = d0 + wv, j = lane & 15, q = lane >> 4;
  STAMP_DECL
  ST0();
  u32 pAr, pAi, pBr, pBi, pCr, pCi, pdt, pds;
  u32 vo = d * 256 + lane * 4, vd = d * 2, uo = (512 * wv + lane) * 1152 + d0 * 2, ul = A_RAW + wv * 8192;
  asm volatile("" : "+v"(vo), "+v"(vd), "+v"(uo));
  asm volatile("" : "+s"(ul));
  KWAIT(K);
  ST0F();
  asm volatile("global_load_dword %0, %8, %10\n\tglobal_load_dword %1, %8, %11\n\tglobal_load_ushort %6, %9, %16\n\tglobal_load_dword %2, %8, %12\n\t"
               "global_load_dword %3, %8, %13\n\tglobal_load_dword %4, %8, %14\n\tglobal_load_dword %5, %8, %15\n\tglobal_load_ushort %7, %9, %17"
               : "=&v"(pAr), "=&v"(pAi), "=&v"(pBr), "=&v"(pBi), "=&v"(pCr), "=&v"(pCi), "=&v"(pdt), "=&v"(pds)
               : "v"(vo), "v"(vd), "s"(kraw(K, 1)), "s"(kraw(K, 2)), "s"(kraw(K, 3)), "s"(kraw(K, 4)), "s"(kraw(K, 5)), "s"(kraw(K, 6)), "s"(kraw(K, 7)), "s"(kraw(K, 8)) : "memory");
  const char* ug = kptr(K, 0);
  char* const outp = kptr(K, 18);
#define UGET(i) glds1(ug + (i) * (64 * 1152), ul + (i) * 1024, uo)
#define U0 4
  if (wv < 4) { _Pragma("unroll") for (int i = 0; i < U0; ++i) UGET(i); }
  u32x4 bnd[6];
  bnd[0] = bandop(j + 16 - 8 * q, 3, 5, 0x3f80); bnd[1] = bandop(j + 16 - 8 * q, 6, 10, 0x3f80); bnd[2] = bandop(j + 16 - 8 * q, 11, 13, 0x3f80);
  bnd[3] = bandop(j + 24 - 8 * q, 14, 18, 0x3f80); bnd[4] = bandop(j + 16 - 8 * q, 5, 11, 0x3f80); bnd[5] = bandop(j + 24 - 8 * q, 12, 20, 0x3f80);
  u32x4 afx = bandop(j + 16 - 8 * q, 3, 4, 1);
  const u32 ut = A_UT + wv * UT_STR, kb = A_KB + wv * KB_STR;
  ldsw(ut + lane * 8, u32x2{0u, 0u});
#pragma unroll
  for (int i = 0; i < 6; ++i) asm volatile("" : "+v"(bnd[i]));
  asm volatile("" : "+v"(afx));
  ST(1);
  if (wv < 4) asm volatile("s_waitcnt vmcnt(%0)" :: "n"(U0) : "memory"); else asm volatile("s_waitcnt vmcnt(0)" ::: "memory");
  asm volatile("" : "+v"(pAr), "+v"(pAi), "+v"(pBr), "+v"(pBi), "+v"(pCr), "+v"(pCi), "+v"(pdt), "+v"(pds) :: "memory");
  ST(2);
  const float dsk = __uint_as_float(pds << 16);
  const float dt = rbf(__builtin_amdgcn_exp2f(__uint_as_float(pdt << 16) * 1.44269504088896341f));
  const f32x2 dt2 = {dt, dt};
  const f32x2 are = dt2 * up2(pAr), aim = dt2 * up2(pAi);
  C2 W; { C2 B = {dt2 * up2(pBr), dt2 * up2(pBi)}, Cc = {up2(pCr), up2(pCi)}; W = cm2(Cc, B); W.i = -W.i; }
  const u32 wrow = kb + lane * 8, rfr = kb + j * KP + q * 16;
  u32x4 Yf[8];
  C2 E8 = cexp2(8.f * are, 8.f * aim);
  {
    C2 Y[16];
    Y[1] = E8;
    ldsw(wrow, u32x2{0x3f803f80u, 0u});
    ldsw(wrow + KP, pk4(Y[1]));
#pragma unroll
    for (int i = 2; i < 16; ++i) { Y[i] = (i & 1) ? cm2(Y[i - 1], Y[1]) : cm2(Y[i / 2], Y[i / 2]); ldsw(wrow + i * KP, pk4(Y[i])); }
#pragma unroll
    for (int s = 0; s < 8; ++s) Yf[s] = ldsr<u32x4>(rfr + 64 * s);
  }
#define U1 6
  if (wv < 4) { _Pragma("unroll") for (int i = U0; i < U1; ++i) UGET(i); }
  E8.i = -E8.i;
  C2 E[8];
  E[1] = cexp2(are, -aim);
  const C2 G = cexp2(128.f * are, -128.f * aim);
  E[2] = cm2(E[1], E[1]); E[3] = cm2(E[2], E[1]); E[4] = cm2(E[2], E[2]); E[5] = cm2(E[4], E[1]); E[6] = cm2(E[4], E[2]); E[7] = cm2(E[4], E[3]);
  f32x4 T0 = {0.f, 0.f, 0.f, 0.f}, T1 = T0, T2 = T0;
  C2 Pc = W;
#pragma unroll
  for (int h = 0; h < 2; ++h) {
    ldsw(wrow + (8 * h) * KP, pk4(Pc));
#pragma unroll
    for (int r = 1; r < 8; ++r) ldsw(wrow + (8 * h + r) * KP, pk4(cm2(Pc, E[r])));
    Pc = cm2(Pc, G);
  }
#pragma unroll
  for (int s = 0; s < 8; ++s) T0 = mf16(ldsr<u32x4>(rfr + 64 * s), Yf[s], T0);
  if (wv < 4) { _Pragma("unroll") for (int i = U1; i < 8; ++i) UGET(i); }
  u32x2 keep[4];
#pragma unroll
  for (int h = 0; h < 2; ++h) {
    keep[2 * h] = pk4(cm2(Pc, E[4])); keep[2 * h + 1] = pk4(cm2(Pc, E8));
    ldsw(wrow + (8 * h) * KP, pk4(Pc)); ldsw(wrow + (8 * h + 1) * KP, pk4(cm2(Pc, E[2])));
    ldsw(wrow + (8 * h + 4) * KP, keep[2 * h]); ldsw(wrow + (8 * h + 5) * KP, pk4(cm2(Pc, E[6])));
    Pc = cm2(Pc, G);
  }
#pragma unroll
  for (int g = 0; g < 4; ++g) {
    ldsw(wrow + (4 * g + 2) * KP, pk4(Pc)); ldsw(wrow + (4 * g + 3) * KP, pk4(cm2(Pc, E[4])));
    Pc = cm2(Pc, G);
  }
#pragma unroll
  for (int s = 0; s < 8; ++s) T1 = mf16(ldsr<u32x4>(rfr + 64 * s), Yf[s], T1);
#pragma unroll
  for (int g = 0; g < 8; ++g) { ldsw(wrow + (4 * (g >> 1) + (g & 1)) * KP, pk4(Pc)); Pc = cm2(Pc, G); }
  ldsw(wrow + 2 * KP, pk4(Pc));
#pragma unroll
  for (int g = 0; g < 4; ++g) ldsw(wrow + (4 * g + 3) * KP, keep[g]);
#pragma unroll
  for (int s = 0; s < 8; ++s) T2 = mf16(ldsr<u32x4>(rfr + 64 * s), Yf[s], T2);
  ST(3);
  const u32 c3 = kb + 3 * CPS;
  ldsw(c3 + lane * 16, u32x4{0u, 0u, 0u, 0u}); ldsw(c3 + 1024 + lane * 16, u32x4{0u, 0u, 0u, 0u});
  {
    const int l0 = 128 * (q >> 1) + 4 * (q & 1) + 8 * j;
    ldsw(c3 + (544 - l0) * 2, u32x2{pack2(T0[3], T0[2]), pack2(T0[1], lane == 0 ? T0[0] + dsk : T0[0])});
    ldsw(c3 + (288 - l0) * 2, u32x2{pack2(T2[3], T1[1]), pack2(T1[0], T1[0])});
    if (lane == 63) ldsw(c3 + 64, u32x2{pack2(0.f, T2[3]), pack2(T2[3], T2[3])});
    const int mu0 = 32 + 8 * q + (j >> 1), od = j & 1;
    u32 p2 = pack2(lane == 0 ? T2[0] : T1[2], T1[3]);
    u32 p3 = pack2(lane == 0 ? T2[2] : T2[0], T2[1]);
    const int ia = lane == 0 ? 647 + 204 - 63 : (od ? 647 + 68 - mu0 : 647 + 204 + 1 - mu0);
    ldsw(c3 + ia * 2, (u16)p2);
    ldsw(c3 + (647 + 136 * od - mu0) * 2, (u16)(p2 >> 16));
    const int i3 = (od ? 967 - 64 : 967 + 84 - 63) - 16 * q - (j >> 1);
    ldsw(c3 + (lane == 0 ? 967 + 84 - 127 : i3) * 2, (u16)p3);
    ldsw(c3 + (i3 - 8) * 2, (u16)(p3 >> 16));
    const u32 k1024 = __builtin_amdgcn_readfirstlane(p2) & 0xffffu;
#pragma unroll
    for (int i = 0; i < 4; ++i) afx[i] = __umul24(afx[i], k1024);
    u32x4 x[2]; u32x2 y[2];
#pragma unroll
    for (int h = 0; h < 2; ++h) { x[h] = ldsr<u32x4>(c3 + h * 1024 + lane * 16); y[h] = ldsr<u32x2>(c3 + h * 1024 + lane * 16 + 16); }
#pragma unroll
    for (int h = 0; h < 2; ++h) {
      u32 a0 = __builtin_amdgcn_alignbit(x[h].y, x[h].x, 16), a1 = __builtin_amdgcn_alignbit(x[h].z, x[h].y, 16), a2 = __builtin_amdgcn_alignbit(x[h].w, x[h].z, 16),
          a3 = __builtin_amdgcn_alignbit(y[h].x, x[h].w, 16), a4 = __builtin_amdgcn_alignbit(y[h].y, y[h].x, 16);
      ldsw(kb + 2 * CPS + h * 1024 + lane * 16, u32x4{a0, a1, a2, a3});
      ldsw(kb + 1 * CPS + h * 1024 + lane * 16, u32x4{x[h].y, x[h].z, x[h].w, y[h].x});
      ldsw(kb + h * 1024 + lane * 16, u32x4{a1, a2, a3, a4});
    }
  }
  u32x4 Af[17], t2[3][2], t3[4];
  {
    const u32 fb = kb + (j & 3) * CPS - 8 * (j >> 2);
    const u32 fa = fb + 16 * q + (544 - 16 - 512) * 2;
#pragma unroll
    for (int n = 0; n < 17; ++n) Af[n] = rd2(fa + (16 - n) * 64, fa + (16 - n) * 64 + 8);
    const u32 ft = fb + 8 * q;
#pragma unroll
    for (int D = 2; D < 5; ++D) { u32 a = ft + (644 - 16 * D) * 2; t2[D - 2][0] = rd2(a, a + 136); t2[D - 2][1] = rd2(a + 272, a + 408); }
#pragma unroll
    for (int D = 4; D < 8; ++D) { u32 a = ft + (964 - 16 * D) * 2; t3[D - 4] = rd2(a, a + 168); }
  }
  ST(4);
  if (wv < 4) {
    asm volatile("s_waitcnt vmcnt(0)" ::: "memory");
    ST(10);
    const u32 ra = ul + (8 * q + 4 * ((j & 3) >> 1) + (j >> 2)) * 16 + (j & 1) * 8;
    const u32 wa = A_UT + (j & 7) * UT_STR + (256 + 512 * wv + 8 * q + 4 * (j >> 3)) * 2;
    s16x4 v[16];
#pragma unroll
    for (int i = 0; i < 16; ++i) v[i] = __builtin_amdgcn_ds_read_tr16_b64_v4i16((LAS s16x4*)(size_t)(ra + i * 512));
    SB;
#pragma unroll
    for (int i = 0; i < 16; ++i) ldsw(wa + i * 64, v[i]);
  }
  ST(5);
  bar();
  ST(6);
  f32x4 acc[8];
#pragma unroll
  for (int i = 0; i < 8; ++i) acc[i] = f32x4{0.f, 0.f, 0.f, 0.f};
  const f32x4 zz = {0.f, 0.f, 0.f, 0.f};
  const u32 ub = ut + (16 * j + 8 * q) * 2;
  const u32 yb = ut + (16 * j + 4 * q) * 2;
#define BADDR(k) (ub + (256 + 256 * ((k) >> 3) - 16 - 32 * ((k) & 7)) * 2)
  constexpr int NB = 4;
  u32x4 Bf[NB], W1;
#pragma unroll
  for (int k = 0; k < NB - 1; ++k) Bf[k] = ldsr<u32x4>(BADDR(k));
  W1 = ldsr<u32x4>(ub + (256 - 24) * 2);
  f32x4 V[6]; u32x4 vp[3];
  __amdgpu_buffer_rsrc_t orr = __builtin_amdgcn_make_buffer_rsrc(outp, 0, 2048 * 1152 * 4, 0x00020000);
  const u32 ora = A_UT + (4 * (q & 1) + (j >> 2)) * UT_STR + (32 * wv + 16 * (q >> 1) + 4 * (j & 3)) * 2;
  const u32 ov = ((32 * wv + 16 * (q >> 1) + j) * 1152 + d0 + 4 * (q & 1)) * 4;
  u32x2 ovv;
#define YSTAGE(t) ldsw(yb + (256 * (t)) * 2, u32x2{pack2(acc[t][0], acc[t][1]), pack2(acc[t][2], acc[t][3])})
#define YREAD(t) ovv = __builtin_bit_cast(u32x2, __builtin_amdgcn_ds_read_tr16_b64_v4i16((LAS s16x4*)(size_t)(ora + (t) * 512)))
#define YSTORE(t) __builtin_amdgcn_raw_buffer_store_b128(u32x4{ovv.x << 16, ovv.x & 0xffff0000u, ovv.y << 16, ovv.y & 0xffff0000u}, orr, ov, (t) * 256 * 4608, OAUXA)
#pragma unroll
  for (int e = 0; e < 8; ++e) {
#pragma unroll
    for (int b = 0; b < 8; ++b) {
      const int k = 8 * e + b;
      if (b == 3 && e > 0) { bar(); YREAD(e - 1); }
      if (k + NB - 1 < 64) Bf[(k + NB - 1) % NB] = ldsr<u32x4>(BADDR(k + NB - 1));
      if (b == 6 && e < 5) W1 = ldsr<u32x4>(ub + (256 + 256 * (e + 1) - 24) * 2);
      const u32x4 B = Bf[k % NB];
      acc[e] = mf16(Af[b], B, acc[e]);
      if (e < 7) acc[e + 1] = mf16(Af[8 + b], B, acc[e + 1]);
      if (b == 0) {
        if (e < 6) { V[0] = mf16(B, bnd[0], zz); V[1] = mf16(B, bnd[1], zz); V[2] = mf16(B, bnd[2], zz); V[3] = mf16(W1, bnd[3], zz); }
        if (e < 4) { V[4] = mf16(B, bnd[4], zz); V[5] = mf16(W1, bnd[5], zz); }
        if (e < 6) acc[e + 2] = mf16(Af[16], B, acc[e + 2]);
        if (e < 4) acc[e + 4] = mf16(afx, B, acc[e + 4]);
        if (e > 0) YSTAGE(e - 1);
      }
      if (b == 1 && e < 6) {
#pragma unroll
        for (int p = 0; p < 3; ++p) if (p < 2 || e < 4)
          vp[p] = u32x4{pack2(V[2 * p][0], V[2 * p][1]), pack2(V[2 * p][2], V[2 * p][3]), pack2(V[2 * p + 1][0], V[2 * p + 1][1]), pack2(V[2 * p + 1][2], V[2 * p + 1][3])};
      }
      if (b >= 2 && b <= 4 && e + b < 8) { acc[e + b] = mf16(vp[0], t2[b - 2][0], acc[e + b]); acc[e + b] = mf16(vp[1], t2[b - 2][1], acc[e + b]); }
      if (b >= 4 && e + b < 8) acc[e + b] = mf16(vp[2], t3[b - 4], acc[e + b]);
      if (b == 4 && e > 0) YSTORE(e - 1);
      __builtin_amdgcn_sched_barrier(0);
    }
  }
  YSTAGE(7);
  ST(7);
  bar();
  ST(8);
  YREAD(7); YSTORE(7);
  ST(9);

  STAMP_FLUSH(P);
}

DEV void pf_role(KA K, int k) {
  const u64 tb = __builtin_amdgcn_s_memrealtime();
  const int lane = threadIdx.x & 63, wv = __builtin_amdgcn_readfirstlane(threadIdx.x >> 6);
  const int g = blockIdx.x & 7, id = k * 8 + wv;
  STAMP_DECL
  KWAIT(K);
  char* const ws = kptr(K, 19);
  const char *c0 = kptr(K, 14), *c1 = kptr(K, 15);
  const u64 ep = load_epoch(ws);
#define CPULL() _Pragma("unroll 1") for (int i = id; i < 18 * CNS; i += 40) { \
    int pc = i + g * (2 * CNS + 2); pc = pc >= 18 * CNS ? pc - 18 * CNS : pc; \
    int m = pc >= 9 * CNS, q = pc - 9 * CNS * m, jj = q / CNS, e = (q - jj * CNS) * 64 + lane, row = e / CNS, sl = e - row * CNS + jj + 9 - CNS; \
    sl = sl >= 9 ? sl - 9 : sl; \
    TOUCH((m ? c1 : c0) + jj * (64 * 1152), wv * 256, row * 1152 + sl * 128); \
    asm volatile("s_waitcnt vmcnt(%0)" :: "n"(CPF - 1) : "memory"); }
  for (int it = 0; it < 4000 && __builtin_amdgcn_s_memrealtime() - tb < WDELAY; ++it) __builtin_amdgcn_s_sleep(2);
  ST(11);
  __amdgpu_buffer_rsrc_t hr = __builtin_amdgcn_make_buffer_rsrc(ws + WS_H, 0, H_BYTES, 0x00020000);
#pragma unroll 1
  for (int i = id; i < 576; i += 40) __builtin_amdgcn_raw_buffer_store_b128(u32x4{MARK, 0u, 0u, 0u}, hr, lane * 16, (g * 576 + i) * 1024, 16);
  ST(12);
  asm volatile("s_waitcnt vmcnt(0)" ::: "memory");
  ST(13);
  bar();
  if (threadIdx.x == 0) {
    __amdgpu_buffer_rsrc_t fr = __builtin_amdgcn_make_buffer_rsrc(ws + WS_FLAG, 0, 64 * 128, 0x00020000);
    __builtin_amdgcn_raw_buffer_store_b128(u32x4{0u, mix32((u32)ep), 0u, mix32((u32)(ep >> 32) ^ 0x5bd1e995u)}, fr, 0, (g * 8 + k) * 128, 16);
  }
  CPULL()
  asm volatile("s_waitcnt vmcnt(0)" ::: "memory");
  ST(10);
  STAMP_FLUSH(P);
}

__global__ __launch_bounds__(512, 2) void fused(Params P) {
  const int b = blockIdx.x, xcd = b & 7, slot = b >> 3;
  KA K;
  asm volatile("s_load_dwordx16 %1, %3, 0x40\n\ts_load_dwordx8 %2, %3, 0x80\n\ts_load_dwordx16 %0, %3, 0x0"
               : "=&s"(K.a), "=&s"(K.b), "=&s"(K.c) : "s"(__builtin_amdgcn_kernarg_segment_ptr()) : "memory");
  u64 pc = 0;
  asm volatile("s_getpc_b64 %0" : "=s"(pc));
  if (__builtin_expect(slot < 18, 1)) {
    g_role(K, 2 * xcd + (slot >= 9), slot >= 9 ? slot - 9 : slot, pc);
  } else if (slot < 27) {
    a_role(K, 72 * xcd + 8 * (slot - 18));
  } else pf_role(K, slot - 27);
}

extern "C" void kernel_launch(void* const* d_in, const int* in_sizes, int n_in, void* d_out, int out_size, void* d_ws, size_t ws_size, hipStream_t stream) {
  Params P;
  for (int i = 0; i < 18; ++i) P.in[i] = (const u16*)d_in[i];
  P.out = (float*)d_out; P.ws = (char*)d_ws;
  (void)hipFuncSetAttribute((const void*)fused, hipFuncAttributeMaxDynamicSharedMemorySize, LDS_BYTES);
#define GRID 256
  fused<<<GRID, 512, LDS_BYTES, stream>>>(P);
}
```

```cpp
#define XROT 2 OAUX=16 HS0=8 HS=3 PBAR=1 DEEP=8 CODEPF=3 ASLP=14 BSLP=3 CPF=4 CNS=9
#define WDELAY 100
#define OAUXA 2
#include <hip/hip_runtime.h>
#include <stdint.h>

typedef unsigned short u16; typedef unsigned u32; typedef unsigned long long u64;
typedef __bf16 bf16x8 __attribute__((ext_vector_type(8)));
typedef __bf16 bf16x2 __attribute__((ext_vector_type(2)));
typedef short s16x4 __attribute__((ext_vector_type(4)));
typedef float f32x16 __attribute__((ext_vector_type(16)));
typedef float f32x4 __attribute__((ext_vector_type(4)));
typedef float f32x2 __attribute__((ext_vector_type(2)));
typedef u32 u32x4 __attribute__((ext_vector_type(4)));
typedef u32 u32x2 __attribute__((ext_vector_type(2)));
#define LAS __attribute__((address_space(3)))
#define GAS __attribute__((address_space(1)))
#define DEV __device__ __forceinline__

struct Params { const u16* in[18]; float* out; char* ws; };
typedef u64 u64x8 __attribute__((ext_vector_type(8)));
typedef u64 u64x4 __attribute__((ext_vector_type(4)));
struct KA { u64x8 a, b; u64x4 c; };
#define KWAIT(k) asm volatile("s_waitcnt lgkmcnt(0)" :: "s"(k.a), "s"(k.b), "s"(k.c) : "memory")
__device__ __forceinline__ u64 kraw(const KA& k, int i) { return i < 8 ? k.a[i & 7] : i < 16 ? k.b[i & 7] : k.c[i & 3]; }
__device__ __forceinline__ char* kptr(const KA& k, int i) { char* p; asm volatile("s_mov_b64 %0, %1" : "=s"(p) : "s"(kraw(k, i))); return p; }

constexpr u32 WS_EPOCH = 0, WS_FLAG = 4096, WS_GRAN = 65536, WS_H = 262144;
constexpr u32 H_BYTES = 16u * 4u * 72u * 1024u;
constexpr u32 LDS_BYTES = 163840, SCR = 147456;
constexpr int SPIN_MAX = 1 << 16;
#define NWG 5
#define HS 1
#define LAUX 16
#define WAUX 16
#define OAUX 0

#define STAMP_DECL
#define ST(i)
#define ST0()
#define ST0F()
#define STAMP_FLUSH(P)

DEV float bf2f(u16 v) { return __uint_as_float(((u32)v) << 16); }
DEV u32 pack2(float a, float b) { bf16x2 r; r[0] = (__bf16)a; r[1] = (__bf16)b; return __builtin_bit_cast(u32, r); }
DEV float rbf(float f) { return (float)(__bf16)f; }
DEV void bar() { asm volatile("s_waitcnt lgkmcnt(0)\n\ts_barrier" ::: "memory"); }
DEV u32 mix32(u32 x) { x *= 0x9E3779B1u; x ^= x >> 15; x *= 0x85EBCA77u; x ^= x >> 13; return x + 0x632BE5ABu; }
DEV f32x2 cmul(f32x2 a, f32x2 b) { return f32x2{a.x * b.x - a.y * b.y, a.x * b.y + a.y * b.x}; }
DEV f32x2 cfma(f32x2 a, f32x2 b, f32x2 c) { return f32x2{fmaf(a.x, b.x, fmaf(-a.y, b.y, c.x)), fmaf(a.x, b.y, fmaf(a.y, b.x, c.y))}; }
DEV f32x2 cexp(float re, float im) {
  float e = __builtin_amdgcn_exp2f(re * 1.44269504088896341f);
  float r = __builtin_amdgcn_fractf(im * 0.15915494309189535f);
  return f32x2{e * __builtin_amdgcn_cosf(r), e * __builtin_amdgcn_sinf(r)};
}
DEV void glds4(const char* gb, u32 ldsb, u32 v0, u32 v1, u32 v2, u32 v3) {
  u32 keep;
  asm volatile("s_mov_b32 %0, m0\n\ts_mov_b32 m0, %2\n\ts_nop 0\n\t"
               "global_load_lds_dwordx4 %3, %1\n\tglobal_load_lds_dwordx4 %4, %1 offset:1024\n\t"
               "global_load_lds_dwordx4 %5, %1 offset:2048\n\tglobal_load_lds_dwordx4 %6, %1 offset:3072\n\ts_mov_b32 m0, %0"
               : "=&s"(keep) : "s"(gb), "s"(ldsb), "v"(v0), "v"(v1), "v"(v2), "v"(v3) : "memory");
}
DEV void glds1(const char* gb, u32 ldsb, u32 v0) {
  u32 keep;
  asm volatile("s_mov_b32 %0, m0\n\ts_mov_b32 m0, %2\n\ts_nop 0\n\tglobal_load_lds_dwordx4 %3, %1\n\ts_mov_b32 m0, %0"
               : "=&s"(keep) : "s"(gb), "s"(ldsb), "v"(v0) : "memory");
}
template <class T> DEV T ldsr(u32 a) { return *(const LAS T*)(size_t)a; }
template <class T> DEV void ldsw(u32 a, T v) { *(LAS T*)(size_t)a = v; }
DEV f32x2 pr(const f32x16& a, int m) { return f32x2{a[2 * m], a[2 * m + 1]}; }

#define RA 1
DEV void swap16(float x, float& lo, float& hi) { auto r = __builtin_amdgcn_permlane16_swap(__float_as_uint(x), __float_as_uint(x), false, false); lo = __uint_as_float(r[0]); hi = __uint_as_float(r[1]); }
DEV void swap32(float x, float& lo, float& hi) { auto r = __builtin_amdgcn_permlane32_swap(__float_as_uint(x), __float_as_uint(x), false, false); lo = __uint_as_float(r[0]); hi = __uint_as_float(r[1]); }
DEV int wrap9(int v) { return v >= 9 ? v - 9 : v; }
DEV u64 load_epoch(const char* ws) { u64 ep; asm volatile("s_load_dwordx2 %0, %1, 0x0\n\ts_waitcnt lgkmcnt(0)" : "=s"(ep) : "s"(ws) : "memory"); return ep; }
DEV void touch64(const char* gb, u32 ldsb, u32 v0) {
  u32 keep;
  asm volatile("s_mov_b32 %0, m0\n\ts_mov_b32 m0, %2\n\ts_nop 0\n\tglobal_load_lds_dword %3, %1\n\ts_mov_b32 m0, %0"
               : "=&s"(keep) : "s"(gb), "s"(ldsb), "v"(v0) : "memory");
}

DEV void touch64s(const char* gb, u32 ldsb, u32 v0) {
  u32 keep;
  asm volatile("s_mov_b32 %0, m0\n\ts_mov_b32 m0, %2\n\ts_nop 0\n\tglobal_load_lds_dword %3, %1 sc1\n\ts_mov_b32 m0, %0"
               : "=&s"(keep) : "s"(gb), "s"(ldsb), "v"(v0) : "memory");
}
#define TOUCH touch64
#define MM(h, cc, acc) __builtin_amdgcn_mfma_f32_32x32x16_bf16(h, cc, acc, 0, 0, 0)
#define SB __builtin_amdgcn_sched_barrier(0)
#define WL(n) __builtin_amdgcn_s_waitcnt(0xC07F | ((n) << 8))
constexpr u32 HREG = 131072, MARK = 0x80000000u;
DEV void g_role(KA K, int c, int j, u64 pc) {
  u32 pl = 0, pr_ = 0, pi_ = 0;
  const int lane = threadIdx.x & 63, wv = __builtin_amdgcn_readfirstlane(threadIdx.x >> 6);
  STAMP_DECL
  ST0();
  const int rot = (j + 4 * (c & 1) + XROT * (c >> 1)) % 9;
  const int l15 = lane & 15, q4 = lane >> 4;
  const int w = wv & 3, l31 = lane & 31, hh = lane >> 5;
  f32x16 acc0, acc1;
  u32 vb[4], aoff[4], ca[4];
  const char* c8;
  u64 ep;
  constexpr int PF = 8, CD = 3;
  u32x4 hf[PF];
  bf16x8 cf[CD + 1][2];
#define CADDR(k, h) (ca[(k) & 3] + (((k) >> 2) & 1) * 8192 + (h) * 4096)
#define PIN(x) asm volatile("" : "+s"(x))
#define HLOAD(o, k) __builtin_amdgcn_raw_buffer_load_b128(hr, ho + ((k) & 3) * 1024, (o) + (((k) >> 2) & 1) * 4096, LAUX)
  const int grp = wv >> 2;
  u32 he, hc, hn, cs, cl, l8, ho, ho4;
#define PINS() do { he = (c * 4 + w) * 72 * 1024; hc = he + wrap9(j + 1 + grp) * 8192; hn = he + wrap9(j + 3 + grp) * 8192; he += 9 * 8192; \
    cs = (3 + grp) * 16384; cl = grp ? HREG : 16384; l8 = HREG + w * 4096; ho = lane * 16; ho4 = ho + 4096; \
    asm volatile("" : "+s"(he), "+s"(hc), "+s"(hn), "+s"(cs), "+s"(cl), "+s"(l8)); asm volatile("" : "+v"(ho), "+v"(ho4)); } while (0)
  u32 vx[4], ko0 = rot * 128, ko1 = wrap9(rot + 1) * 128, sl0 = w * 4096;
  if (wv >= 4) {
#pragma unroll
    for (int q = 0; q < 4; ++q) {
      int i = 2 * w + (q >> 1), qq = 2 * (q & 1) + (lane >> 5), r = (lane >> 3) & 3;
      int time = 32 * qq + 16 * (r & 1) + 2 * i + (r >> 1);
      u32 ch = (u32)(((lane & 7) ^ ((4 * (q & 1) + (lane >> 4)) & 7)) << 4) + 3072 - 1024 * q;
      vx[q] = (128 * c + time) * 1152 + ch;
      vb[q] = (64 * j + 32 * (w & 1) + 8 * q + (lane >> 3)) * 1152 + ch;
    }
  } else {
#pragma unroll
    for (int q = 0; q < 4; ++q) { vx[q] = (64 * j + 16 * wv + l15) * 2; vb[q] = 0; }
  }
  asm volatile("" : "+v"(vx[0]), "+v"(vx[1]), "+v"(vx[2]), "+v"(vx[3]), "+v"(vb[0]), "+v"(vb[1]), "+v"(vb[2]), "+v"(vb[3]));
  asm volatile("" : "+s"(ko0), "+s"(ko1), "+s"(sl0));
  KWAIT(K);
  ST0F();
  char* ws;
  __amdgpu_buffer_rsrc_t hr;
  if (wv < 4)
    asm volatile("global_load_ushort %0, %3, %4\n\tglobal_load_ushort %1, %3, %5\n\tglobal_load_ushort %2, %3, %6"
                 : "=&v"(pl), "=&v"(pr_), "=&v"(pi_) : "v"(vx[0]), "s"(kraw(K, 16)), "s"(kraw(K, 10)), "s"(kraw(K, 11)) : "memory");
  if (__builtin_expect(wv >= 4, 1)) {
    const char* xb = kptr(K, 9) - 3072;
    glds4(xb + ko0, sl0, vx[0], vx[1], vx[2], vx[3]);
    const char *b0 = kptr(K, 12), *b1 = kptr(K, 13), *bb = (w < 2 ? b0 : b1) - 3072;
    glds4(bb + ko0, sl0 + 16384, vb[0], vb[1], vb[2], vb[3]);
    glds4(xb + ko1, sl0 + 32768, vx[0], vx[1], vx[2], vx[3]);
    glds4(bb + ko1, sl0 + 49152, vb[0], vb[1], vb[2], vb[3]);
    const char *c0 = kptr(K, 14), *c1 = kptr(K, 15), *cb = (w < 2 ? c0 : c1) - 3072;
    ws = kptr(K, 19);
    hr = __builtin_amdgcn_make_buffer_rsrc(ws + WS_H, 0, H_BYTES, 0x00020000);
    ST(8);
    u32 dk0, dk1;
    asm volatile("global_load_ushort %0, %2, %3\n\tglobal_load_ushort %1, %2, %3 offset:64" : "=&v"(dk0), "=&v"(dk1) : "v"((64 * j + l31) * 2), "s"(kraw(K, 17)) : "memory");
    ST(9);
    s16x4 xs[8];
    asm volatile("s_waitcnt vmcnt(10)" ::: "memory");
    const char* ce = cb + ((w & 1) ? -32 * 1152 : 32 * 1152);
#define CPIECE(i) glds4((((i) & 1) ? cb : ce) + wrap9(j + (i)) * 128, (((i) + 2) & 7) * 16384 + w * 4096, vb[0], vb[1], vb[2], vb[3])
    int kn = wrap9(rot + 2);
#pragma unroll 1
    for (int s = 0; s < 9; ++s) {
      bar();
      if (s <= 6) {
        u32 sl = ((s + 2) & 3) * 32768 + w * 4096;
        glds4(xb + kn * 128, sl, vx[0], vx[1], vx[2], vx[3]);
        glds4(bb + kn * 128, sl + 16384, vb[0], vb[1], vb[2], vb[3]);
      } else { CPIECE(2 * s - 14); CPIECE(2 * s - 13); }
      if (wrap9(rot + s) == j) {
        const int e = (lane >> 2) & 3, pp = lane & 3;
        u32 sb = (s & 3) * 32768 + (32 * hh + 4 * w + 16 * (e >> 1) + 2 * (e & 1)) * 128 + (((2 * ((lane >> 4) & 1) + (pp >> 1)) ^ ((w << 1) | (e & 1))) << 4) + (pp & 1) * 8;
#pragma unroll
        for (int g = 0; g < 4; ++g) {
          xs[g] = __builtin_amdgcn_ds_read_tr16_b64_v4i16((LAS s16x4*)(size_t)(sb + (64 * (g & 1) + (g >> 1)) * 128));
          xs[4 + g] = __builtin_amdgcn_ds_read_tr16_b64_v4i16((LAS s16x4*)(size_t)((sb ^ 64) + (64 * (g & 1) + (g >> 1)) * 128));
        }
      }
      kn = wrap9(kn + 1);
      if (s < 8) asm volatile("s_waitcnt vmcnt(8)" ::: "memory");
    }
    bar();
    ST(3);
    asm volatile("" : "+v"(dk0), "+v"(dk1));
    const float dsk0 = __uint_as_float(dk0 << 16), dsk1 = __uint_as_float(dk1 << 16);
#pragma unroll
    for (int i = 4; i < 8; ++i) CPIECE(i);
#pragma unroll
    for (int r = 0; r < 16; ++r) {
      acc1[r] = rbf(bf2f((u16)xs[r >> 2][r & 3]) * dsk0);
      acc0[r] = rbf(bf2f((u16)xs[4 + (r >> 2)][r & 3]) * dsk1);
    }
#pragma unroll
    for (int s = 0; s < 4; ++s) aoff[s] = l31 * 128 + (((2 * s + hh) ^ ((l31 >> 1) & 7)) << 4);
    u32 ra[2];
#pragma unroll
    for (int u = 0; u < 2; ++u) {
      int row = 8 * (q4 >> 1) + 4 * u + (l15 >> 2);
      ra[u] = HREG + w * 4096 + row * 64 + (((2 * (q4 & 1) + ((l15 & 3) >> 1)) ^ ((row >> 1) & 3)) << 4) + (l15 & 1) * 8;
    }
    asm volatile("" : "+v"(acc0), "+v"(acc1), "+v"(ra[0]), "+v"(ra[1]), "+v"(aoff[0]), "+v"(aoff[1]), "+v"(aoff[2]), "+v"(aoff[3]));
#pragma unroll
    for (int s = 0; s < 4; ++s) ca[s] = aoff[s] + 2 * 16384;
    c8 = ce + wrap9(j + 8) * 128;
    asm volatile("" : "+s"(c8));
    PINS();
    ep = 0;
    if (c == 15 && j == 0 && wv == 4) {
      ep = load_epoch(ws);
      GAS u64* fp = (GAS u64*)(ws + WS_FLAG + ((lane >> 3) * 8 + ((lane & 7) < NWG - 1 ? (lane & 7) : NWG - 1)) * 128);
      const u64 ta = (u64)mix32((u32)ep) << 32, tb = (u64)mix32((u32)(ep >> 32) ^ 0x5bd1e995u) << 32;
#pragma unroll 1
      for (int it = 0; it < SPIN_MAX; ++it) {
        if (__all(__hip_atomic_load(fp, __ATOMIC_RELAXED, __HIP_MEMORY_SCOPE_AGENT) == ta && __hip_atomic_load(fp + 1, __ATOMIC_RELAXED, __HIP_MEMORY_SCOPE_AGENT) == tb)) break;
        __builtin_amdgcn_s_sleep(4);
      }
    }
    asm volatile("s_waitcnt vmcnt(0)" ::: "memory");
    ST(1);
    bar();
    ST(4);
#define hx hf
#pragma unroll
    for (int k = 0; k < 8; ++k) {
      u32x2 l2 = __builtin_bit_cast(u32x2, __builtin_amdgcn_ds_read_tr16_b64_v4i16((LAS s16x4*)(size_t)(ra[0] + (k & 3) * 1024 + (k >> 2) * 16384)));
      u32x2 h2 = __builtin_bit_cast(u32x2, __builtin_amdgcn_ds_read_tr16_b64_v4i16((LAS s16x4*)(size_t)(ra[1] + (k & 3) * 1024 + (k >> 2) * 16384)));
      hx[k] = u32x4{l2.x, l2.y, h2.x, h2.y};
      if (k < CD) { cf[k][0] = ldsr<bf16x8>(ca[k]); cf[k][1] = ldsr<bf16x8>(ca[k] + 4096); }
    }
    SB;
#pragma unroll
    for (int k = 0; k < 8; ++k) {
      bf16x8 a = __builtin_bit_cast(bf16x8, hx[k]);
      acc0 = MM(a, cf[k & 3][0], acc0);
      asm volatile("" : "+v"(acc0)); SB;
      if (k + CD < 8) cf[(k + CD) & 3][0] = ldsr<bf16x8>(CADDR(k + CD, 0));
      SB;
      acc1 = MM(a, cf[k & 3][1], acc1);
      asm volatile("" : "+v"(acc1)); SB;
      if (k + CD < 8) cf[(k + CD) & 3][1] = ldsr<bf16x8>(CADDR(k + CD, 1));
      SB;
    }
#pragma unroll
    for (int q = 0; q < 7; ++q) { hf[q] = __builtin_amdgcn_raw_buffer_load_b128(hr, (q < 4 ? ho : ho4) + (q & 3) * 1024, hc, LAUX); SB; }
  }
  int wq = wv;
  asm volatile("" : "+s"(wq));
  if (__builtin_expect(wq < 4, 1)) {
    asm volatile("s_load_dwordx2 %0, %1, 0x0" : "=&s"(ep) : "s"(kraw(K, 19)) : "memory");
    ws = kptr(K, 19);
    hr = __builtin_amdgcn_make_buffer_rsrc(ws + WS_H, 0, H_BYTES, 0x00020000);
    if (wv == 0 && (blockIdx.x >> 3) < CODEPF) {
      touch64((const char*)(pc & ~(u64)127), SCR, (blockIdx.x >> 3) * 8192 + lane * 128);
      asm volatile("s_waitcnt vmcnt(1)" ::: "memory");
    } else asm volatile("s_waitcnt vmcnt(0)" ::: "memory");
    asm volatile("" : "+v"(pl), "+v"(pr_), "+v"(pi_) :: "memory");
    u32 hbase = (c * 4 * 72 + j * 8 + wv) * 1024;
    float dt = rbf(__builtin_amdgcn_exp2f(__uint_as_float(pl << 16) * 1.44269504088896341f));
    float Lr = __uint_as_float(pr_ << 16), Li = __uint_as_float(pi_ << 16);
    f32x2 lam = cexp(dt * Lr, dt * Li);
    float inv = 2.f / (Lr * Lr + Li * Li);
    f32x2 coef = cmul(f32x2{lam.x - 1.f, lam.y}, f32x2{Lr * inv, -Li * inv});
    f32x2 mu = cmul(lam, lam); mu = cmul(mu, mu); mu = cmul(mu, mu); mu = cmul(mu, mu);
    f32x2 nu = cmul(mu, mu), nu2 = cmul(nu, nu), La = cmul(nu2, nu2);
    f32x2 La2 = cmul(La, La), La4 = cmul(La2, La2);
    f32x2 one = {1.f, 0.f};
    f32x2 Lg = cmul((q4 & 2) ? La2 : one, (q4 & 1) ? La : one);
    f32x2 nuq = (q4 & 1) ? nu : one;
    f32x2 cm0 = cmul(coef, cmul((q4 & 2) ? nu2 : one, nuq)), cm1 = cmul(cm0, mu);
    asm volatile("s_waitcnt lgkmcnt(0)" : "+s"(ep) :: "memory");
    u32 tlo = mix32((u32)ep), thi = mix32((u32)(ep >> 32) ^ 0x5bd1e995u);
    asm volatile("" :: "s"(tlo), "s"(thi));
    asm volatile("" : "+v"(lam), "+v"(coef), "+v"(mu), "+v"(La4), "+v"(Lg), "+v"(cm0), "+v"(cm1), "+v"(nu), "+v"(nu2), "+v"(nuq));
    f32x4 ar[8], ai[8];
#pragma unroll
    for (int i = 0; i < 8; ++i) { ar[i] = f32x4{0.f, 0.f, 0.f, 0.f}; ai[i] = ar[i]; asm volatile("" : "+v"(ar[i]), "+v"(ai[i])); }
    ST(1);
    u32 xa0 = l15 * 128 + ((q4 ^ (l15 >> 1)) << 4), xa1 = xa0 ^ 64, ba0 = xa0 + 16384 + wv * 2048, ba1 = ba0 ^ 64;
    bf16x8 A[2][8], Bq[2][2];
#define STEP(cs, RD, xa, ba, G10, G11, G12, G13) do { WL(4); SB; \
      _Pragma("unroll") for (int i = 0; i < 8; ++i) { if (i == 4) { if (RD) WL(8); else WL(0); SB; } \
        ar[i] = __builtin_amdgcn_mfma_f32_16x16x32_bf16(A[cs][i], Bq[cs][0], ar[i], 0, 0, 0); asm volatile("" : "+v"(ar[i])); SB; \
        if (RD) { if (i == 0) Bq[cs ^ 1][0] = ldsr<bf16x8>(ba); else if (i < 5) A[cs ^ 1][2 * i - 2] = ldsr<bf16x8>(xa + (2 * i - 2) * 2048); } \
        if (i == 5) { G10; } if (i == 6) { G12; } SB; \
        ai[i] = __builtin_amdgcn_mfma_f32_16x16x32_bf16(A[cs][i], Bq[cs][1], ai[i], 0, 0, 0); asm volatile("" : "+v"(ai[i])); SB; \
        if (RD) { if (i == 0) Bq[cs ^ 1][1] = ldsr<bf16x8>(ba + 8192); else if (i < 5) A[cs ^ 1][2 * i - 1] = ldsr<bf16x8>(xa + (2 * i - 1) * 2048); } \
        if (i == 5) { G11; } if (i == 6) { G13; } SB; } } while (0)
#define ADV(v) do { v = (v + 32768) & 131071; asm volatile("" : "+v"(v)); } while (0)
    bar();
    ST(2);
    Bq[0][0] = ldsr<bf16x8>(ba0); Bq[0][1] = ldsr<bf16x8>(ba0 + 8192);
#pragma unroll
    for (int i = 0; i < 8; ++i) A[0][i] = ldsr<bf16x8>(xa0 + i * 2048);
    SB;
#pragma unroll 1
    for (int s = 0; s < 8; ++s) {
      STEP(0, 1, xa1, ba1, ADV(xa0), ADV(ba0), asm volatile("s_barrier" ::: "memory"), );
      STEP(1, 1, xa0, ba0, ADV(xa1), ADV(ba1), , );
    }
    STEP(0, 1, xa1, ba1, , , , bar());
    STEP(1, 0, xa0, ba0, asm volatile("s_waitcnt vmcnt(0)" ::: "memory"), , , );
    ST(3);
    const f32x2 lr2 = {lam.x, lam.x}, li2 = {lam.y, lam.y};
    f32x2 Br[16], Bi[16];
#pragma unroll
    for (int i = 0; i < 8; ++i) { Br[2 * i] = f32x2{ar[i][0], ar[i][1]}; Br[2 * i + 1] = f32x2{ar[i][2], ar[i][3]}; Bi[2 * i] = f32x2{ai[i][0], ai[i][1]}; Bi[2 * i + 1] = f32x2{ai[i][2], ai[i][3]}; }
    f32x2 sr = Br[0], si = Bi[0];
#pragma unroll
    for (int k = 1; k < 16; ++k) {
      f32x2 t1 = __builtin_elementwise_fma(lr2, sr, Br[k]), t2 = __builtin_elementwise_fma(lr2, si, Bi[k]);
      f32x2 n1 = __builtin_elementwise_fma(-li2, si, t1), n2 = __builtin_elementwise_fma(li2, sr, t2);
      sr = n1; si = n2;
    }
    const f32x2 S0 = {sr.x, si.x};
    f32x2 T = cfma(mu, S0, f32x2{sr.y, si.y}), lo, hi;
    { float a, b; swap16(T.x, a, b); lo.x = a; hi.x = b; swap16(T.y, a, b); lo.y = a; hi.y = b; }
    f32x2 U = cfma(nu, lo, hi), E = (q4 & 1) ? lo : f32x2{0.f, 0.f};
    { float a, b; swap32(U.x, a, b); lo.x = a; hi.x = b; swap32(U.y, a, b); lo.y = a; hi.y = b; }
    f32x2 tot = cfma(nu2, lo, hi);
    if (q4 == 0 && c < 15) {
      u32x4 gv = {__float_as_uint(tot.x), tlo, __float_as_uint(tot.y), thi};
      __amdgpu_buffer_rsrc_t gr = __builtin_amdgcn_make_buffer_rsrc(ws + WS_GRAN, 0, 9 * 16 * 1024, 0x00020000);
      __builtin_amdgcn_raw_buffer_store_b128(gv, gr, l15 * 16, (j * 16 + c) * 1024 + wv * 256, 16);
    }
#pragma unroll
    for (int k = 0; k < 16; ++k) asm volatile("" : "+v"(Br[k]), "+v"(Bi[k]) :: "memory");
    ST(4);
    if (q4 & 2) E = cfma(nuq, lo, E);
    f32x2 La_ = cmul(coef, E), Lb_ = cmul(coef, cfma(mu, E, S0));
    const f32x2 cr2 = {coef.x, coef.x}, ci2 = {coef.y, coef.y};
#pragma unroll
    for (int k = 0; k < 16; ++k) {
      f32x2 n1 = __builtin_elementwise_fma(-ci2, Bi[k], cr2 * Br[k]), n2 = __builtin_elementwise_fma(ci2, Br[k], cr2 * Bi[k]);
      Br[k] = n1; Bi[k] = n2;
    }
#pragma unroll
    for (int k = 0; k < 16; ++k) asm volatile("" : "+v"(Br[k]), "+v"(Bi[k]));
    asm volatile("" : "+v"(La_), "+v"(Lb_));
    const u32 wa = HREG + q4 * 4096 + wv * 1024 + l15 * 64;
    u32 wa4[4];
#pragma unroll
    for (int q = 0; q < 4; ++q) wa4[q] = wa + ((q ^ ((l15 >> 1) & 3)) << 4);
    asm volatile("" : "+v"(wa4[0]), "+v"(wa4[1]), "+v"(wa4[2]), "+v"(wa4[3]));
    u32 ra[2];
#pragma unroll
    for (int u = 0; u < 2; ++u) {
      int row = 8 * (q4 >> 1) + 4 * u + (l15 >> 2);
      ra[u] = HREG + wv * 1024 + row * 64 + (((2 * (q4 & 1) + ((l15 & 3) >> 1)) ^ ((row >> 1) & 3)) << 4) + (l15 & 1) * 8;
    }
#pragma unroll
    for (int s = 0; s < 4; ++s) aoff[s] = l31 * 128 + (((2 * s + hh) ^ ((l31 >> 1) & 7)) << 4);
    c8 = ws;
#pragma unroll
    for (int r = 0; r < 16; ++r) { acc0[r] = 0.f; acc1[r] = 0.f; }
    asm volatile("" : "+v"(ra[0]), "+v"(ra[1]), "+v"(aoff[0]), "+v"(aoff[1]), "+v"(aoff[2]), "+v"(aoff[3]), "+v"(acc0), "+v"(acc1));
    PINS(); PIN(hbase);
    ST(8);
    f32x2 H;
    {
      const char* gb = ws;
      u32 go[4];
      const u32 fo = WS_FLAG + ((c >> 1) * 8 + ((lane & 7) < NWG - 1 ? (lane & 7) : NWG - 1)) * 128;
#pragma unroll
      for (int n = 0; n < 4; ++n) { int cs = c - 1 - q4 - 4 * n; go[n] = cs >= 0 ? WS_GRAN + j * 16384 + wv * 256 + cs * 1024 + l15 * 16 : fo; }
      u32x4 g0, g1, g2, g3;
#pragma unroll 1
      for (int it = 0; it < SPIN_MAX; ++it) {
        asm volatile("global_load_dwordx4 %0, %4, %8 sc1\n\tglobal_load_dwordx4 %1, %5, %8 sc1\n\tglobal_load_dwordx4 %2, %6, %8 sc1\n\tglobal_load_dwordx4 %3, %7, %8 sc1\n\t"
                     "s_waitcnt vmcnt(0)"
                     : "=&v"(g0), "=&v"(g1), "=&v"(g2), "=&v"(g3) : "v"(go[0]), "v"(go[1]), "v"(go[2]), "v"(go[3]), "s"(gb) : "memory");
        u32 bad = (((g0.y ^ tlo) | (g1.y ^ tlo)) | ((g2.y ^ tlo) | (g3.y ^ tlo))) | (((g0.w ^ thi) | (g1.w ^ thi)) | ((g2.w ^ thi) | (g3.w ^ thi)));
        if (!__any(bad != 0)) break;
        __builtin_amdgcn_s_sleep(HS);
      }
      f32x2 s3 = {__uint_as_float(g3.x), __uint_as_float(g3.z)}, s2 = {__uint_as_float(g2.x), __uint_as_float(g2.z)};
      f32x2 s1 = {__uint_as_float(g1.x), __uint_as_float(g1.z)}, s0 = {__uint_as_float(g0.x), __uint_as_float(g0.z)};
      H = cmul(Lg, cfma(La4, cfma(La4, cfma(La4, s3, s2), s1), s0));
      float a, b;
      swap16(H.x, a, b); H.x = a + b; swap16(H.y, a, b); H.y = a + b;
      swap32(H.x, a, b); H.x = a + b; swap32(H.y, a, b); H.y = a + b;
    }
    ST(5);
    f32x2 sa = cfma(cm0, H, La_), sb_ = cfma(cm1, H, Lb_);
    sr = f32x2{sa.x, sb_.x}; si = f32x2{sa.y, sb_.y};
#define HPK(g, hf) u32x4{HP1(g, 8 * (hf)), HP1(g, 8 * (hf) + 2), HP1(g, 8 * (hf) + 4), HP1(g, 8 * (hf) + 6)}
#define HP1(g, k) (((g) >> 1) ? pack2(-Bi[k][(g) & 1], -Bi[(k) + 1][(g) & 1]) : pack2(Br[k][(g) & 1], Br[(k) + 1][(g) & 1]))
    u32x4 hv[8];
#pragma unroll
    for (int k = 0; k < 16; ++k) {
      f32x2 t1 = __builtin_elementwise_fma(lr2, sr, Br[k]), t2 = __builtin_elementwise_fma(lr2, si, Bi[k]);
      f32x2 n1 = __builtin_elementwise_fma(-li2, si, t1), n2 = __builtin_elementwise_fma(li2, sr, t2);
      sr = n1; si = n2; Br[k] = sr; Bi[k] = si;
      if (k >= 8 && !(k & 1)) { const int g = (k - 8) >> 1; SB; hv[g] = HPK(g, 0); ldsw(wa4[2 * (g & 1)] + (g >> 1) * 16384, hv[g]); SB; }
    }
#pragma unroll
    for (int g = 0; g < 4; ++g) { hv[4 + g] = HPK(g, 1); asm volatile("" : "+v"(hv[4 + g])); ldsw(wa4[2 * (g & 1) + 1] + (g >> 1) * 16384, hv[4 + g]); SB; }
    asm volatile("" :: "v"(hv[0]), "v"(hv[1]), "v"(hv[2]), "v"(hv[3]), "v"(hv[4]), "v"(hv[5]), "v"(hv[6]), "v"(hv[7]));
#pragma unroll
    for (int q = 0; q < 8; ++q) {
      u32x2 l2 = __builtin_bit_cast(u32x2, __builtin_amdgcn_ds_read_tr16_b64_v4i16((LAS s16x4*)(size_t)(ra[0] + (q & 3) * 4096 + (q >> 2) * 16384)));
      u32x2 h2 = __builtin_bit_cast(u32x2, __builtin_amdgcn_ds_read_tr16_b64_v4i16((LAS s16x4*)(size_t)(ra[1] + (q & 3) * 4096 + (q >> 2) * 16384)));
      hv[q] = u32x4{l2.x, l2.y, h2.x, h2.y}; SB;
    }
#pragma unroll
    for (int q = 0; q < 8; ++q) {
      hv[q].x = (int)hv[q].x > (int)(MARK + 1) ? hv[q].x : MARK + 1;
      __builtin_amdgcn_raw_buffer_store_b128(hv[q], hr, ho, hbase + ((q & 3) * 72 + (q >> 2) * 4) * 1024, 16); SB;
    }
    ST(6);
    bar();
#pragma unroll
    for (int q = 0; q < 7; ++q) { hf[q] = __builtin_amdgcn_raw_buffer_load_b128(hr, (q < 4 ? ho : ho4) + (q & 3) * 1024, hc, LAUX); SB; }
  }
  ST(10);
  {
#define SLP 4
#define BARI 19
#define NOCHK 0
    u32 hp = 0, cp = 0;
#define CNT()
#define RST()
#define IMIN(a, b) ((int)(a) < (int)(b) ? (a) : (b))
#define ANY7(t, o) __any(IMIN(IMIN(IMIN(t[o].x, t[o + 1].x), IMIN(t[o + 2].x, t[o + 3].x)), IMIN(IMIN(t[o + 4].x, t[o + 5].x), t[o + 6].x)) == MARK)
#pragma unroll
    for (int q = 0; q < CD; ++q) { ca[q] = aoff[q] + cs; cf[q][0] = ldsr<bf16x8>(CADDR(q, 0)); cf[q][1] = ldsr<bf16x8>(CADDR(q, 1)); }
    SB;
    ST(11);
    u64 mk[8], any = 0;
#pragma unroll
    for (int q = 0; q < 8; ++q) mk[q] = 0;
#define MARKED(q) do { mk[q] = __ballot(hf[q].x == MARK); PIN(mk[q]); } while (0)
#define REDO(o, cb_) do { u32x4 t[8]; \
        _Pragma("unroll 1") for (int it = 0; it < SPIN_MAX; ++it) { \
          _Pragma("unroll") for (int q = 1; q < 8; ++q) t[q] = HLOAD(o, q); \
          if (!ANY7(t, 1)) break; \
          __builtin_amdgcn_s_sleep(SLP); } \
        _Pragma("unroll") for (int q = 1; q < 8; ++q) if (mk[q] != 0) { \
          bf16x8 a2 = __builtin_bit_cast(bf16x8, ((mk[q] >> lane) & 1) ? t[q] : u32x4{0u, 0u, 0u, 0u}); \
          const u32 cq = aoff[q & 3] + (cb_) + (q >> 2) * 8192; \
          acc0 = MM(a2, ldsr<bf16x8>(cq), acc0); \
          acc1 = MM(a2, ldsr<bf16x8>(cq + 4096), acc1); } } while (0)
#define FIX0() do { PIN(mk[0]); if (__builtin_expect((any | mk[0]) != 0, 0)) { CNT(); \
        if (any != 0) REDO(hp, cp); \
        if (mk[0] != 0) { \
          _Pragma("unroll 1") for (int it = 0; it < SPIN_MAX; ++it) { hf[0] = HLOAD(hc, 0); if (!__any(hf[0].x == MARK)) break; __builtin_amdgcn_s_sleep(SLP); } \
          bf16x8 a2 = __builtin_bit_cast(bf16x8, ((mk[0] >> lane) & 1) ? hf[0] : u32x4{0u, 0u, 0u, 0u}); \
          acc0 = MM(a2, cf[0][0], acc0); \
          acc1 = MM(a2, cf[0][1], acc1); SB; \
          _Pragma("unroll") for (int r = 1; r < 8; ++r) { hf[r] = HLOAD(hc, r); SB; } } } } while (0)
    u32 xa_ = (grp ? HREG + 16384 : 2 * 16384) + w * 4096 + lane * 16, xb_ = (grp ? 2 * 16384 : HREG + 16384) + w * 4096 + lane * 16;
    u32 ov = ((128 * c + 32 * w + 4 * hh) * 1152 + 576 + 64 * j + 32 * grp + l31) * 4;
    asm volatile("" : "+v"(ov));
#define AS 0
    asm volatile("" : "+v"(xa_), "+v"(xb_));
    f32x4 px[2][4];
#define XK 5
#define PIECE(NX) _Pragma("unroll") for (int k = 0; k < 8; ++k) { \
      bf16x8 a = __builtin_bit_cast(bf16x8, hf[k]); \
      const int kn2 = k + CD; \
      acc0 = MM(a, cf[k & 3][0], acc0); \
      asm volatile("" : "+v"(acc0)); SB; \
      if (!NOCHK) { MARKED(k); SB; } \
      if (kn2 >= 8 && NX) ca[kn2 & 3] = aoff[kn2 & 3] + cs; \
      if (k == 0) ca[3] = aoff[3] + cs; \
      if (kn2 < 8 || NX) cf[kn2 & 3][0] = ldsr<bf16x8>(CADDR(kn2, 0)); \
      if (k == 0) hf[7] = HLOAD(hc, 7); \
      if (k >= 1 && NX) hf[k - 1] = HLOAD(hn, k - 1); \
      SB; \
      acc1 = MM(a, cf[k & 3][1], acc1); \
      asm volatile("" : "+v"(acc1)); SB; \
      if (!NOCHK && k >= 1) { PIN(mk[k]); any = k == 1 ? mk[1] : any | mk[k]; PIN(any); SB; } \
      if (kn2 < 8 || NX) cf[kn2 & 3][1] = ldsr<bf16x8>(CADDR(kn2, 1)); \
      if (NX) { \
        if (k == 1) { cp = cs; cs = n == 2 ? cl : (cs + 32768) & 131071; PIN(cs); PIN(cp); } \
        if (k == 2) { if (n == 0 && grp) glds4(c8, l8, vb[0], vb[1], vb[2], vb[3]); } \
        if (k == 3) { if (n == 2) asm volatile("s_barrier" ::: "memory"); } } \
      if (AS && !NX && k == XK) WL(0); \
      if (AS && !NX && k == XK && grp) { asm volatile("s_barrier" ::: "memory"); \
        _Pragma("unroll") for (int q = 0; q < 4; ++q) { px[0][q] = ldsr<f32x4>(xa_ + q * 1024); px[1][q] = ldsr<f32x4>(xb_ + q * 1024); } } \
      if (!NOCHK && k == 0) FIX0(); \
      if (k == 7 && NX) { RST(); hp = hc; hc = hn; hn += 16384; hn = hn >= he ? hn - 9 * 8192 : hn; PIN(hc); PIN(hn); } \
      SB; }
#define PRI 0
    if (PRI > 0) { if (!grp) __builtin_amdgcn_s_setprio(PRI); } else if (PRI < 0) { if (grp) __builtin_amdgcn_s_setprio(-(PRI)); }
    SB;
#pragma unroll 1
    for (int n = 0; n < 3; ++n) { PIECE(1) }
    { const int n = 3; PIECE(0) }
    if (!NOCHK && __builtin_expect(any != 0, 0)) { CNT(); REDO(hc, cs); }
    ST(12);
    __amdgpu_buffer_rsrc_t orr = __builtin_amdgcn_make_buffer_rsrc(kptr(K, 18), 0, 2048 * 1152 * 4, 0x00020000);
#pragma unroll
    for (int q = 0; q < 4; ++q) ldsw(xa_ + q * 1024, f32x4{acc1[4 * q], acc1[4 * q + 1], acc1[4 * q + 2], acc1[4 * q + 3]});
    bar();
    if (c == 15 && j == 0 && threadIdx.x == 256)
      __hip_atomic_store((GAS u64*)(ws + WS_EPOCH), ((u64)((u32)(ep >> 32) + 1u) << 32) | (u32)((u32)ep + 1u), __ATOMIC_RELAXED, __HIP_MEMORY_SCOPE_AGENT);
#pragma unroll
    for (int q = 0; q < 4; ++q) {
      f32x4 o = ldsr<f32x4>(xb_ + q * 1024);
#pragma unroll
      for (int e = 0; e < 4; ++e) acc1[4 * q + e] = acc0[4 * q + e] + o[e];
    }
    asm volatile("" : "+v"(acc1));
#pragma unroll
    for (int r = 0; r < 16; ++r) __builtin_amdgcn_raw_buffer_store_b32(__float_as_uint(acc1[r]), orr, ov, (8 * (r >> 2) + (r & 3)) * 4608, OAUX);
    ST(13);
    ST(7);
  }
  STAMP_FLUSH(P);
}

struct C2 { f32x2 r, i; };
DEV C2 cm2(const C2& a, const C2& b) {
  C2 o; o.r = __builtin_elementwise_fma(-a.i, b.i, a.r * b.r); o.i = __builtin_elementwise_fma(a.i, b.r, a.r * b.i); return o;
}
DEV C2 cexp2(f32x2 re, f32x2 im) { f32x2 a = cexp(re.x, im.x), b = cexp(re.y, im.y); C2 o; o.r = f32x2{a.x, b.x}; o.i = f32x2{a.y, b.y}; return o; }
DEV u32x2 pk4(const C2& a) { return u32x2{pack2(a.r.x, a.r.y), pack2(a.i.x, a.i.y)}; }
DEV f32x2 up2(u32 v) { return f32x2{__uint_as_float(v << 16), __uint_as_float(v & 0xffff0000u)}; }
DEV f32x4 mf16(u32x4 a, u32x4 b, f32x4 c) { return __builtin_amdgcn_mfma_f32_16x16x32_bf16(__builtin_bit_cast(bf16x8, a), __builtin_bit_cast(bf16x8, b), c, 0, 0, 0); }
DEV u32x4 bandop(int x, int lo, int hi, u32 mul) {
  u32 m = (u32)((((1ull << (hi - lo + 1)) - 1) << 32) >> (32 + hi - x)) & 0xffu;
  u32 sp = m | (m << 15);
  u32x4 o;
#pragma unroll
  for (int i = 0; i < 4; ++i) o[i] = ((sp >> (2 * i)) & 0x10001u) * mul;
  return o;
}
DEV u32x4 rd2(u32 a0, u32 a1) {
  u32x2 x = *(volatile const LAS u32x2*)(size_t)a0, y = *(volatile const LAS u32x2*)(size_t)a1; return u32x4{x.x, x.y, y.x, y.y};
}
constexpr u32 A_RAW = 0, A_UT = 32768, UT_STR = 4640, A_KB = A_UT + 8 * UT_STR, KB_STR = 8704, KP = 544, CPS = 2112;

DEV void a_role(KA K, int d0) {
  const int lane = threadIdx.x & 63, wv = __builtin_amdgcn_readfirstlane(threadIdx.x >> 6);
  const int d = d0 + wv, j = lane & 15, q = lane >> 4;
  STAMP_DECL
  ST0();
  u32 pAr, pAi, pBr, pBi, pCr, pCi, pdt, pds;
  u32 vo = d * 256 + lane * 4, vd = d * 2, uo = (512 * wv + lane) * 1152 + d0 * 2, ul = A_RAW + wv * 8192;
  asm volatile("" : "+v"(vo), "+v"(vd), "+v"(uo));
  asm volatile("" : "+s"(ul));
  KWAIT(K);
  ST0F();
  asm volatile("global_load_dword %0, %8, %10\n\tglobal_load_dword %1, %8, %11\n\tglobal_load_ushort %6, %9, %16\n\tglobal_load_dword %2, %8, %12\n\t"
               "global_load_dword %3, %8, %13\n\tglobal_load_dword %4, %8, %14\n\tglobal_load_dword %5, %8, %15\n\tglobal_load_ushort %7, %9, %17"
               : "=&v"(pAr), "=&v"(pAi), "=&v"(pBr), "=&v"(pBi), "=&v"(pCr), "=&v"(pCi), "=&v"(pdt), "=&v"(pds)
               : "v"(vo), "v"(vd), "s"(kraw(K, 1)), "s"(kraw(K, 2)), "s"(kraw(K, 3)), "s"(kraw(K, 4)), "s"(kraw(K, 5)), "s"(kraw(K, 6)), "s"(kraw(K, 7)), "s"(kraw(K, 8)) : "memory");
  const char* ug = kptr(K, 0);
  char* const outp = kptr(K, 18);
#define UGET(i) glds1(ug + (i) * (64 * 1152), ul + (i) * 1024, uo)
#define U0 4
  if (wv < 4) { _Pragma("unroll") for (int i = 0; i < U0; ++i) UGET(i); }
  u32x4 bnd[6];
  bnd[0] = bandop(j + 16 - 8 * q, 3, 5, 0x3f80); bnd[1] = bandop(j + 16 - 8 * q, 6, 10, 0x3f80); bnd[2] = bandop(j + 16 - 8 * q, 11, 13, 0x3f80);
  bnd[3] = bandop(j + 24 - 8 * q, 14, 18, 0x3f80); bnd[4] = bandop(j + 16 - 8 * q, 5, 11, 0x3f80); bnd[5] = bandop(j + 24 - 8 * q, 12, 20, 0x3f80);
  u32x4 afx = bandop(j + 16 - 8 * q, 3, 4, 1);
  const u32 ut = A_UT + wv * UT_STR, kb = A_KB + wv * KB_STR;
  ldsw(ut + lane * 8, u32x2{0u, 0u});
#pragma unroll
  for (int i = 0; i < 6; ++i) asm volatile("" : "+v"(bnd[i]));
  asm volatile("" : "+v"(afx));
  ST(1);
  if (wv < 4) asm volatile("s_waitcnt vmcnt(%0)" :: "n"(U0) : "memory"); else asm volatile("s_waitcnt vmcnt(0)" ::: "memory");
  asm volatile("" : "+v"(pAr), "+v"(pAi), "+v"(pBr), "+v"(pBi), "+v"(pCr), "+v"(pCi), "+v"(pdt), "+v"(pds) :: "memory");
  ST(2);
  const float dsk = __uint_as_float(pds << 16);
  const float dt = rbf(__builtin_amdgcn_exp2f(__uint_as_float(pdt << 16) * 1.44269504088896341f));
  const f32x2 dt2 = {dt, dt};
  const f32x2 are = dt2 * up2(pAr), aim = dt2 * up2(pAi);
  C2 W; { C2 B = {dt2 * up2(pBr), dt2 * up2(pBi)}, Cc = {up2(pCr), up2(pCi)}; W = cm2(Cc, B); W.i = -W.i; }
  const u32 wrow = kb + lane * 8, rfr = kb + j * KP + q * 16;
  u32x4 Yf[8];
  C2 E8 = cexp2(8.f * are, 8.f * aim);
  {
    C2 Y[16];
    Y[1] = E8;
    ldsw(wrow, u32x2{0x3f803f80u, 0u});
    ldsw(wrow + KP, pk4(Y[1]));
#pragma unroll
    for (int i = 2; i < 16; ++i) { Y[i] = (i & 1) ? cm2(Y[i - 1], Y[1]) : cm2(Y[i / 2], Y[i / 2]); ldsw(wrow + i * KP, pk4(Y[i])); }
#pragma unroll
    for (int s = 0; s < 8; ++s) Yf[s] = ldsr<u32x4>(rfr + 64 * s);
  }
#define U1 6
  if (wv < 4) { _Pragma("unroll") for (int i = U0; i < U1; ++i) UGET(i); }
  E8.i = -E8.i;
  C2 E[8];
  E[1] = cexp2(are, -aim);
  const C2 G = cexp2(128.f * are, -128.f * aim);
  E[2] = cm2(E[1], E[1]); E[3] = cm2(E[2], E[1]); E[4] = cm2(E[2], E[2]); E[5] = cm2(E[4], E[1]); E[6] = cm2(E[4], E[2]); E[7] = cm2(E[4], E[3]);
  f32x4 T0 = {0.f, 0.f, 0.f, 0.f}, T1 = T0, T2 = T0;
  C2 Pc = W;
#pragma unroll
  for (int h = 0; h < 2; ++h) {
    ldsw(wrow + (8 * h) * KP, pk4(Pc));
#pragma unroll
    for (int r = 1; r < 8; ++r) ldsw(wrow + (8 * h + r) * KP, pk4(cm2(Pc, E[r])));
    Pc = cm2(Pc, G);
  }
#pragma unroll
  for (int s = 0; s < 8; ++s) T0 = mf16(ldsr<u32x4>(rfr + 64 * s), Yf[s], T0);
  if (wv < 4) { _Pragma("unroll") for (int i = U1; i < 8; ++i) UGET(i); }
  u32x2 keep[4];
#pragma unroll
  for (int h = 0; h < 2; ++h) {
    keep[2 * h] = pk4(cm2(Pc, E[4])); keep[2 * h + 1] = pk4(cm2(Pc, E8));
    ldsw(wrow + (8 * h) * KP, pk4(Pc)); ldsw(wrow + (8 * h + 1) * KP, pk4(cm2(Pc, E[2])));
    ldsw(wrow + (8 * h + 4) * KP, keep[2 * h]); ldsw(wrow + (8 * h + 5) * KP, pk4(cm2(Pc, E[6])));
    Pc = cm2(Pc, G);
  }
#pragma unroll
  for (int g = 0; g < 4; ++g) {
    ldsw(wrow + (4 * g + 2) * KP, pk4(Pc)); ldsw(wrow + (4 * g + 3) * KP, pk4(cm2(Pc, E[4])));
    Pc = cm2(Pc, G);
  }
#pragma unroll
  for (int s = 0; s < 8; ++s) T1 = mf16(ldsr<u32x4>(rfr + 64 * s), Yf[s], T1);
#pragma unroll
  for (int g = 0; g < 8; ++g) { ldsw(wrow + (4 * (g >> 1) + (g & 1)) * KP, pk4(Pc)); Pc = cm2(Pc, G); }
  ldsw(wrow + 2 * KP, pk4(Pc));
#pragma unroll
  for (int g = 0; g < 4; ++g) ldsw(wrow + (4 * g + 3) * KP, keep[g]);
#pragma unroll
  for (int s = 0; s < 8; ++s) T2 = mf16(ldsr<u32x4>(rfr + 64 * s), Yf[s], T2);
  ST(3);
  const u32 c3 = kb + 3 * CPS;
  ldsw(c3 + lane * 16, u32x4{0u, 0u, 0u, 0u}); ldsw(c3 + 1024 + lane * 16, u32x4{0u, 0u, 0u, 0u});
  {
    const int l0 = 128 * (q >> 1) + 4 * (q & 1) + 8 * j;
    ldsw(c3 + (544 - l0) * 2, u32x2{pack2(T0[3], T0[2]), pack2(T0[1], lane == 0 ? T0[0] + dsk : T0[0])});
    ldsw(c3 + (288 - l0) * 2, u32x2{pack2(T2[3], T1[1]), pack2(T1[0], T1[0])});
    if (lane == 63) ldsw(c3 + 64, u32x2{pack2(0.f, T2[3]), pack2(T2[3], T2[3])});
    const int mu0 = 32 + 8 * q + (j >> 1), od = j & 1;
    u32 p2 = pack2(lane == 0 ? T2[0] : T1[2], T1[3]);
    u32 p3 = pack2(lane == 0 ? T2[2] : T2[0], T2[1]);
    const int ia = lane == 0 ? 647 + 204 - 63 : (od ? 647 + 68 - mu0 : 647 + 204 + 1 - mu0);
    ldsw(c3 + ia * 2, (u16)p2);
    ldsw(c3 + (647 + 136 * od - mu0) * 2, (u16)(p2 >> 16));
    const int i3 = (od ? 967 - 64 : 967 + 84 - 63) - 16 * q - (j >> 1);
    ldsw(c3 + (lane == 0 ? 967 + 84 - 127 : i3) * 2, (u16)p3);
    ldsw(c3 + (i3 - 8) * 2, (u16)(p3 >> 16));
    const u32 k1024 = __builtin_amdgcn_readfirstlane(p2) & 0xffffu;
#pragma unroll
    for (int i = 0; i < 4; ++i) afx[i] = __umul24(afx[i], k1024);
    u32x4 x[2]; u32x2 y[2];
#pragma unroll
    for (int h = 0; h < 2; ++h) { x[h] = ldsr<u32x4>(c3 + h * 1024 + lane * 16); y[h] = ldsr<u32x2>(c3 + h * 1024 + lane * 16 + 16); }
#pragma unroll
    for (int h = 0; h < 2; ++h) {
      u32 a0 = __builtin_amdgcn_alignbit(x[h].y, x[h].x, 16), a1 = __builtin_amdgcn_alignbit(x[h].z, x[h].y, 16), a2 = __builtin_amdgcn_alignbit(x[h].w, x[h].z, 16),
          a3 = __builtin_amdgcn_alignbit(y[h].x, x[h].w, 16), a4 = __builtin_amdgcn_alignbit(y[h].y, y[h].x, 16);
      ldsw(kb + 2 * CPS + h * 1024 + lane * 16, u32x4{a0, a1, a2, a3});
      ldsw(kb + 1 * CPS + h * 1024 + lane * 16, u32x4{x[h].y, x[h].z, x[h].w, y[h].x});
      ldsw(kb + h * 1024 + lane * 16, u32x4{a1, a2, a3, a4});
    }
  }
  u32x4 Af[17], t2[3][2], t3[4];
  {
    const u32 fb = kb + (j & 3) * CPS - 8 * (j >> 2);
    const u32 fa = fb + 16 * q + (544 - 16 - 512) * 2;
#pragma unroll
    for (int n = 0; n < 17; ++n) Af[n] = rd2(fa + (16 - n) * 64, fa + (16 - n) * 64 + 8);
    const u32 ft = fb + 8 * q;
#pragma unroll
    for (int D = 2; D < 5; ++D) { u32 a = ft + (644 - 16 * D) * 2; t2[D - 2][0] = rd2(a, a + 136); t2[D - 2][1] = rd2(a + 272, a + 408); }
#pragma unroll
    for (int D = 4; D < 8; ++D) { u32 a = ft + (964 - 16 * D) * 2; t3[D - 4] = rd2(a, a + 168); }
  }
  ST(4);
  if (wv < 4) {
    asm volatile("s_waitcnt vmcnt(0)" ::: "memory");
    ST(10);
    const u32 ra = ul + (8 * q + 4 * ((j & 3) >> 1) + (j >> 2)) * 16 + (j & 1) * 8;
    const u32 wa = A_UT + (j & 7) * UT_STR + (256 + 512 * wv + 8 * q + 4 * (j >> 3)) * 2;
    s16x4 v[16];
#pragma unroll
    for (int i = 0; i < 16; ++i) v[i] = __builtin_amdgcn_ds_read_tr16_b64_v4i16((LAS s16x4*)(size_t)(ra + i * 512));
    SB;
#pragma unroll
    for (int i = 0; i < 16; ++i) ldsw(wa + i * 64, v[i]);
  }
  ST(5);
  bar();
  ST(6);
  f32x4 acc[8];
#pragma unroll
  for (int i = 0; i < 8; ++i) acc[i] = f32x4{0.f, 0.f, 0.f, 0.f};
  const f32x4 zz = {0.f, 0.f, 0.f, 0.f};
  const u32 ub = ut + (16 * j + 8 * q) * 2;
  const u32 yb = ut + (16 * j + 4 * q) * 2;
#define BADDR(k) (ub + (256 + 256 * ((k) >> 3) - 16 - 32 * ((k) & 7)) * 2)
  constexpr int NB = 4;
  u32x4 Bf[NB], W1;
#pragma unroll
  for (int k = 0; k < NB - 1; ++k) Bf[k] = ldsr<u32x4>(BADDR(k));
  W1 = ldsr<u32x4>(ub + (256 - 24) * 2);
  f32x4 V[6]; u32x4 vp[3];
  __amdgpu_buffer_rsrc_t orr = __builtin_amdgcn_make_buffer_rsrc(outp, 0, 2048 * 1152 * 4, 0x00020000);
  const u32 ora = A_UT + (4 * (q & 1) + (j >> 2)) * UT_STR + (32 * wv + 16 * (q >> 1) + 4 * (j & 3)) * 2;
  const u32 ov = ((32 * wv + 16 * (q >> 1) + j) * 1152 + d0 + 4 * (q & 1)) * 4;
  u32x2 ovv;
#define YSTAGE(t) ldsw(yb + (256 * (t)) * 2, u32x2{pack2(acc[t][0], acc[t][1]), pack2(acc[t][2], acc[t][3])})
#define YREAD(t) ovv = __builtin_bit_cast(u32x2, __builtin_amdgcn_ds_read_tr16_b64_v4i16((LAS s16x4*)(size_t)(ora + (t) * 512)))
#define YSTORE(t) __builtin_amdgcn_raw_buffer_store_b128(u32x4{ovv.x << 16, ovv.x & 0xffff0000u, ovv.y << 16, ovv.y & 0xffff0000u}, orr, ov, (t) * 256 * 4608, OAUXA)
#pragma unroll
  for (int e = 0; e < 8; ++e) {
#pragma unroll
    for (int b = 0; b < 8; ++b) {
      const int k = 8 * e + b;
      if (b == 3 && e > 0) { bar(); YREAD(e - 1); }
      if (k + NB - 1 < 64) Bf[(k + NB - 1) % NB] = ldsr<u32x4>(BADDR(k + NB - 1));
      if (b == 6 && e < 5) W1 = ldsr<u32x4>(ub + (256 + 256 * (e + 1) - 24) * 2);
      const u32x4 B = Bf[k % NB];
      acc[e] = mf16(Af[b], B, acc[e]);
      if (e < 7) acc[e + 1] = mf16(Af[8 + b], B, acc[e + 1]);
      if (b == 0) {
        if (e < 6) { V[0] = mf16(B, bnd[0], zz); V[1] = mf16(B, bnd[1], zz); V[2] = mf16(B, bnd[2], zz); V[3] = mf16(W1, bnd[3], zz); }
        if (e < 4) { V[4] = mf16(B, bnd[4], zz); V[5] = mf16(W1, bnd[5], zz); }
        if (e < 6) acc[e + 2] = mf16(Af[16], B, acc[e + 2]);
        if (e < 4) acc[e + 4] = mf16(afx, B, acc[e + 4]);
        if (e > 0) YSTAGE(e - 1);
      }
      if (b == 1 && e < 6) {
#pragma unroll
        for (int p = 0; p < 3; ++p) if (p < 2 || e < 4)
          vp[p] = u32x4{pack2(V[2 * p][0], V[2 * p][1]), pack2(V[2 * p][2], V[2 * p][3]), pack2(V[2 * p + 1][0], V[2 * p + 1][1]), pack2(V[2 * p + 1][2], V[2 * p + 1][3])};
      }
      if (b >= 2 && b <= 4 && e + b < 8) { acc[e + b] = mf16(vp[0], t2[b - 2][0], acc[e + b]); acc[e + b] = mf16(vp[1], t2[b - 2][1], acc[e + b]); }
      if (b >= 4 && e + b < 8) acc[e + b] = mf16(vp[2], t3[b - 4], acc[e + b]);
      if (b == 4 && e > 0) YSTORE(e - 1);
      __builtin_amdgcn_sched_barrier(0);
    }
  }
  YSTAGE(7);
  ST(7);
  bar();
  ST(8);
  YREAD(7); YSTORE(7);
  ST(9);

  STAMP_FLUSH(P);
}

DEV void pf_role(KA K, int k) {
  const u64 tb = __builtin_amdgcn_s_memrealtime();
  const int lane = threadIdx.x & 63, wv = __builtin_amdgcn_readfirstlane(threadIdx.x >> 6);
  const int g = blockIdx.x & 7, id = k * 8 + wv;
  STAMP_DECL
  KWAIT(K);
  char* const ws = kptr(K, 19);
  const char *c0 = kptr(K, 14), *c1 = kptr(K, 15);
  const u64 ep = load_epoch(ws);
#define CNS 9
#define CPULL() _Pragma("unroll 1") for (int i = id; i < 18 * CNS; i += 40) { \
    int pc = i + g * (2 * CNS + 2); pc = pc >= 18 * CNS ? pc - 18 * CNS : pc; \
    int m = pc >= 9 * CNS, q = pc - 9 * CNS * m, jj = q / CNS, e = (q - jj * CNS) * 64 + lane, row = e / CNS, sl = e - row * CNS + jj + 9 - CNS; \
    sl = sl >= 9 ? sl - 9 : sl; \
    TOUCH((m ? c1 : c0) + jj * (64 * 1152), wv * 256, row * 1152 + sl * 128); \
    asm volatile("s_waitcnt vmcnt(%0)" :: "n"(CPF - 1) : "memory"); }
  for (int it = 0; it < 4000 && __builtin_amdgcn_s_memrealtime() - tb < WDELAY; ++it) __builtin_amdgcn_s_sleep(2);
  ST(11);
  __amdgpu_buffer_rsrc_t hr = __builtin_amdgcn_make_buffer_rsrc(ws + WS_H, 0, H_BYTES, 0x00020000);
#pragma unroll 1
  for (int i = id; i < 576; i += 40) __builtin_amdgcn_raw_buffer_store_b128(u32x4{MARK, 0u, 0u, 0u}, hr, lane * 16, (g * 576 + i) * 1024, 16);
  ST(12);
  asm volatile("s_waitcnt vmcnt(0)" ::: "memory");
  ST(13);
  bar();
  if (threadIdx.x == 0) {
    __amdgpu_buffer_rsrc_t fr = __builtin_amdgcn_make_buffer_rsrc(ws + WS_FLAG, 0, 64 * 128, 0x00020000);
    __builtin_amdgcn_raw_buffer_store_b128(u32x4{0u, mix32((u32)ep), 0u, mix32((u32)(ep >> 32) ^ 0x5bd1e995u)}, fr, 0, (g * 8 + k) * 128, 16);
  }
  CPULL()
  asm volatile("s_waitcnt vmcnt(0)" ::: "memory");
  ST(10);
  STAMP_FLUSH(P);
}

__global__ __launch_bounds__(512, 2) void fused(Params P) {
  const int b = blockIdx.x, xcd = b & 7, slot = b >> 3;
  KA K;
  asm volatile("s_load_dwordx16 %1, %3, 0x40\n\ts_load_dwordx8 %2, %3, 0x80\n\ts_load_dwordx16 %0, %3, 0x0"
               : "=&s"(K.a), "=&s"(K.b), "=&s"(K.c) : "s"(__builtin_amdgcn_kernarg_segment_ptr()) : "memory");
  u64 pc = 0;
  if (__builtin_expect(slot < 18, 1)) {
    g_role(K, 2 * xcd + (slot >= 9), slot >= 9 ? slot - 9 : slot, pc);
  } else if (slot < 27) {
    a_role(K, 72 * xcd + 8 * (slot - 18));
  } else pf_role(K, slot - 27);
}

extern "C" void kernel_launch(void* const* d_in, const int* in_sizes, int n_in, void* d_out, int out_size, void* d_ws, size_t ws_size, hipStream_t stream) {
  Params P;
  for (int i = 0; i < 18; ++i) P.in[i] = (const u16*)d_in[i];
  P.out = (float*)d_out; P.ws = (char*)d_ws;
  (void)hipFuncSetAttribute((const void*)fused, hipFuncAttributeMaxDynamicSharedMemorySize, LDS_BYTES);
#define GRID 256
  fused<<<GRID, 512, LDS_BYTES, stream>>>(P);
}
```

```cpp
#include <hip/hip_runtime.h>
#include <stdint.h>

typedef unsigned short u16; typedef unsigned u32; typedef unsigned long long u64;
typedef __bf16 bf16x8 __attribute__((ext_vector_type(8)));
typedef __bf16 bf16x2 __attribute__((ext_vector_type(2)));
typedef short s16x4 __attribute__((ext_vector_type(4)));
typedef float f32x16 __attribute__((ext_vector_type(16)));
typedef float f32x4 __attribute__((ext_vector_type(4)));
typedef float f32x2 __attribute__((ext_vector_type(2)));
typedef u32 u32x4 __attribute__((ext_vector_type(4)));
typedef u32 u32x2 __attribute__((ext_vector_type(2)));
#define LAS __attribute__((address_space(3)))
#define GAS __attribute__((address_space(1)))
#define DEV __device__ __forceinline__

struct Params { const u16* in[18]; float* out; char* ws; };
typedef u64 u64x8 __attribute__((ext_vector_type(8)));
typedef u64 u64x4 __attribute__((ext_vector_type(4)));
struct KA { u64x8 a, b; u64x4 c; };
#define KWAIT(k) asm volatile("s_waitcnt lgkmcnt(0)" :: "s"(k.a), "s"(k.b), "s"(k.c) : "memory")
__device__ __forceinline__ u64 kraw(const KA& k, int i) { return i < 8 ? k.a[i & 7] : i < 16 ? k.b[i & 7] : k.c[i & 3]; }
__device__ __forceinline__ char* kptr(const KA& k, int i) { char* p; asm volatile("s_mov_b64 %0, %1" : "=s"(p) : "s"(kraw(k, i))); return p; }

constexpr u32 WS_EPOCH = 0, WS_FLAG = 4096, WS_GRAN = 65536, WS_H = 262144;
constexpr u32 H_BYTES = 16u * 4u * 72u * 1024u;
constexpr u32 LDS_BYTES = 163840, SCR = 147456;
constexpr int SPIN_MAX = 1 << 16;
#define NWG 5
#define LAUX 16

#define XROT 2
#define XPERM 0x64753210
#define OAUX 16
#define OAUXA 2
#define DEEP 8
#define P1OV 2
#define CODEPF 3
#define CNS 9
#define CPF 4
#define WDELAY 150
#define NH 2
#define H0 0
#define HS0 4
#define HS 3
#define SLP 1
#define ASLP 15
#define BSLP 4

DEV float bf2f(u16 v) { return __uint_as_float(((u32)v) << 16); }
DEV u32 pack2(float a, float b) { bf16x2 r; r[0] = (__bf16)a; r[1] = (__bf16)b; return __builtin_bit_cast(u32, r); }
DEV float rbf(float f) { return (float)(__bf16)f; }
DEV void bar() { asm volatile("s_waitcnt lgkmcnt(0)\n\ts_barrier" ::: "memory"); }
DEV u32 mix32(u32 x) { x *= 0x9E3779B1u; x ^= x >> 15; x *= 0x85EBCA77u; x ^= x >> 13; return x + 0x632BE5ABu; }
DEV f32x2 cmul(f32x2 a, f32x2 b) { return f32x2{a.x * b.x - a.y * b.y, a.x * b.y + a.y * b.x}; }
DEV f32x2 cfma(f32x2 a, f32x2 b, f32x2 c) { return f32x2{fmaf(a.x, b.x, fmaf(-a.y, b.y, c.x)), fmaf(a.x, b.y, fmaf(a.y, b.x, c.y))}; }
DEV f32x2 cexp(float re, float im) {
  float e = __builtin_amdgcn_exp2f(re * 1.44269504088896341f);
  float r = __builtin_amdgcn_fractf(im * 0.15915494309189535f);
  return f32x2{e * __builtin_amdgcn_cosf(r), e * __builtin_amdgcn_sinf(r)};
}
DEV void glds4(const char* gb, u32 ldsb, u32 v0, u32 v1, u32 v2, u32 v3) {
  u32 keep;
  asm volatile("s_mov_b32 %0, m0\n\ts_mov_b32 m0, %2\n\ts_nop 0\n\t"
               "global_load_lds_dwordx4 %3, %1\n\tglobal_load_lds_dwordx4 %4, %1 offset:1024\n\t"
               "global_load_lds_dwordx4 %5, %1 offset:2048\n\tglobal_load_lds_dwordx4 %6, %1 offset:3072\n\ts_mov_b32 m0, %0"
               : "=&s"(keep) : "s"(gb), "s"(ldsb), "v"(v0), "v"(v1), "v"(v2), "v"(v3) : "memory");
}
DEV void glds1(const char* gb, u32 ldsb, u32 v0) {
  u32 keep;
  asm volatile("s_mov_b32 %0, m0\n\ts_mov_b32 m0, %2\n\ts_nop 0\n\tglobal_load_lds_dwordx4 %3, %1\n\ts_mov_b32 m0, %0"
               : "=&s"(keep) : "s"(gb), "s"(ldsb), "v"(v0) : "memory");
}
template <class T> DEV T ldsr(u32 a) { return *(const LAS T*)(size_t)a; }
template <class T> DEV void ldsw(u32 a, T v) { *(LAS T*)(size_t)a = v; }
DEV f32x2 pr(const f32x16& a, int m) { return f32x2{a[2 * m], a[2 * m + 1]}; }

DEV void swap16(float x, float& lo, float& hi) { auto r = __builtin_amdgcn_permlane16_swap(__float_as_uint(x), __float_as_uint(x), false, false); lo = __uint_as_float(r[0]); hi = __uint_as_float(r[1]); }
DEV void swap32(float x, float& lo, float& hi) { auto r = __builtin_amdgcn_permlane32_swap(__float_as_uint(x), __float_as_uint(x), false, false); lo = __uint_as_float(r[0]); hi = __uint_as_float(r[1]); }
DEV int wrap9(int v) { return v >= 9 ? v - 9 : v; }
DEV u64 load_epoch(const char* ws) { u64 ep; asm volatile("s_load_dwordx2 %0, %1, 0x0\n\ts_waitcnt lgkmcnt(0)" : "=s"(ep) : "s"(ws) : "memory"); return ep; }
DEV void touch64(const char* gb, u32 ldsb, u32 v0) {
  u32 keep;
  asm volatile("s_mov_b32 %0, m0\n\ts_mov_b32 m0, %2\n\ts_nop 0\n\tglobal_load_lds_dword %3, %1\n\ts_mov_b32 m0, %0"
               : "=&s"(keep) : "s"(gb), "s"(ldsb), "v"(v0) : "memory");
}

#define MM(h, cc, acc) __builtin_amdgcn_mfma_f32_32x32x16_bf16(h, cc, acc, 0, 0, 0)
#define SB __builtin_amdgcn_sched_barrier(0)
#define WL(n) __builtin_amdgcn_s_waitcnt(0xC07F | ((n) << 8))
constexpr u32 HREG = 131072, MARK = 0x80000000u;
DEV void g_role(KA K, int c, int j, u64 pc) {
  u32 pl = 0, pr_ = 0, pi_ = 0;
  const int lane = threadIdx.x & 63, wv = __builtin_amdgcn_readfirstlane(threadIdx.x >> 6);
  const int rot = (j + 4 * (c & 1) + XROT * (c >> 1)) % 9;
  const int l15 = lane & 15, q4 = lane >> 4;
  const int w = wv & 3, l31 = lane & 31, hh = lane >> 5;
  f32x16 acc0, acc1;
  u32 vb[4], aoff[4], ca[4];
  const char* c8;
  u64 ep;
  constexpr int PF = 8, CD = 3;
  u32x4 hd[32];
  u32 ht[4], csn[4];
  u32x4 hf[PF];
  bf16x8 cf[CD + 1][2];
#define CADDR(k, h) (ca[(k) & 3] + (((k) >> 2) & 1) * 8192 + (h) * 4096)
#define PIN(x) asm volatile("" : "+s"(x))
  const int grp = wv >> 2;
  u32 he, hc, hn, cs, cl, l8, ho, ho4;
#define PINS() do { he = (c * 4 + w) * 72 * 1024; hc = he + wrap9(j + 1 + grp) * 8192; hn = he + wrap9(j + 3 + grp) * 8192; he += 9 * 8192; \
    cs = (3 + grp) * 16384; cl = grp ? HREG : 16384; l8 = HREG + w * 4096; ho = lane * 16; ho4 = ho + 4096; \
    asm volatile("" : "+s"(he), "+s"(hc), "+s"(hn), "+s"(cs), "+s"(cl), "+s"(l8)); asm volatile("" : "+v"(ho), "+v"(ho4)); } while (0)
  u32 vx[4], ko0 = rot * 128, ko1 = wrap9(rot + 1) * 128, sl0 = w * 4096;
  if (wv >= 4) {
#pragma unroll
    for (int q = 0; q < 4; ++q) {
      int i = 2 * w + (q >> 1), qq = 2 * (q & 1) + (lane >> 5), r = (lane >> 3) & 3;
      int time = 32 * qq + 16 * (r & 1) + 2 * i + (r >> 1);
      u32 ch = (u32)(((lane & 7) ^ ((4 * (q & 1) + (lane >> 4)) & 7)) << 4) + 3072 - 1024 * q;
      vx[q] = (128 * c + time) * 1152 + ch;
      vb[q] = (64 * j + 32 * (w & 1) + 8 * q + (lane >> 3)) * 1152 + ch;
    }
  } else {
#pragma unroll
    for (int q = 0; q < 4; ++q) { vx[q] = (64 * j + 16 * wv + l15) * 2; vb[q] = 0; }
  }
  asm volatile("" : "+v"(vx[0]), "+v"(vx[1]), "+v"(vx[2]), "+v"(vx[3]), "+v"(vb[0]), "+v"(vb[1]), "+v"(vb[2]), "+v"(vb[3]));
  asm volatile("" : "+s"(ko0), "+s"(ko1), "+s"(sl0));
  KWAIT(K);
  char* ws;
  __amdgpu_buffer_rsrc_t hr;
  if (wv < 4)
    asm volatile("global_load_ushort %0, %3, %4\n\tglobal_load_ushort %1, %3, %5\n\tglobal_load_ushort %2, %3, %6"
                 : "=&v"(pl), "=&v"(pr_), "=&v"(pi_) : "v"(vx[0]), "s"(kraw(K, 16)), "s"(kraw(K, 10)), "s"(kraw(K, 11)) : "memory");
  if (__builtin_expect(wv >= 4, 1)) {
    const char* xb = kptr(K, 9) - 3072;
    glds4(xb + ko0, sl0, vx[0], vx[1], vx[2], vx[3]);
    const char *b0 = kptr(K, 12), *b1 = kptr(K, 13), *bb = (w < 2 ? b0 : b1) - 3072;
    glds4(bb + ko0, sl0 + 16384, vb[0], vb[1], vb[2], vb[3]);
    glds4(xb + ko1, sl0 + 32768, vx[0], vx[1], vx[2], vx[3]);
    glds4(bb + ko1, sl0 + 49152, vb[0], vb[1], vb[2], vb[3]);
    const char *c0 = kptr(K, 14), *c1 = kptr(K, 15), *cb = (w < 2 ? c0 : c1) - 3072;
    ws = kptr(K, 19);
    hr = __builtin_amdgcn_make_buffer_rsrc(ws + WS_H, 0, H_BYTES, 0x00020000);
    u32 dk0, dk1;
    asm volatile("global_load_ushort %0, %2, %3\n\tglobal_load_ushort %1, %2, %3 offset:64" : "=&v"(dk0), "=&v"(dk1) : "v"((64 * j + l31) * 2), "s"(kraw(K, 17)) : "memory");
    s16x4 xs[8];
    asm volatile("s_waitcnt vmcnt(10)" ::: "memory");
    const char* ce = cb + ((w & 1) ? -32 * 1152 : 32 * 1152);
#define CPIECE(i) glds4((((i) & 1) ? cb : ce) + wrap9(j + (i)) * 128, (((i) + 2) & 7) * 16384 + w * 4096, vb[0], vb[1], vb[2], vb[3])
#define RAD 2
    int kn = wrap9(rot + RAD);
#pragma unroll 1
    for (int s = 0; s < 9; ++s) {
      bar();
      if (s <= 8 - RAD) {
        u32 sl = ((s + RAD) & 3) * 32768 + w * 4096;
        glds4(xb + kn * 128, sl, vx[0], vx[1], vx[2], vx[3]);
        glds4(bb + kn * 128, sl + 16384, vb[0], vb[1], vb[2], vb[3]);
      } else { CPIECE(2 * s - 18 + 2 * RAD); CPIECE(2 * s - 17 + 2 * RAD); }
      if (wrap9(rot + s) == j) {
        const int e = (lane >> 2) & 3, pp = lane & 3;
        u32 sb = (s & 3) * 32768 + (32 * hh + 4 * w + 16 * (e >> 1) + 2 * (e & 1)) * 128 + (((2 * ((lane >> 4) & 1) + (pp >> 1)) ^ ((w << 1) | (e & 1))) << 4) + (pp & 1) * 8;
#pragma unroll
        for (int g = 0; g < 4; ++g) {
          xs[g] = __builtin_amdgcn_ds_read_tr16_b64_v4i16((LAS s16x4*)(size_t)(sb + (64 * (g & 1) + (g >> 1)) * 128));
          xs[4 + g] = __builtin_amdgcn_ds_read_tr16_b64_v4i16((LAS s16x4*)(size_t)((sb ^ 64) + (64 * (g & 1) + (g >> 1)) * 128));
        }
      }
      kn = wrap9(kn + 1);
      if (s < 8) asm volatile("s_waitcnt vmcnt(8)" ::: "memory");
    }
    bar();
    asm volatile("" : "+v"(dk0), "+v"(dk1));
    const float dsk0 = __uint_as_float(dk0 << 16), dsk1 = __uint_as_float(dk1 << 16);
#pragma unroll
    for (int i = 2 * RAD; i < 8; ++i) CPIECE(i);
#pragma unroll
    for (int r = 0; r < 16; ++r) {
      acc1[r] = rbf(bf2f((u16)xs[r >> 2][r & 3]) * dsk0);
      acc0[r] = rbf(bf2f((u16)xs[4 + (r >> 2)][r & 3]) * dsk1);
    }
#pragma unroll
    for (int s = 0; s < 4; ++s) aoff[s] = l31 * 128 + (((2 * s + hh) ^ ((l31 >> 1) & 7)) << 4);
    u32 ra[2];
#pragma unroll
    for (int u = 0; u < 2; ++u) {
      int row = 8 * (q4 >> 1) + 4 * u + (l15 >> 2);
      ra[u] = HREG + w * 4096 + row * 64 + (((2 * (q4 & 1) + ((l15 & 3) >> 1)) ^ ((row >> 1) & 3)) << 4) + (l15 & 1) * 8;
    }
    asm volatile("" : "+v"(acc0), "+v"(acc1), "+v"(ra[0]), "+v"(ra[1]), "+v"(aoff[0]), "+v"(aoff[1]), "+v"(aoff[2]), "+v"(aoff[3]));
#pragma unroll
    for (int s = 0; s < 4; ++s) ca[s] = aoff[s] + 2 * 16384;
    c8 = ce + wrap9(j + 8) * 128;
    asm volatile("" : "+s"(c8));
    PINS();
    ep = 0;
    if (c == 15 && j == 0 && wv == 4) {
      ep = load_epoch(ws);
      GAS u64* fp = (GAS u64*)(ws + WS_FLAG + ((lane >> 3) * 8 + ((lane & 7) < NWG - 1 ? (lane & 7) : NWG - 1)) * 128);
      const u64 ta = (u64)mix32((u32)ep) << 32, tb = (u64)mix32((u32)(ep >> 32) ^ 0x5bd1e995u) << 32;
#pragma unroll 1
      for (int it = 0; it < SPIN_MAX; ++it) {
        if (__all(__hip_atomic_load(fp, __ATOMIC_RELAXED, __HIP_MEMORY_SCOPE_AGENT) == ta && __hip_atomic_load(fp + 1, __ATOMIC_RELAXED, __HIP_MEMORY_SCOPE_AGENT) == tb)) break;
        __builtin_amdgcn_s_sleep(4);
      }
    }
    asm volatile("s_waitcnt vmcnt(0)" ::: "memory");
    bar();
#pragma unroll
    for (int k = 0; k < 8; ++k) {
      u32x2 l2 = __builtin_bit_cast(u32x2, __builtin_amdgcn_ds_read_tr16_b64_v4i16((LAS s16x4*)(size_t)(ra[0] + (k & 3) * 1024 + (k >> 2) * 16384)));
      u32x2 h2 = __builtin_bit_cast(u32x2, __builtin_amdgcn_ds_read_tr16_b64_v4i16((LAS s16x4*)(size_t)(ra[1] + (k & 3) * 1024 + (k >> 2) * 16384)));
      hf[k] = u32x4{l2.x, l2.y, h2.x, h2.y};
      if (k < CD) { cf[k][0] = ldsr<bf16x8>(ca[k]); cf[k][1] = ldsr<bf16x8>(ca[k] + 4096); }
    }
    SB;
#pragma unroll
    for (int k = 0; k < 8; ++k) {
      bf16x8 a = __builtin_bit_cast(bf16x8, hf[k]);
      acc0 = MM(a, cf[k & 3][0], acc0);
      asm volatile("" : "+v"(acc0)); SB;
      if (k == 4) glds4(c8, l8, vb[0], vb[1], vb[2], vb[3]);
      if (k + CD < 8) cf[(k + CD) & 3][0] = ldsr<bf16x8>(CADDR(k + CD, 0));
      SB;
      acc1 = MM(a, cf[k & 3][1], acc1);
      asm volatile("" : "+v"(acc1)); SB;
      if (k + CD < 8) cf[(k + CD) & 3][1] = ldsr<bf16x8>(CADDR(k + CD, 1));
      SB;
    }
    __builtin_amdgcn_s_sleep(BSLP);
  }
  int wq = wv;
  asm volatile("" : "+s"(wq));
  if (__builtin_expect(wq < 4, 1)) {
    asm volatile("s_load_dwordx2 %0, %1, 0x0" : "=&s"(ep) : "s"(kraw(K, 19)) : "memory");
    ws = kptr(K, 19);
    hr = __builtin_amdgcn_make_buffer_rsrc(ws + WS_H, 0, H_BYTES, 0x00020000);
    if (wv == 0 && (blockIdx.x >> 3) < CODEPF) {
      touch64((const char*)(pc & ~(u64)127), SCR, (blockIdx.x >> 3) * 8192 + lane * 128);
      asm volatile("s_waitcnt vmcnt(1)" ::: "memory");
    } else asm volatile("s_waitcnt vmcnt(0)" ::: "memory");
    asm volatile("" : "+v"(pl), "+v"(pr_), "+v"(pi_) :: "memory");
    u32 hbase = (c * 4 * 72 + j * 8 + wv) * 1024;
    float dt = rbf(__builtin_amdgcn_exp2f(__uint_as_float(pl << 16) * 1.44269504088896341f));
    float Lr = __uint_as_float(pr_ << 16), Li = __uint_as_float(pi_ << 16);
    f32x2 lam = cexp(dt * Lr, dt * Li);
    float inv = 2.f / (Lr * Lr + Li * Li);
    f32x2 coef = cmul(f32x2{lam.x - 1.f, lam.y}, f32x2{Lr * inv, -Li * inv});
    f32x2 mu = cmul(lam, lam); mu = cmul(mu, mu); mu = cmul(mu, mu); mu = cmul(mu, mu);
    f32x2 nu = cmul(mu, mu), nu2 = cmul(nu, nu), La = cmul(nu2, nu2);
    f32x2 La2 = cmul(La, La), La4 = cmul(La2, La2);
    f32x2 one = {1.f, 0.f};
    f32x2 Lg = cmul((q4 & 2) ? La2 : one, (q4 & 1) ? La : one);
    f32x2 nuq = (q4 & 1) ? nu : one;
    f32x2 cm0 = cmul(coef, cmul((q4 & 2) ? nu2 : one, nuq)), cm1 = cmul(cm0, mu);
    asm volatile("s_waitcnt lgkmcnt(0)" : "+s"(ep) :: "memory");
    u32 tlo = mix32((u32)ep), thi = mix32((u32)(ep >> 32) ^ 0x5bd1e995u);
    asm volatile("" :: "s"(tlo), "s"(thi));
    asm volatile("" : "+v"(lam), "+v"(coef), "+v"(mu), "+v"(La4), "+v"(Lg), "+v"(cm0), "+v"(cm1), "+v"(nu), "+v"(nu2), "+v"(nuq));
    const char* gb = ws;
    u32 go[4];
    const u32 fo = WS_FLAG + ((c >> 1) * 8 + ((lane & 7) < NWG - 1 ? (lane & 7) : NWG - 1)) * 128;
#pragma unroll
    for (int n = 0; n < 4; ++n) { int cs = c - 1 - q4 - 4 * n; go[n] = cs >= 0 ? WS_GRAN + j * 16384 + wv * 256 + cs * 1024 + l15 * 16 : fo; }
    u32 gst = (q4 == 0 && c < 15) ? (j * 16 + c) * 1024 + wv * 256 + l15 * 16 : 0x7fffffffu;
    __amdgpu_buffer_rsrc_t gr = __builtin_amdgcn_make_buffer_rsrc(ws + WS_GRAN, 0, 9 * 16 * 1024, 0x00020000);
    asm volatile("" : "+v"(go[0]), "+v"(go[1]), "+v"(go[2]), "+v"(go[3]), "+v"(gst));
    u32x4 g0, g1, g2, g3;
#define GPOLL() asm volatile("global_load_dwordx4 %0, %4, %8 sc1\n\tglobal_load_dwordx4 %1, %5, %8 sc1\n\tglobal_load_dwordx4 %2, %6, %8 sc1\n\tglobal_load_dwordx4 %3, %7, %8 sc1" \
                     : "=&v"(g0), "=&v"(g1), "=&v"(g2), "=&v"(g3) : "v"(go[0]), "v"(go[1]), "v"(go[2]), "v"(go[3]), "s"(gb) : "memory")
#define GWAIT() asm volatile("s_waitcnt vmcnt(0)" : "+v"(g0), "+v"(g1), "+v"(g2), "+v"(g3) :: "memory")
#define GBAD() __any(((((g0.y ^ tlo) | (g1.y ^ tlo)) | ((g2.y ^ tlo) | (g3.y ^ tlo))) | (((g0.w ^ thi) | (g1.w ^ thi)) | ((g2.w ^ thi) | (g3.w ^ thi)))) != 0)
    f32x4 ar[8], ai[8];
#pragma unroll
    for (int i = 0; i < 8; ++i) { ar[i] = f32x4{0.f, 0.f, 0.f, 0.f}; ai[i] = ar[i]; asm volatile("" : "+v"(ar[i]), "+v"(ai[i])); }
    u32 xa0 = l15 * 128 + ((q4 ^ (l15 >> 1)) << 4), xa1 = xa0 ^ 64, ba0 = xa0 + 16384 + wv * 2048, ba1 = ba0 ^ 64;
    bf16x8 A[2][8], Bq[2][2];
#define STEP(cs, RD, xa, ba, G10, G11, G12, G13) do { WL(4); SB; \
      _Pragma("unroll") for (int i = 0; i < 8; ++i) { if (i == 4) { if (RD) WL(8); else WL(0); SB; } \
        ar[i] = __builtin_amdgcn_mfma_f32_16x16x32_bf16(A[cs][i], Bq[cs][0], ar[i], 0, 0, 0); asm volatile("" : "+v"(ar[i])); SB; \
        if (RD) { if (i == 0) Bq[cs ^ 1][0] = ldsr<bf16x8>(ba); else if (i < 5) A[cs ^ 1][2 * i - 2] = ldsr<bf16x8>(xa + (2 * i - 2) * 2048); } \
        if (i == 5) { G10; } if (i == 6) { G12; } SB; \
        ai[i] = __builtin_amdgcn_mfma_f32_16x16x32_bf16(A[cs][i], Bq[cs][1], ai[i], 0, 0, 0); asm volatile("" : "+v"(ai[i])); SB; \
        if (RD) { if (i == 0) Bq[cs ^ 1][1] = ldsr<bf16x8>(ba + 8192); else if (i < 5) A[cs ^ 1][2 * i - 1] = ldsr<bf16x8>(xa + (2 * i - 1) * 2048); } \
        if (i == 5) { G11; } if (i == 6) { G13; } SB; } } while (0)
#define ADV(v) do { v = (v + 32768) & 131071; asm volatile("" : "+v"(v)); } while (0)
    bar();
    Bq[0][0] = ldsr<bf16x8>(ba0); Bq[0][1] = ldsr<bf16x8>(ba0 + 8192);
#pragma unroll
    for (int i = 0; i < 8; ++i) A[0][i] = ldsr<bf16x8>(xa0 + i * 2048);
    SB;
#pragma unroll 1
    for (int s = 0; s < 8; ++s) {
      STEP(0, 1, xa1, ba1, ADV(xa0), ADV(ba0), asm volatile("s_barrier" ::: "memory"), );
      STEP(1, 1, xa0, ba0, ADV(xa1), ADV(ba1), , );
    }
    STEP(0, 1, xa1, ba1, , , , bar());
    const f32x2 lr2 = {lam.x, lam.x}, li2 = {lam.y, lam.y};
    f32x2 Br[16], Bi[16];
    f32x2 sr, si;
#define P1(k) do { f32x2 t1 = __builtin_elementwise_fma(lr2, sr, Br[k]), t2 = __builtin_elementwise_fma(lr2, si, Bi[k]); \
      f32x2 n1 = __builtin_elementwise_fma(-li2, si, t1), n2 = __builtin_elementwise_fma(li2, sr, t2); sr = n1; si = n2; } while (0)
#define P1T(i) do { Br[2 * (i)] = f32x2{ar[i][0], ar[i][1]}; Br[2 * (i) + 1] = f32x2{ar[i][2], ar[i][3]}; Bi[2 * (i)] = f32x2{ai[i][0], ai[i][1]}; Bi[2 * (i) + 1] = f32x2{ai[i][2], ai[i][3]}; \
      if ((i) == 0) { sr = Br[0]; si = Bi[0]; } else P1(2 * (i)); P1(2 * (i) + 1); asm volatile("" : "+v"(sr), "+v"(si)); } while (0)
    WL(4); SB;
#pragma unroll
    for (int i = 0; i < 8; ++i) {
      if (i == 4) { WL(0); SB; }
      ar[i] = __builtin_amdgcn_mfma_f32_16x16x32_bf16(A[1][i], Bq[1][0], ar[i], 0, 0, 0); asm volatile("" : "+v"(ar[i])); SB;
      ai[i] = __builtin_amdgcn_mfma_f32_16x16x32_bf16(A[1][i], Bq[1][1], ai[i], 0, 0, 0); asm volatile("" : "+v"(ai[i])); SB;
      if (i >= P1OV) { P1T(i - P1OV); SB; }
    }
#pragma unroll
    for (int i = 8 - P1OV; i < 8; ++i) P1T(i);
    const f32x2 S0 = {sr.x, si.x};
    f32x2 T = cfma(mu, S0, f32x2{sr.y, si.y}), lo, hi;
    { float a, b; swap16(T.x, a, b); lo.x = a; hi.x = b; swap16(T.y, a, b); lo.y = a; hi.y = b; }
    f32x2 U = cfma(nu, lo, hi), E = (q4 & 1) ? lo : f32x2{0.f, 0.f};
    { float a, b; swap32(U.x, a, b); lo.x = a; hi.x = b; swap32(U.y, a, b); lo.y = a; hi.y = b; }
    f32x2 tot = cfma(nu2, lo, hi);
    __builtin_amdgcn_raw_buffer_store_b128(u32x4{__float_as_uint(tot.x), tlo, __float_as_uint(tot.y), thi}, gr, gst, 0, 16);
#pragma unroll
    for (int k = 0; k < 16; ++k) asm volatile("" : "+v"(Br[k]), "+v"(Bi[k]) :: "memory");
    if (q4 & 2) E = cfma(nuq, lo, E);
    f32x2 La_ = cmul(coef, E), Lb_ = cmul(coef, cfma(mu, E, S0));
    const f32x2 cr2 = {coef.x, coef.x}, ci2 = {coef.y, coef.y};
#pragma unroll
    for (int k = 0; k < 16; ++k) {
      f32x2 n1 = __builtin_elementwise_fma(-ci2, Bi[k], cr2 * Br[k]), n2 = __builtin_elementwise_fma(ci2, Br[k], cr2 * Bi[k]);
      Br[k] = n1; Bi[k] = n2;
    }
#pragma unroll
    for (int k = 0; k < 16; ++k) asm volatile("" : "+v"(Br[k]), "+v"(Bi[k]));
    asm volatile("" : "+v"(La_), "+v"(Lb_));
    const u32 wa = HREG + q4 * 4096 + wv * 1024 + l15 * 64;
    u32 wa4[4];
#pragma unroll
    for (int q = 0; q < 4; ++q) wa4[q] = wa + ((q ^ ((l15 >> 1) & 3)) << 4);
    asm volatile("" : "+v"(wa4[0]), "+v"(wa4[1]), "+v"(wa4[2]), "+v"(wa4[3]));
    u32 ra[2];
#pragma unroll
    for (int u = 0; u < 2; ++u) {
      int row = 8 * (q4 >> 1) + 4 * u + (l15 >> 2);
      ra[u] = HREG + wv * 1024 + row * 64 + (((2 * (q4 & 1) + ((l15 & 3) >> 1)) ^ ((row >> 1) & 3)) << 4) + (l15 & 1) * 8;
    }
#pragma unroll
    for (int s = 0; s < 4; ++s) aoff[s] = l31 * 128 + (((2 * s + hh) ^ ((l31 >> 1) & 7)) << 4);
    c8 = ws;
#pragma unroll
    for (int r = 0; r < 16; ++r) { acc0[r] = 0.f; acc1[r] = 0.f; }
    asm volatile("" : "+v"(ra[0]), "+v"(ra[1]), "+v"(aoff[0]), "+v"(aoff[1]), "+v"(aoff[2]), "+v"(aoff[3]), "+v"(acc0), "+v"(acc1));
    PINS(); PIN(hbase);
    f32x2 H;
    {
      __builtin_amdgcn_s_sleep(HS0);
      GPOLL();
      GWAIT();
      if (GBAD()) {
        __builtin_amdgcn_s_sleep(HS);
#pragma unroll 1
        for (int it = 0; it < SPIN_MAX; ++it) {
          GPOLL(); GWAIT();
          if (!GBAD()) break;
          __builtin_amdgcn_s_sleep(HS);
        }
      }
      f32x2 s3 = {__uint_as_float(g3.x), __uint_as_float(g3.z)}, s2 = {__uint_as_float(g2.x), __uint_as_float(g2.z)};
      f32x2 s1 = {__uint_as_float(g1.x), __uint_as_float(g1.z)}, s0 = {__uint_as_float(g0.x), __uint_as_float(g0.z)};
      H = cmul(Lg, cfma(La4, cfma(La4, cfma(La4, s3, s2), s1), s0));
      float a, b;
      swap16(H.x, a, b); H.x = a + b; swap16(H.y, a, b); H.y = a + b;
      swap32(H.x, a, b); H.x = a + b; swap32(H.y, a, b); H.y = a + b;
    }
    f32x2 sa = cfma(cm0, H, La_), sb_ = cfma(cm1, H, Lb_);
    sr = f32x2{sa.x, sb_.x}; si = f32x2{sa.y, sb_.y};
#define HPK(g, hf) u32x4{HP1(g, 8 * (hf)), HP1(g, 8 * (hf) + 2), HP1(g, 8 * (hf) + 4), HP1(g, 8 * (hf) + 6)}
#define HP1(g, k) (((g) >> 1) ? pack2(-Bi[k][(g) & 1], -Bi[(k) + 1][(g) & 1]) : pack2(Br[k][(g) & 1], Br[(k) + 1][(g) & 1]))
    u32x4 hv[8];
#pragma unroll
    for (int k = 0; k < 16; ++k) {
      f32x2 t1 = __builtin_elementwise_fma(lr2, sr, Br[k]), t2 = __builtin_elementwise_fma(lr2, si, Bi[k]);
      f32x2 n1 = __builtin_elementwise_fma(-li2, si, t1), n2 = __builtin_elementwise_fma(li2, sr, t2);
      sr = n1; si = n2; Br[k] = sr; Bi[k] = si;
      if (k >= 8 && !(k & 1)) { const int g = (k - 8) >> 1; SB; hv[g] = HPK(g, 0); ldsw(wa4[2 * (g & 1)] + (g >> 1) * 16384, hv[g]); SB; }
    }
#pragma unroll
    for (int g = 0; g < 4; ++g) { hv[4 + g] = HPK(g, 1); asm volatile("" : "+v"(hv[4 + g])); ldsw(wa4[2 * (g & 1) + 1] + (g >> 1) * 16384, hv[4 + g]); SB; }
    asm volatile("" :: "v"(hv[0]), "v"(hv[1]), "v"(hv[2]), "v"(hv[3]), "v"(hv[4]), "v"(hv[5]), "v"(hv[6]), "v"(hv[7]));
#pragma unroll
    for (int q = 0; q < 8; ++q) {
      u32x2 l2 = __builtin_bit_cast(u32x2, __builtin_amdgcn_ds_read_tr16_b64_v4i16((LAS s16x4*)(size_t)(ra[0] + (q & 3) * 4096 + (q >> 2) * 16384)));
      u32x2 h2 = __builtin_bit_cast(u32x2, __builtin_amdgcn_ds_read_tr16_b64_v4i16((LAS s16x4*)(size_t)(ra[1] + (q & 3) * 4096 + (q >> 2) * 16384)));
      hv[q] = u32x4{l2.x, l2.y, h2.x, h2.y}; SB;
    }
#pragma unroll
    for (int q = 0; q < 8; ++q) {
      hv[q].x = (int)hv[q].x > (int)(MARK + 1) ? hv[q].x : MARK + 1;
      __builtin_amdgcn_raw_buffer_store_b128(hv[q], hr, ho, hbase + ((q & 3) * 72 + (q >> 2) * 4) * 1024, 16); SB;
    }
    bar();
    __builtin_amdgcn_s_sleep(ASLP);
  }
  {
#define BARI 19
#define DLOAD(i) __builtin_amdgcn_raw_buffer_load_b128(hr, (((i) & 4) ? ho4 : ho) + ((i) & 3) * 1024, ht[(i) >> 3], LAUX)
    ht[0] = hc; ht[1] = hn;
    ht[2] = hn + 16384; ht[2] = ht[2] >= he ? ht[2] - 9 * 8192 : ht[2];
    ht[3] = ht[2] + 16384; ht[3] = ht[3] >= he ? ht[3] - 9 * 8192 : ht[3];
#pragma unroll
    for (int i = 0; i < DEEP; ++i) { hd[i] = DLOAD(i); SB; }
#pragma unroll
    for (int q = 0; q < CD; ++q) { ca[q] = aoff[q] + cs; cf[q][0] = ldsr<bf16x8>(CADDR(q, 0)); cf[q][1] = ldsr<bf16x8>(CADDR(q, 1)); }
    ca[3] = aoff[3] + cs;
    SB;
    u32 xa_ = (grp ? HREG + 16384 : 2 * 16384) + w * 4096 + lane * 16, xb_ = (grp ? 2 * 16384 : HREG + 16384) + w * 4096 + lane * 16;
    u32 ov = ((128 * c + 32 * w + 4 * hh) * 1152 + 576 + 64 * j + 32 * grp + l31) * 4;
    asm volatile("" : "+v"(ov), "+v"(xa_), "+v"(xb_));
#define FRESH(i) do { if (__builtin_expect(__any(hd[i].x == MARK), 0)) { \
        _Pragma("unroll 1") for (int it = 0; it < SPIN_MAX; ++it) { __builtin_amdgcn_s_sleep(SLP); \
          _Pragma("unroll") for (int r = (i); r <= ((i) | 7); ++r) hd[r] = DLOAD(r); \
          __builtin_amdgcn_s_waitcnt(0x0F70); \
          if (!__any(hd[i].x == MARK)) break; } } } while (0)
    FRESH(0);
#pragma unroll
    for (int i = 0; i < 32; ++i) {
      const int k = i & 7, n = i >> 3, kn2 = k + CD; const bool NX = n < 3;
      bf16x8 a = __builtin_bit_cast(bf16x8, hd[i]);
      acc0 = MM(a, cf[k & 3][0], acc0);
      asm volatile("" : "+v"(acc0)); SB;
      if (kn2 >= 8 && NX) ca[kn2 & 3] = aoff[kn2 & 3] + cs;
      if (k == 0 && n > 0) ca[3] = aoff[3] + cs;
      if (kn2 < 8 || NX) cf[kn2 & 3][0] = ldsr<bf16x8>(CADDR(kn2, 0));
      if (i + DEEP < 32) hd[i + DEEP] = DLOAD(i + DEEP);
      SB;
      acc1 = MM(a, cf[k & 3][1], acc1);
      asm volatile("" : "+v"(acc1)); SB;
      if (kn2 < 8 || NX) cf[kn2 & 3][1] = ldsr<bf16x8>(CADDR(kn2, 1));
      if (NX && k == 1) { cs = n == 2 ? cl : (cs + 32768) & 131071; PIN(cs); }
      if (i == BARI) asm volatile("s_barrier" ::: "memory");
      SB;
      if (i < 31) FRESH(i + 1);
      SB;
    }
    __amdgpu_buffer_rsrc_t orr = __builtin_amdgcn_make_buffer_rsrc(kptr(K, 18), 0, 2048 * 1152 * 4, 0x00020000);
#pragma unroll
    for (int q = 0; q < 4; ++q) ldsw(xa_ + q * 1024, f32x4{acc1[4 * q], acc1[4 * q + 1], acc1[4 * q + 2], acc1[4 * q + 3]});
    bar();
    if (c == 15 && j == 0 && threadIdx.x == 256)
      __hip_atomic_store((GAS u64*)(ws + WS_EPOCH), ((u64)((u32)(ep >> 32) + 1u) << 32) | (u32)((u32)ep + 1u), __ATOMIC_RELAXED, __HIP_MEMORY_SCOPE_AGENT);
#pragma unroll
    for (int q = 0; q < 4; ++q) {
      f32x4 o = ldsr<f32x4>(xb_ + q * 1024);
#pragma unroll
      for (int e = 0; e < 4; ++e) acc1[4 * q + e] = acc0[4 * q + e] + o[e];
    }
    asm volatile("" : "+v"(acc1));
#pragma unroll
    for (int r = 0; r < 16; ++r) __builtin_amdgcn_raw_buffer_store_b32(__float_as_uint(acc1[r]), orr, ov, (8 * (r >> 2) + (r & 3)) * 4608, OAUX);
  }
}

struct C2 { f32x2 r, i; };
DEV C2 cm2(const C2& a, const C2& b) {
  C2 o; o.r = __builtin_elementwise_fma(-a.i, b.i, a.r * b.r); o.i = __builtin_elementwise_fma(a.i, b.r, a.r * b.i); return o;
}
DEV C2 cexp2(f32x2 re, f32x2 im) { f32x2 a = cexp(re.x, im.x), b = cexp(re.y, im.y); C2 o; o.r = f32x2{a.x, b.x}; o.i = f32x2{a.y, b.y}; return o; }
DEV u32x2 pk4(const C2& a) { return u32x2{pack2(a.r.x, a.r.y), pack2(a.i.x, a.i.y)}; }
DEV f32x2 up2(u32 v) { return f32x2{__uint_as_float(v << 16), __uint_as_float(v & 0xffff0000u)}; }
DEV f32x4 mf16(u32x4 a, u32x4 b, f32x4 c) { return __builtin_amdgcn_mfma_f32_16x16x32_bf16(__builtin_bit_cast(bf16x8, a), __builtin_bit_cast(bf16x8, b), c, 0, 0, 0); }
DEV u32x4 bandop(int x, int lo, int hi, u32 mul) {
  u32 m = (u32)((((1ull << (hi - lo + 1)) - 1) << 32) >> (32 + hi - x)) & 0xffu;
  u32 sp = m | (m << 15);
  u32x4 o;
#pragma unroll
  for (int i = 0; i < 4; ++i) o[i] = ((sp >> (2 * i)) & 0x10001u) * mul;
  return o;
}
DEV u32x4 rd2(u32 a0, u32 a1) {
  u32x2 x = *(volatile const LAS u32x2*)(size_t)a0, y = *(volatile const LAS u32x2*)(size_t)a1; return u32x4{x.x, x.y, y.x, y.y};
}
constexpr u32 A_RAW = 0, A_UT = 32768, UT_STR = 4640, A_KB = A_UT + 8 * UT_STR, KB_STR = 8704, KP = 544, CPS = 2112;

DEV void a_role(KA K, int d0) {
  const int lane = threadIdx.x & 63, wv = __builtin_amdgcn_readfirstlane(threadIdx.x >> 6);
  const int d = d0 + wv, j = lane & 15, q = lane >> 4;
  u32 pAr, pAi, pBr, pBi, pCr, pCi, pdt, pds;
  u32 vo = d * 256 + lane * 4, vd = d * 2, uo = (512 * wv + lane) * 1152 + d0 * 2, ul = A_RAW + wv * 8192;
  asm volatile("" : "+v"(vo), "+v"(vd), "+v"(uo));
  asm volatile("" : "+s"(ul));
  KWAIT(K);
  asm volatile("global_load_dword %0, %8, %10\n\tglobal_load_dword %1, %8, %11\n\tglobal_load_ushort %6, %9, %16\n\tglobal_load_dword %2, %8, %12\n\t"
               "global_load_dword %3, %8, %13\n\tglobal_load_dword %4, %8, %14\n\tglobal_load_dword %5, %8, %15\n\tglobal_load_ushort %7, %9, %17"
               : "=&v"(pAr), "=&v"(pAi), "=&v"(pBr), "=&v"(pBi), "=&v"(pCr), "=&v"(pCi), "=&v"(pdt), "=&v"(pds)
               : "v"(vo), "v"(vd), "s"(kraw(K, 1)), "s"(kraw(K, 2)), "s"(kraw(K, 3)), "s"(kraw(K, 4)), "s"(kraw(K, 5)), "s"(kraw(K, 6)), "s"(kraw(K, 7)), "s"(kraw(K, 8)) : "memory");
  const char* ug = kptr(K, 0);
  char* const outp = kptr(K, 18);
#define UGET(i) glds1(ug + (i) * (64 * 1152), ul + (i) * 1024, uo)
  asm volatile("s_barrier" ::: "memory");
#define U0 4
  if (wv < 4) { _Pragma("unroll") for (int i = 0; i < U0; ++i) UGET(i); }
  u32x4 bnd[6];
  bnd[0] = bandop(j + 16 - 8 * q, 3, 5, 0x3f80); bnd[1] = bandop(j + 16 - 8 * q, 6, 10, 0x3f80); bnd[2] = bandop(j + 16 - 8 * q, 11, 13, 0x3f80);
  bnd[3] = bandop(j + 24 - 8 * q, 14, 18, 0x3f80); bnd[4] = bandop(j + 16 - 8 * q, 5, 11, 0x3f80); bnd[5] = bandop(j + 24 - 8 * q, 12, 20, 0x3f80);
  u32x4 afx = bandop(j + 16 - 8 * q, 3, 4, 1);
  const u32 ut = A_UT + wv * UT_STR, kb = A_KB + wv * KB_STR;
  ldsw(ut + lane * 8, u32x2{0u, 0u});
#pragma unroll
  for (int i = 0; i < 6; ++i) asm volatile("" : "+v"(bnd[i]));
  asm volatile("" : "+v"(afx));
  if (wv < 4) asm volatile("s_waitcnt vmcnt(%0)" :: "n"(U0) : "memory"); else asm volatile("s_waitcnt vmcnt(0)" ::: "memory");
  asm volatile("" : "+v"(pAr), "+v"(pAi), "+v"(pBr), "+v"(pBi), "+v"(pCr), "+v"(pCi), "+v"(pdt), "+v"(pds) :: "memory");
  const float dsk = __uint_as_float(pds << 16);
  const float dt = rbf(__builtin_amdgcn_exp2f(__uint_as_float(pdt << 16) * 1.44269504088896341f));
  const f32x2 dt2 = {dt, dt};
  const f32x2 are = dt2 * up2(pAr), aim = dt2 * up2(pAi);
  C2 W; { C2 B = {dt2 * up2(pBr), dt2 * up2(pBi)}, Cc = {up2(pCr), up2(pCi)}; W = cm2(Cc, B); W.i = -W.i; }
  const u32 wrow = kb + lane * 8, rfr = kb + j * KP + q * 16;
  u32x4 Yf[8];
  C2 E8 = cexp2(8.f * are, 8.f * aim);
  {
    C2 Y[16];
    Y[1] = E8;
    ldsw(wrow, u32x2{0x3f803f80u, 0u});
    ldsw(wrow + KP, pk4(Y[1]));
#pragma unroll
    for (int i = 2; i < 16; ++i) { Y[i] = (i & 1) ? cm2(Y[i - 1], Y[1]) : cm2(Y[i / 2], Y[i / 2]); ldsw(wrow + i * KP, pk4(Y[i])); }
#pragma unroll
    for (int s = 0; s < 8; ++s) Yf[s] = ldsr<u32x4>(rfr + 64 * s);
  }
#define U1 6
  if (wv < 4) { _Pragma("unroll") for (int i = U0; i < U1; ++i) UGET(i); }
  E8.i = -E8.i;
  C2 E[8];
  E[1] = cexp2(are, -aim);
  const C2 G = cexp2(128.f * are, -128.f * aim);
  E[2] = cm2(E[1], E[1]); E[3] = cm2(E[2], E[1]); E[4] = cm2(E[2], E[2]); E[5] = cm2(E[4], E[1]); E[6] = cm2(E[4], E[2]); E[7] = cm2(E[4], E[3]);
  f32x4 T0 = {0.f, 0.f, 0.f, 0.f}, T1 = T0, T2 = T0;
  C2 Pc = W;
#pragma unroll
  for (int h = 0; h < 2; ++h) {
    ldsw(wrow + (8 * h) * KP, pk4(Pc));
#pragma unroll
    for (int r = 1; r < 8; ++r) ldsw(wrow + (8 * h + r) * KP, pk4(cm2(Pc, E[r])));
    Pc = cm2(Pc, G);
  }
#pragma unroll
  for (int s = 0; s < 8; ++s) T0 = mf16(ldsr<u32x4>(rfr + 64 * s), Yf[s], T0);
#define U2 8
  if (wv < 4) { _Pragma("unroll") for (int i = U1; i < U2; ++i) UGET(i); }
  u32x2 keep[4];
#pragma unroll
  for (int h = 0; h < 2; ++h) {
    keep[2 * h] = pk4(cm2(Pc, E[4])); keep[2 * h + 1] = pk4(cm2(Pc, E8));
    ldsw(wrow + (8 * h) * KP, pk4(Pc)); ldsw(wrow + (8 * h + 1) * KP, pk4(cm2(Pc, E[2])));
    ldsw(wrow + (8 * h + 4) * KP, keep[2 * h]); ldsw(wrow + (8 * h + 5) * KP, pk4(cm2(Pc, E[6])));
    Pc = cm2(Pc, G);
  }
#pragma unroll
  for (int g = 0; g < 4; ++g) {
    ldsw(wrow + (4 * g + 2) * KP, pk4(Pc)); ldsw(wrow + (4 * g + 3) * KP, pk4(cm2(Pc, E[4])));
    Pc = cm2(Pc, G);
  }
#pragma unroll
  for (int s = 0; s < 8; ++s) T1 = mf16(ldsr<u32x4>(rfr + 64 * s), Yf[s], T1);
#pragma unroll
  for (int g = 0; g < 8; ++g) { ldsw(wrow + (4 * (g >> 1) + (g & 1)) * KP, pk4(Pc)); Pc = cm2(Pc, G); }
  ldsw(wrow + 2 * KP, pk4(Pc));
#pragma unroll
  for (int g = 0; g < 4; ++g) ldsw(wrow + (4 * g + 3) * KP, keep[g]);
#pragma unroll
  for (int s = 0; s < 8; ++s) T2 = mf16(ldsr<u32x4>(rfr + 64 * s), Yf[s], T2);
  if (wv < 4) { _Pragma("unroll") for (int i = U2; i < 8; ++i) UGET(i); }
  const u32 c3 = kb + 3 * CPS;
  ldsw(c3 + lane * 16, u32x4{0u, 0u, 0u, 0u}); ldsw(c3 + 1024 + lane * 16, u32x4{0u, 0u, 0u, 0u});
  {
    const int l0 = 128 * (q >> 1) + 4 * (q & 1) + 8 * j;
    ldsw(c3 + (544 - l0) * 2, u32x2{pack2(T0[3], T0[2]), pack2(T0[1], lane == 0 ? T0[0] + dsk : T0[0])});
    ldsw(c3 + (288 - l0) * 2, u32x2{pack2(T2[3], T1[1]), pack2(T1[0], T1[0])});
    if (lane == 63) ldsw(c3 + 64, u32x2{pack2(0.f, T2[3]), pack2(T2[3], T2[3])});
    const int mu0 = 32 + 8 * q + (j >> 1), od = j & 1;
    u32 p2 = pack2(lane == 0 ? T2[0] : T1[2], T1[3]);
    u32 p3 = pack2(lane == 0 ? T2[2] : T2[0], T2[1]);
    const int ia = lane == 0 ? 647 + 204 - 63 : (od ? 647 + 68 - mu0 : 647 + 204 + 1 - mu0);
    ldsw(c3 + ia * 2, (u16)p2);
    ldsw(c3 + (647 + 136 * od - mu0) * 2, (u16)(p2 >> 16));
    const int i3 = (od ? 967 - 64 : 967 + 84 - 63) - 16 * q - (j >> 1);
    ldsw(c3 + (lane == 0 ? 967 + 84 - 127 : i3) * 2, (u16)p3);
    ldsw(c3 + (i3 - 8) * 2, (u16)(p3 >> 16));
    const u32 k1024 = __builtin_amdgcn_readfirstlane(p2) & 0xffffu;
#pragma unroll
    for (int i = 0; i < 4; ++i) afx[i] = __umul24(afx[i], k1024);
    u32x4 x[2]; u32x2 y[2];
#pragma unroll
    for (int h = 0; h < 2; ++h) { x[h] = ldsr<u32x4>(c3 + h * 1024 + lane * 16); y[h] = ldsr<u32x2>(c3 + h * 1024 + lane * 16 + 16); }
#pragma unroll
    for (int h = 0; h < 2; ++h) {
      u32 a0 = __builtin_amdgcn_alignbit(x[h].y, x[h].x, 16), a1 = __builtin_amdgcn_alignbit(x[h].z, x[h].y, 16), a2 = __builtin_amdgcn_alignbit(x[h].w, x[h].z, 16),
          a3 = __builtin_amdgcn_alignbit(y[h].x, x[h].w, 16), a4 = __builtin_amdgcn_alignbit(y[h].y, y[h].x, 16);
      ldsw(kb + 2 * CPS + h * 1024 + lane * 16, u32x4{a0, a1, a2, a3});
      ldsw(kb + 1 * CPS + h * 1024 + lane * 16, u32x4{x[h].y, x[h].z, x[h].w, y[h].x});
      ldsw(kb + h * 1024 + lane * 16, u32x4{a1, a2, a3, a4});
    }
  }
  u32x4 Af[17], t2[3][2], t3[4];
  {
    const u32 fb = kb + (j & 3) * CPS - 8 * (j >> 2);
    const u32 fa = fb + 16 * q + (544 - 16 - 512) * 2;
#pragma unroll
    for (int n = 0; n < 17; ++n) Af[n] = rd2(fa + (16 - n) * 64, fa + (16 - n) * 64 + 8);
    const u32 ft = fb + 8 * q;
#pragma unroll
    for (int D = 2; D < 5; ++D) { u32 a = ft + (644 - 16 * D) * 2; t2[D - 2][0] = rd2(a, a + 136); t2[D - 2][1] = rd2(a + 272, a + 408); }
#pragma unroll
    for (int D = 4; D < 8; ++D) { u32 a = ft + (964 - 16 * D) * 2; t3[D - 4] = rd2(a, a + 168); }
  }
  if (wv < 4) {
    asm volatile("s_waitcnt vmcnt(0)" ::: "memory");
    const u32 ra = ul + (8 * q + 4 * ((j & 3) >> 1) + (j >> 2)) * 16 + (j & 1) * 8;
    const u32 wa = A_UT + (j & 7) * UT_STR + (256 + 512 * wv + 8 * q + 4 * (j >> 3)) * 2;
    s16x4 v[16];
#pragma unroll
    for (int i = 0; i < 16; ++i) v[i] = __builtin_amdgcn_ds_read_tr16_b64_v4i16((LAS s16x4*)(size_t)(ra + i * 512));
    SB;
#pragma unroll
    for (int i = 0; i < 16; ++i) ldsw(wa + i * 64, v[i]);
  }
  bar();
  f32x4 acc[8];
#pragma unroll
  for (int i = 0; i < 8; ++i) acc[i] = f32x4{0.f, 0.f, 0.f, 0.f};
  const f32x4 zz = {0.f, 0.f, 0.f, 0.f};
  const u32 ub = ut + (16 * j + 8 * q) * 2;
  const u32 yb = ut + (16 * j + 4 * q) * 2;
#define BADDR(k) (ub + (256 + 256 * ((k) >> 3) - 16 - 32 * ((k) & 7)) * 2)
  constexpr int NB = 4;
  u32x4 Bf[NB], W1;
#pragma unroll
  for (int k = 0; k < NB - 1; ++k) Bf[k] = ldsr<u32x4>(BADDR(k));
  W1 = ldsr<u32x4>(ub + (256 - 24) * 2);
  f32x4 V[6]; u32x4 vp[3];
  __amdgpu_buffer_rsrc_t orr = __builtin_amdgcn_make_buffer_rsrc(outp, 0, 2048 * 1152 * 4, 0x00020000);
  const u32 ora = A_UT + (4 * (q & 1) + (j >> 2)) * UT_STR + (32 * wv + 16 * (q >> 1) + 4 * (j & 3)) * 2;
  const u32 ov = ((32 * wv + 16 * (q >> 1) + j) * 1152 + d0 + 4 * (q & 1)) * 4;
  u32x2 ovv;
#define YSTAGE(t) ldsw(yb + (256 * (t)) * 2, u32x2{pack2(acc[t][0], acc[t][1]), pack2(acc[t][2], acc[t][3])})
#define YREAD(t) ovv = __builtin_bit_cast(u32x2, __builtin_amdgcn_ds_read_tr16_b64_v4i16((LAS s16x4*)(size_t)(ora + (t) * 512)))
#define YSTORE(t) __builtin_amdgcn_raw_buffer_store_b128(u32x4{ovv.x << 16, ovv.x & 0xffff0000u, ovv.y << 16, ovv.y & 0xffff0000u}, orr, ov, (t) * 256 * 4608, OAUXA)
#pragma unroll
  for (int e = 0; e < 8; ++e) {
#pragma unroll
    for (int b = 0; b < 8; ++b) {
      const int k = 8 * e + b;
      if (b == 3 && e > 0) { bar(); YREAD(e - 1); }
      if (k + NB - 1 < 64) Bf[(k + NB - 1) % NB] = ldsr<u32x4>(BADDR(k + NB - 1));
      if (b == 6 && e < 5) W1 = ldsr<u32x4>(ub + (256 + 256 * (e + 1) - 24) * 2);
      const u32x4 B = Bf[k % NB];
      acc[e] = mf16(Af[b], B, acc[e]);
      if (e < 7) acc[e + 1] = mf16(Af[8 + b], B, acc[e + 1]);
      if (b == 0) {
        if (e < 6) { V[0] = mf16(B, bnd[0], zz); V[1] = mf16(B, bnd[1], zz); V[2] = mf16(B, bnd[2], zz); V[3] = mf16(W1, bnd[3], zz); }
        if (e < 4) { V[4] = mf16(B, bnd[4], zz); V[5] = mf16(W1, bnd[5], zz); }
        if (e < 6) acc[e + 2] = mf16(Af[16], B, acc[e + 2]);
        if (e < 4) acc[e + 4] = mf16(afx, B, acc[e + 4]);
        if (e > 0) YSTAGE(e - 1);
      }
      if (b == 1 && e < 6) {
#pragma unroll
        for (int p = 0; p < 3; ++p) if (p < 2 || e < 4)
          vp[p] = u32x4{pack2(V[2 * p][0], V[2 * p][1]), pack2(V[2 * p][2], V[2 * p][3]), pack2(V[2 * p + 1][0], V[2 * p + 1][1]), pack2(V[2 * p + 1][2], V[2 * p + 1][3])};
      }
      if (b >= 2 && b <= 4 && e + b < 8) { acc[e + b] = mf16(vp[0], t2[b - 2][0], acc[e + b]); acc[e + b] = mf16(vp[1], t2[b - 2][1], acc[e + b]); }
      if (b >= 4 && e + b < 8) acc[e + b] = mf16(vp[2], t3[b - 4], acc[e + b]);
      if (b == 4 && e > 0) YSTORE(e - 1);
      __builtin_amdgcn_sched_barrier(0);
    }
  }
  YSTAGE(7);
  bar();
  YREAD(7); YSTORE(7);

}

DEV void pf_role(KA K, int k, int g, int m, int nh, int x) {
  const u64 tb = __builtin_amdgcn_s_memrealtime();
  const int lane = threadIdx.x & 63, wv = __builtin_amdgcn_readfirstlane(threadIdx.x >> 6);
  const int id = k * 8 + wv;
  KWAIT(K);
  char* const ws = kptr(K, 19);
  const char *c0 = kptr(K, 14), *c1 = kptr(K, 15);
  const u64 ep = load_epoch(ws);
#define CPULL() _Pragma("unroll 1") for (int i = m * 8 + wv; i < 18 * CNS; i += 8 * nh) { \
    int pc = i + x * (2 * CNS + 2); pc = pc >= 18 * CNS ? pc - 18 * CNS : pc; \
    int mm = pc >= 9 * CNS, q = pc - 9 * CNS * mm, jj = q / CNS, e = (q - jj * CNS) * 64 + lane, row = e / CNS, sl = e - row * CNS + jj + 9 - CNS; \
    sl = sl >= 9 ? sl - 9 : sl; \
    touch64((mm ? c1 : c0) + jj * (64 * 1152), wv * 256, row * 1152 + sl * 128); \
    asm volatile("s_waitcnt vmcnt(%0)" :: "n"(CPF - 1) : "memory"); }
  for (int it = 0; it < 4000 && __builtin_amdgcn_s_memrealtime() - tb < WDELAY; ++it) __builtin_amdgcn_s_sleep(2);
  __amdgpu_buffer_rsrc_t hr = __builtin_amdgcn_make_buffer_rsrc(ws + WS_H, 0, H_BYTES, 0x00020000);
#pragma unroll 1
  for (int i = id; i < 576; i += 8 * NWG) __builtin_amdgcn_raw_buffer_store_b128(u32x4{MARK, 0u, 0u, 0u}, hr, lane * 16, (g * 576 + i) * 1024, 16);
  asm volatile("s_waitcnt vmcnt(0)" ::: "memory");
  bar();
  if (threadIdx.x == 0) {
    __amdgpu_buffer_rsrc_t fr = __builtin_amdgcn_make_buffer_rsrc(ws + WS_FLAG, 0, 64 * 128, 0x00020000);
    __builtin_amdgcn_raw_buffer_store_b128(u32x4{0u, mix32((u32)ep), 0u, mix32((u32)(ep >> 32) ^ 0x5bd1e995u)}, fr, lane * 16, (g * 8 + k) * 128, 16);
  }
  CPULL()
  asm volatile("s_waitcnt vmcnt(0)" ::: "memory");
}

__global__ __launch_bounds__(512, 2) void fused(Params P) {
  const int b = blockIdx.x, xcd = (XPERM >> (4 * (b & 7))) & 7, slot = b >> 3;
  KA K;
  asm volatile("s_load_dwordx16 %1, %3, 0x40\n\ts_load_dwordx8 %2, %3, 0x80\n\ts_load_dwordx16 %0, %3, 0x0"
               : "=&s"(K.a), "=&s"(K.b), "=&s"(K.c) : "s"(__builtin_amdgcn_kernarg_segment_ptr()) : "memory");
  u64 pc = 0;
  asm volatile("s_getpc_b64 %0" : "=s"(pc));
  if (__builtin_expect(slot < 18, 1)) {
    g_role(K, 2 * xcd + (slot >= 9), slot >= 9 ? slot - 9 : slot, pc);
  } else {
    const int hv = (xcd - H0) & 7, ea = hv < NH ? 8 / NH : 0, m = slot - 26 - ea;
    if (m < 0) a_role(K, slot < 26 ? 64 * xcd + 8 * (slot - 18) : 512 + 8 * (hv * (8 / NH) + slot - 26));
    else {
      const int li = hv - NH, per = 8 / NH - 1;
      pf_role(K, m < 5 ? m : 5 - per + li % per, m < 5 ? xcd : (H0 + li / per) & 7, m, 6 - ea, xcd);
    }
  }
}

extern "C" void kernel_launch(void* const* d_in, const int* in_sizes, int n_in, void* d_out, int out_size, void* d_ws, size_t ws_size, hipStream_t stream) {
  Params P;
  for (int i = 0; i < 18; ++i) P.in[i] = (const u16*)d_in[i];
  P.out = (float*)d_out; P.ws = (char*)d_ws;
  (void)hipFuncSetAttribute((const void*)fused, hipFuncAttributeMaxDynamicSharedMemorySize, LDS_BYTES);
  fused<<<256, 512, LDS_BYTES, stream>>>(P);
}
```
